```python
import torch

def _softmax(x, axis=-1):
    return torch.softmax(x, dim=axis)
def _gelu(x):
    return torch.nn.functional.gelu(x, approximate="tanh")
def _silu(x):
    return torch.nn.functional.silu(x)
def _sigmoid(x):
    return torch.sigmoid(x)
def _layernorm(x, axis=-1, eps=1e-5):
    m = x.mean(dim=axis, keepdim=True)
    v = x.var(dim=axis, keepdim=True, unbiased=False)
    return (x - m) / torch.sqrt(v + eps)
def _scatter_add(tgt, idx, src, axis=0):
    return tgt.clone().index_add_(axis, idx, src)
def _segment_sum(src, seg, n):
    out = torch.zeros((n,) + src.shape[1:], dtype=src.dtype)
    return out.index_add_(0, seg, src)
def _pad(x, pw):
    flat = tuple(v for pair in reversed(pw) for v in pair)
    return torch.nn.functional.pad(x, flat)

kernel_name = 'concat_heads__gqa_block_cached__compressed_kv_cla__decode'

def setup_inputs():

    _w = (lambda a: (lambda t: t.to(torch.bfloat16) if t.is_floating_point() else t)(torch.as_tensor(a)))
    def _A_setup():
        B, Sq, D = 4, 1, 576
        Hq, Hkv, Sk, Dh = 32, 16, 4096, 128
        assert Hq % Hkv == 0, (Hq, Hkv)
        return {
            "x": _w((lambda *s: torch.randn(*s, dtype=torch.bfloat16) * 1.6)(B, Sq, D)),
            "k_cache": _w((lambda *s: torch.randn(*s, dtype=torch.bfloat16) * 1.6)(B, Hkv, Sk, Dh)),
            "v_cache": _w((lambda *s: torch.randn(*s, dtype=torch.bfloat16) * 0.05)(B, Hkv, Sk, Dh)),
            "wq": _w((lambda *s: torch.randn(*s, dtype=torch.bfloat16) * 1.6)(D, Hq * Dh) * (D ** -0.5)),
            "wo": _w((lambda *s: torch.randn(*s, dtype=torch.bfloat16) * 0.05)(Hq * Dh, D) * ((Hq * Dh) ** -0.5)),
        }
    _A_in = _A_setup()

    def _B_setup():
        B, H, Sq, Sk, D, M = 4, 32, 1, 4096, 128, 576
        Dc = 4 * D
        return {
            "x": _w((lambda *s: torch.randn(*s, dtype=torch.bfloat16) * 3.2)(B, Sq, M)),
            "c_kv": _w((lambda *s: torch.randn(*s, dtype=torch.bfloat16) * 0.1)(B, Sk, Dc)),
            "w_q": _w((lambda *s: torch.randn(*s, dtype=torch.bfloat16) * 6.4)(M, H, D) * (M ** -0.5)),
            "w_uk": _w((lambda *s: torch.randn(*s, dtype=torch.bfloat16) * 6.4)(Dc, H, D) * (Dc ** -0.5)),
            "w_uv": _w((lambda *s: torch.randn(*s, dtype=torch.bfloat16) * 0.1)(Dc, H, D) * (Dc ** -0.5)),
            "w_o": _w((lambda *s: torch.randn(*s, dtype=torch.bfloat16) * 0.1)(H, D, M) * ((H * D) ** -0.5)),
        }
    _B_in = _B_setup()

    return {
        "A_x": _A_in["x"],
        "A_k_cache": _A_in["k_cache"],
        "A_v_cache": _A_in["v_cache"],
        "A_wq": _A_in["wq"],
        "A_wo": _A_in["wo"],
        "B_x": _B_in["x"],
        "B_c_kv": _B_in["c_kv"],
        "B_w_q": _B_in["w_q"],
        "B_w_uk": _B_in["w_uk"],
        "B_w_uv": _B_in["w_uv"],
        "B_w_o": _B_in["w_o"],
    }

def reference(A_x, A_k_cache, A_v_cache, A_wq, A_wo, B_x, B_c_kv, B_w_q, B_w_uk, B_w_uv, B_w_o):
    def _A_ref(x, k_cache, v_cache, wq, wo):
        B, Sq, D = x.shape
        Hq, Dh = 32, 128
        Hkv = k_cache.shape[1]
        rep = Hq // Hkv
        q = (lambda x, *d: x.permute(*d))((x @ wq).reshape(B, Sq, Hq, Dh), 0, 2, 1, 3)
        k = (lambda x, n, axis: torch.repeat_interleave(x, n, dim=axis))(k_cache, rep, axis=1)
        v = (lambda x, n, axis: torch.repeat_interleave(x, n, dim=axis))(v_cache, rep, axis=1)
        scores = torch.einsum("bhqd,bhkd->bhqk", q, k) * (Dh ** -0.5)
        p = _softmax(scores, axis=-1)
        o = torch.einsum("bhqk,bhkd->bhqd", p, v)
        return (lambda x, *d: x.permute(*d))(o, 0, 2, 1, 3).reshape(B, Sq, Hq * Dh) @ wo

    def _B_ref(x, c_kv, w_q, w_uk, w_uv, w_o):
        B, Sq, M = x.shape[0], x.shape[1], x.shape[2]
        Sk = c_kv.shape[1]
        H, D = w_q.shape[1], w_q.shape[2]
        q = torch.einsum("bqm,mhd->bhqd", x, w_q)
        k = torch.einsum("bkc,chd->bhkd", c_kv, w_uk)
        v = torch.einsum("bkc,chd->bhkd", c_kv, w_uv)
        scores = torch.einsum("bhqd,bhkd->bhqk", q, k) * (D ** -0.5)
        off = Sk - Sq
        qi = torch.arange(Sq)[:, None]
        kj = torch.arange(Sk)[None, :]
        cmask = kj <= (qi + off)
        scores = torch.where(cmask[None, None], scores, -1e9)
        p = _softmax(scores, axis=-1)
        ctx = torch.einsum("bhqk,bhkd->bhqd", p, v)
        return torch.einsum("bhqd,hdm->bqm", ctx, w_o)

    a = _A_ref(A_x, A_k_cache, A_v_cache, A_wq, A_wo)
    b = _B_ref(B_x, B_c_kv, B_w_q, B_w_uk, B_w_uv, B_w_o)
    return torch.concatenate([a, b], axis=-1)
```

```cpp
#include <hip/hip_runtime.h>
#include <hip/hip_bf16.h>
#include <stdint.h>
#define EMU 0
typedef unsigned short u16;
__device__ __forceinline__ float bf2f(u16 v) { return __uint_as_float(((unsigned)v) << 16); }
__device__ __forceinline__ u16 f2bf(float f) { unsigned u = __float_as_uint(f); u += 0x7FFFu + ((u >> 16) & 1u); return (u16)(u >> 16); }
__device__ __forceinline__ float rbf(float f) { return bf2f(f2bf(f)); }
__device__ __forceinline__ float emu(float f) { return EMU ? rbf(f) : f; }

__global__ void k_q(const u16* x, const u16* w, float* q) {
  int n = blockIdx.x * blockDim.x + threadIdx.x;
  float acc[4] = {0, 0, 0, 0};
  for (int m = 0; m < 576; ++m) {
    float wv = bf2f(w[(size_t)m * 4096 + n]);
    for (int b = 0; b < 4; ++b) acc[b] += bf2f(x[b * 576 + m]) * wv;
  }
  for (int b = 0; b < 4; ++b) q[b * 4096 + n] = emu(acc[b]);
}
__global__ void k_qabs(const float* q, const u16* wuk, float* qabs) {
  int c = blockIdx.x * blockDim.x + threadIdx.x;
  int h = blockIdx.y;
  float acc[4] = {0, 0, 0, 0};
  for (int d = 0; d < 128; ++d) {
    float wv = bf2f(wuk[((size_t)c * 32 + h) * 128 + d]);
    for (int b = 0; b < 4; ++b) acc[b] += q[b * 4096 + h * 128 + d] * wv;
  }
  for (int b = 0; b < 4; ++b) qabs[(b * 32 + h) * 512 + c] = acc[b];
}
__global__ void k_sA(const float* q, const u16* K, float* s) {
  int k = blockIdx.x * blockDim.x + threadIdx.x;
  int h = blockIdx.y, b = blockIdx.z;
  const u16* kr = K + (((size_t)b * 16 + h / 2) * 4096 + k) * 128;
  float acc = 0;
  for (int d = 0; d < 128; ++d) acc += q[b * 4096 + h * 128 + d] * bf2f(kr[d]);
  s[((size_t)b * 32 + h) * 4096 + k] = emu(emu(acc) * 0.08838834764831845f);
}
__global__ void k_sB(const float* qabs, const u16* C, float* s) {
  int k = blockIdx.x * blockDim.x + threadIdx.x;
  int h = blockIdx.y, b = blockIdx.z;
  const u16* kr = C + ((size_t)b * 4096 + k) * 512;
  float acc = 0;
  for (int d = 0; d < 512; ++d) acc += qabs[(b * 32 + h) * 512 + d] * bf2f(kr[d]);
  s[((size_t)b * 32 + h) * 4096 + k] = emu(emu(acc) * 0.08838834764831845f);
}
__global__ void k_softmax(float* s) {
  __shared__ float red[256];
  float* r = s + (size_t)blockIdx.x * 4096;
  int t = threadIdx.x;
  float m = -INFINITY;
  for (int i = t; i < 4096; i += 256) m = fmaxf(m, r[i]);
  red[t] = m; __syncthreads();
  for (int o = 128; o > 0; o >>= 1) { if (t < o) red[t] = fmaxf(red[t], red[t + o]); __syncthreads(); }
  m = red[0]; __syncthreads();
  float l = 0;
  for (int i = t; i < 4096; i += 256) { float e = __expf(r[i] - m); r[i] = e; l += e; }
  red[t] = l; __syncthreads();
  for (int o = 128; o > 0; o >>= 1) { if (t < o) red[t] += red[t + o]; __syncthreads(); }
  l = red[0];
  for (int i = t; i < 4096; i += 256) r[i] = emu(r[i] / l);
}
__global__ void k_pvA(const float* p, const u16* V, float* o) {
  int d = threadIdx.x; int h = blockIdx.x, b = blockIdx.y;
  const u16* v = V + ((size_t)b * 16 + h / 2) * 4096 * 128;
  const float* pr = p + ((size_t)b * 32 + h) * 4096;
  float acc = 0;
  for (int k = 0; k < 4096; ++k) acc += pr[k] * bf2f(v[(size_t)k * 128 + d]);
  o[b * 4096 + h * 128 + d] = emu(acc);
}
__global__ void k_pvB(const float* p, const u16* C, float* cl) {
  int c = threadIdx.x; int h = blockIdx.x, b = blockIdx.y;
  const u16* v = C + (size_t)b * 4096 * 512;
  const float* pr = p + ((size_t)b * 32 + h) * 4096;
  float acc = 0;
  for (int k = 0; k < 4096; ++k) acc += pr[k] * bf2f(v[(size_t)k * 512 + c]);
  cl[(b * 32 + h) * 512 + c] = acc;
}
__global__ void k_uv(const float* cl, const u16* wuv, float* ctx) {
  int d = threadIdx.x; int h = blockIdx.x, b = blockIdx.y;
  float acc = 0;
  for (int c = 0; c < 512; ++c) acc += cl[(b * 32 + h) * 512 + c] * bf2f(wuv[((size_t)c * 32 + h) * 128 + d]);
  ctx[b * 4096 + h * 128 + d] = emu(acc);
}
__global__ void k_out(const float* o, const u16* w, u16* out, int off) {
  int m = blockIdx.x * 64 + threadIdx.x; int b = blockIdx.y;
  float acc = 0;
  for (int k = 0; k < 4096; ++k) acc += o[b * 4096 + k] * bf2f(w[(size_t)k * 576 + m]);
  out[b * 1152 + off + m] = f2bf(acc);
}

extern "C" void kernel_launch(void* const* d_in, const int* in_sizes, int n_in, void* d_out, int out_size, void* d_ws, size_t ws_size, hipStream_t stream) {
  const u16 *Ax = (const u16*)d_in[0], *AK = (const u16*)d_in[1], *AV = (const u16*)d_in[2], *Awq = (const u16*)d_in[3], *Awo = (const u16*)d_in[4];
  const u16 *Bx = (const u16*)d_in[5], *BC = (const u16*)d_in[6], *Bwq = (const u16*)d_in[7], *Bwuk = (const u16*)d_in[8], *Bwuv = (const u16*)d_in[9], *Bwo = (const u16*)d_in[10];
  float* ws = (float*)d_ws;
  float *qA = ws, *qB = ws + 16384, *qabs = ws + 32768, *sA = ws + 32768 + 65536, *sB = sA + 524288, *oA = sB + 524288, *cl = oA + 16384, *ctx = cl + 65536;
  u16* out = (u16*)d_out;
  k_q<<<64, 64, 0, stream>>>(Ax, Awq, qA);
  k_q<<<64, 64, 0, stream>>>(Bx, Bwq, qB);
  k_qabs<<<dim3(8, 32), 64, 0, stream>>>(qB, Bwuk, qabs);
  k_sA<<<dim3(16, 32, 4), 256, 0, stream>>>(qA, AK, sA);
  k_sB<<<dim3(16, 32, 4), 256, 0, stream>>>(qabs, BC, sB);
  k_softmax<<<256, 256, 0, stream>>>(sA);
  k_pvA<<<dim3(32, 4), 128, 0, stream>>>(sA, AV, oA);
  k_pvB<<<dim3(32, 4), 512, 0, stream>>>(sB, BC, cl);
  k_uv<<<dim3(32, 4), 128, 0, stream>>>(cl, Bwuv, ctx);
  k_out<<<dim3(9, 4), 64, 0, stream>>>(oA, Awo, out, 0);
  k_out<<<dim3(9, 4), 64, 0, stream>>>(ctx, Bwo, out, 576);
}
```

```cpp
#include <hip/hip_runtime.h>
#include <stdint.h>

typedef unsigned int u32;
typedef unsigned long long u64;
typedef unsigned short u16;
typedef u32 v4u __attribute__((ext_vector_type(4)));
typedef u32 v2u __attribute__((ext_vector_type(2)));
typedef u32 v8u __attribute__((ext_vector_type(8)));
typedef u32 v16u __attribute__((ext_vector_type(16)));
typedef float f32x2 __attribute__((ext_vector_type(2)));
typedef float f32x4 __attribute__((ext_vector_type(4)));
typedef float f32x16 __attribute__((ext_vector_type(16)));
typedef __bf16 bf16x8 __attribute__((ext_vector_type(8)));
typedef __bf16 bf16x2 __attribute__((ext_vector_type(2)));
typedef short s16x4 __attribute__((ext_vector_type(4)));
#define DI __device__ __forceinline__
#define LAS __attribute__((address_space(3)))
#define GAS __attribute__((address_space(1)))
#define RLX_AGENT __ATOMIC_RELAXED, __HIP_MEMORY_SCOPE_AGENT

#define STAMPS 0
#define CODE_BYTES 27700
#define PACE 8

constexpr int WS_EPOCH = 0;
constexpr int WS_CNT = 1024;
constexpr int WS_FQ = 8192;
constexpr int WS_FP = 12288;
constexpr int WS_ML = 16384;
constexpr int WS_E3M = 32768;
constexpr int WS_QABS = 65536;
constexpr int WS_E1 = 262144;
constexpr int WS_E2 = 524288;
constexpr int WS_E3 = 1 << 20;
constexpr int WS_PART = 2 << 20;
constexpr int WS_E4 = 4 << 20;
constexpr int WS_E6 = 7 << 20;
constexpr int WS_BYTES = 9 << 20;
constexpr int LDS_BYTES = 163840;
constexpr int L_QBW = 0, L_QAW = 36864, L_WO = 73728;
constexpr int L_XV = 110592;
constexpr int L_QS = 139264;
constexpr int L_FL = L_QS + 1024;
constexpr int L_FM = L_FL + 8192;
constexpr int L_OA = L_FM + 128;
constexpr int L_X = 131072;
constexpr int L_SX = L_X, L_P = L_X + 16384, L_WM = L_X + 24576, L_WL = L_X + 25600;
constexpr int L_SYNC = L_X + 26624;
constexpr int L_W2 = 65536;
constexpr int L_QB = L_OA + 256;
constexpr int L_CL = L_QB + 1280;
constexpr int L_CT = L_CL + 4352;
static_assert(L_CT + 768 <= L_WM, "LDS map");

constexpr float SCALE = 0.08838834764831845f;
constexpr float LOG2E = 1.4426950408889634f;
constexpr u32 SPIN_CAP = 1u << 18;

struct Params {
  const char *Ax, *AK, *AV, *Awq, *Awo, *Bx, *BC, *Bwq, *Bwuk, *Bwuv, *Bwo;
  u16* out;
  char* ws;
};

DI f32x4 mfma16(v4u a, v4u b, f32x4 c) {
  return __builtin_amdgcn_mfma_f32_16x16x32_bf16(__builtin_bit_cast(bf16x8, a), __builtin_bit_cast(bf16x8, b), c, 0, 0, 0);
}
DI f32x16 mfma32(v4u a, v4u b, f32x16 c) {
  return __builtin_amdgcn_mfma_f32_32x32x16_bf16(__builtin_bit_cast(bf16x8, a), __builtin_bit_cast(bf16x8, b), c, 0, 0, 0);
}
DI u32 pk2(float a, float b) { f32x2 v = {a, b}; return __builtin_bit_cast(u32, __builtin_convertvector(v, bf16x2)); }
DI float rbf(float a) { return (float)(__bf16)a; }
DI float asf(u32 x) { return __builtin_bit_cast(float, x); }
DI u32 asu(float x) { return __builtin_bit_cast(u32, x); }
DI float ex2(float x) { return __builtin_amdgcn_exp2f(x); }
template <int CTRL> DI float dppf(float x) { return asf(__builtin_amdgcn_mov_dpp(asu(x), CTRL, 0xf, 0xf, true)); }
template <int CTRL> DI u32 dppu(u32 x) { return __builtin_amdgcn_mov_dpp(x, CTRL, 0xf, 0xf, true); }
constexpr int XOR1 = 0xB1, XOR2 = 0x4E, XOR7 = 0x141, ROR8 = 0x128, ROR4 = 0x124;
DI float x16max(float x) { auto s = __builtin_amdgcn_permlane16_swap(asu(x), asu(x), false, false); return fmaxf(asf(s[0]), asf(s[1])); }
DI float x32max(float x) { auto s = __builtin_amdgcn_permlane32_swap(asu(x), asu(x), false, false); return fmaxf(asf(s[0]), asf(s[1])); }
DI float x16sum(float x) { auto s = __builtin_amdgcn_permlane16_swap(asu(x), asu(x), false, false); return asf(s[0]) + asf(s[1]); }
DI float x32sum(float x) { auto s = __builtin_amdgcn_permlane32_swap(asu(x), asu(x), false, false); return asf(s[0]) + asf(s[1]); }

#define BAR() asm volatile("s_waitcnt lgkmcnt(0)\n\ts_barrier" ::: "memory")
#define VMWAIT(n) asm volatile("s_waitcnt vmcnt(%0)" ::"n"(n) : "memory")
#define SB() __builtin_amdgcn_sched_barrier(0)

DI v4u lld128(LAS char* L, u32 o) { return *(LAS v4u*)(L + o); }
DI void lst128(LAS char* L, u32 o, v4u v) { *(LAS v4u*)(L + o) = v; }
DI v2u ltr(LAS char* L, u32 o) { return __builtin_bit_cast(v2u, __builtin_amdgcn_ds_read_tr16_b64_v4i16((LAS s16x4*)(L + o))); }
DI v4u ltr2(LAS char* L, u32 o0, u32 o1) { v2u a = ltr(L, o0), b = ltr(L, o1); return v4u{a.x, a.y, b.x, b.y}; }
DI void dma16(const char* sbase, u32 voff, u32 lds) {
  u32 keep;
  asm volatile("s_mov_b32 %0, m0\n\ts_mov_b32 m0, %3\n\ts_nop 2\n\tglobal_load_lds_dwordx4 %1, %2 nt\n\ts_mov_b32 m0, %0"
               : "=&s"(keep) : "v"(voff), "s"(sbase), "s"(lds) : "memory");
}
DI v4u ldnt(const char* base, u32 voff, int imm) { return __builtin_nontemporal_load((const GAS v4u*)((const GAS char*)base + voff + imm)); }
DI v4u ldg(const char* base, u32 voff) { return *(const GAS v4u*)((const GAS char*)base + voff); }
typedef __amdgpu_buffer_rsrc_t rsrc_t;
DI v4u ld_sc1(rsrc_t rs, u32 o) { return __builtin_amdgcn_raw_buffer_load_b128(rs, o, 0, 16); }
DI v2u ld_sc1_8(rsrc_t rs, u32 o) { return __builtin_amdgcn_raw_buffer_load_b64(rs, o, 0, 16); }
DI void st_sc1(rsrc_t rs, u32 o, v4u v) { __builtin_amdgcn_raw_buffer_store_b128(v, rs, o, 0, 16); }
DI void st_sc1_8(rsrc_t rs, u32 o, v2u v) { __builtin_amdgcn_raw_buffer_store_b64(v, rs, o, 0, 16); }
DI void st_gran(rsrc_t rs, u32 o, float a, float b, u32 tA, u32 tB) { st_sc1(rs, o, v4u{asu(a), tA, asu(b), tB}); }
template <int N> DI void poll_gran(rsrc_t rs, const u32 (&o)[N], v4u (&v)[N], u32 tA, u32 tB) {
  for (u32 spins = 0;; ++spins) {
    bool ok = true;
#pragma unroll
    for (int i = 0; i < N; ++i) v[i] = ld_sc1(rs, o[i]);
#pragma unroll
    for (int i = 0; i < N; ++i) ok = ok && v[i].y == tA && v[i].w == tB;
    if (__all(ok) || spins > SPIN_CAP) break;
    __builtin_amdgcn_s_sleep(1);
  }
}
DI void poll_flags(rsrc_t rs, u32 o, u32 tA, u32 tB) {
  for (u32 spins = 0;; ++spins) {
    const v2u v = ld_sc1_8(rs, o);
    if (__all(v.x == tA && v.y == tB) || spins > SPIN_CAP) break;
    __builtin_amdgcn_s_sleep(2);
  }
  __builtin_amdgcn_fence(__ATOMIC_ACQUIRE, "wavefront");
}

DI u32 lds_inc(LAS char* L, u32 o) {
  asm volatile("s_waitcnt lgkmcnt(0)" ::: "memory");
  u32 old = 0;
  if ((threadIdx.x & 63) == 0) old = __hip_atomic_fetch_add((LAS u32*)(L + o), 1u, __ATOMIC_RELAXED, __HIP_MEMORY_SCOPE_WORKGROUP);
  return __builtin_amdgcn_readfirstlane(old);
}
DI void lds_wait(LAS char* L, u32 o, u32 want) {
  for (u32 spins = 0; spins < (1u << 24); ++spins) {
    if (__builtin_amdgcn_readfirstlane(__hip_atomic_load((LAS u32*)(L + o), __ATOMIC_RELAXED, __HIP_MEMORY_SCOPE_WORKGROUP)) >= want) break;
    __builtin_amdgcn_s_sleep(1);
  }
  asm volatile("" ::: "memory");
}

#define STAMP(i) do {} while (0)

DI f32x4 gemv288(LAS char* L, u32 base, u32 xo) {
  f32x4 d = {0.f, 0.f, 0.f, 0.f};
#pragma unroll
  for (int ks = 0; ks < 9; ++ks) d = mfma16(lld128(L, xo + ks * 64), ltr2(L, base + ks * 4096, base + ks * 4096 + 512), d);
  return d;
}

struct Acc { float m, l; f32x4 o; };
DI float a_qk(const v4u (&K)[8], const v4u (&QF)[8], bool t1) {
  f32x4 d = {0.f, 0.f, 0.f, 0.f};
#pragma unroll
  for (int j = 0; j < 8; ++j) d = mfma16(K[j], QF[j], d);
  const float keep = t1 ? d[3] : d[0], send = t1 ? d[1] : d[2];
  return keep + dppf<XOR1>(send);
}
DI u32 a_soft(float S, Acc& a) {
  const float t = rbf(rbf(S) * SCALE) * LOG2E;
  float cm = fmaxf(t, dppf<XOR1>(t));
  cm = fmaxf(cm, dppf<ROR4>(cm));
  cm = fmaxf(cm, dppf<ROR8>(cm));
  cm = x32max(x16max(cm));
  const float mn = fmaxf(a.m, cm);
  const float al = ex2(a.m - mn), p = ex2(t - mn);
  a.l = a.l * al + p;
  a.m = mn;
  a.o *= al;
  return pk2(p, dppf<XOR2>(p));
}
template <int J> DI void a_pv1(u32 PP, v4u V, const u32 (&sel)[4], Acc& a) {
  const u32 X = dppu<0x150 + 4 * (J >> 1) + (J & 1)>(PP);
  const v4u B = {__builtin_amdgcn_perm(X, X, sel[0]), __builtin_amdgcn_perm(X, X, sel[1]), __builtin_amdgcn_perm(X, X, sel[2]), __builtin_amdgcn_perm(X, X, sel[3])};
  a.o = mfma16(V, B, a.o);
}
DI void a_pv(u32 PP, const v4u (&V)[8], const u32 (&sel)[4], Acc& a) {
  a_pv1<0>(PP, V[0], sel, a); a_pv1<1>(PP, V[1], sel, a); a_pv1<2>(PP, V[2], sel, a); a_pv1<3>(PP, V[3], sel, a);
  a_pv1<4>(PP, V[4], sel, a); a_pv1<5>(PP, V[5], sel, a); a_pv1<6>(PP, V[6], sel, a); a_pv1<7>(PP, V[7], sel, a);
}
DI void a_loadq(LAS char* L, int pair, int lane, v4u (&QF)[8]) {
  const int n = lane & 15, g = lane >> 4;
  const u32 o = L_QS + pair * 512 + ((n >> 1) & 1) * 256 + (2 * g + (n & 1)) * 16;
  const v4u F0 = lld128(L, o), F1 = lld128(L, o + 128), Z = {0u, 0u, 0u, 0u};
#pragma unroll
  for (int u = 0; u < 4; ++u) { const bool on = (n >> 2) == u; QF[2 * u] = on ? F0 : Z; QF[2 * u + 1] = on ? F1 : Z; }
}
DI void a_flush(LAS char* L, int w, int pair, int lane, const Acc& a) {
  const int n = lane & 15, G = lane >> 4, h = (n >> 1) & 1, jp = ((n >> 2) << 1) | (n & 1);
  float l = a.l + dppf<XOR1>(a.l);
  l += dppf<ROR4>(l);
  l += dppf<ROR8>(l);
  l = x32sum(x16sum(l));
  LAS float* f = (LAS float*)(L + L_FL) + ((w * 2 + pair) * 2 + h) * 128 + 32 * G + jp;
#pragma unroll
  for (int r = 0; r < 4; ++r) f[8 * r] = a.o[r];
  if ((lane & 61) == 0) *(LAS f32x2*)(L + L_FM + ((w * 2 + pair) * 2 + h) * 8) = f32x2{a.m, l};
}
DI void a_reset(Acc& a) { a.m = -INFINITY; a.l = 0.f; a.o = f32x4{0.f, 0.f, 0.f, 0.f}; }
DI u32 fetch_q(rsrc_t rs, int g, int t, u32 tA, u32 tB) {
  const u32 o0 = WS_E2 + (g * 512 + t) * 16;
  const u32 o[2] = {o0, o0 + 4096};
  v4u v[2];
  poll_gran<2>(rs, o, v, tA, tB);
  return pk2(asf(v[0].x) + asf(v[1].x), asf(v[0].z) + asf(v[1].z));
}
DI void put_q(LAS char* L, int t, u32 pk) {
  *(LAS u16*)(L + L_QS + t * 2) = (u16)pk;
  *(LAS u16*)(L + L_QS + 512 + t * 2) = (u16)(pk >> 16);
}
DI u32 sw2(u32 row) { return (row & 2) | ((row >> 1) & 4); }
DI u32 fsw(u32 row) { return ((row & 3) << 2) | ((row >> 2) & 3); }
DI u32 voff1152(int n, int lane) { const u32 e = n * 64 + lane, row = e / 72, pos = e - row * 72; return row * 1152 + (pos ^ sw2(row)) * 16; }
template <int NL> DI void reduce_col(rsrc_t rs, u32 box, int col, int lane, u32 tA, u32 tB, u16* out_) {
  GAS u16* out = (GAS u16*)out_;
  u32 o[NL];
  v4u v[NL];
#pragma unroll
  for (int i = 0; i < NL; ++i) o[i] = box + (((col >> 2) * (32 * NL) + 32 * i + (lane >> 1)) * 8 + (col & 3) * 2 + (lane & 1)) * 16;
  poll_gran<NL>(rs, o, v, tA, tB);
  float a = 0.f, b = 0.f;
#pragma unroll
  for (int i = 0; i < NL; ++i) { a += asf(v[i].x); b += asf(v[i].z); }
  a += dppf<XOR2>(a); b += dppf<XOR2>(b);
  a += dppf<ROR4>(a); b += dppf<ROR4>(b);
  a += dppf<ROR8>(a); b += dppf<ROR8>(b);
  a = x32sum(x16sum(a)); b = x32sum(x16sum(b));
  const u32 pk = pk2(a, b);
  if (lane < 2) { out[(2 * lane) * 1152 + col] = (u16)pk; out[(2 * lane + 1) * 1152 + col] = (u16)(pk >> 16); }
}

template <int NL> DI void reduce_quad(rsrc_t rs, u32 box, int quad, int lane, u32 tA, u32 tB, u16* out_) {
  GAS u16* out = (GAS u16*)out_;
  u32 o[NL];
  v4u v[NL];
#pragma unroll
  for (int i = 0; i < NL; ++i) o[i] = box + (quad * NL + i) * 1024 + lane * 16;
  poll_gran<NL>(rs, o, v, tA, tB);
  float a = 0.f, b = 0.f;
#pragma unroll
  for (int i = 0; i < NL; ++i) { a += asf(v[i].x); b += asf(v[i].z); }
  a += dppf<ROR8>(a); b += dppf<ROR8>(b);
  a = x32sum(x16sum(a)); b = x32sum(x16sum(b));
  const u32 pk = pk2(a, b);
  if (lane < 8) { GAS u16* o2 = out + (2 * (lane & 1)) * 1152 + quad * 4 + (lane >> 1); o2[0] = (u16)pk; o2[1152] = (u16)(pk >> 16); }
}

template <bool FIRST>
DI void ba_pass(LAS char* L, int w, int lane, const v4u (&qf)[16], f32x16 (&acc)[2], float& m, float& l) {
  const int kb = w & 3, ch = w >> 2, hh = lane >> 5, r31 = lane & 31;
  f32x16 S;
#pragma unroll
  for (int i = 0; i < 16; ++i) S[i] = 0.f;
  const u32 rowb = (32 * kb + r31) * 1024 + ch * 512, f = fsw(r31);
#pragma unroll
  for (int ks = 0; ks < 16; ++ks) S = mfma32(lld128(L, rowb + (ks >> 3) * 256 + (((2 * (ks & 7) + hh) ^ f) * 16)), qf[ks], S);
  float s[8];
  {
    v4u a, b;
#pragma unroll
    for (int j = 0; j < 4; ++j) { a[j] = asu(ch ? S[j] : S[8 + j]); b[j] = asu(ch ? S[4 + j] : S[12 + j]); }
    lst128(L, L_SX + w * 2048 + lane * 16, a);
    lst128(L, L_SX + w * 2048 + 1024 + lane * 16, b);
    BAR();
    a = lld128(L, L_SX + (w ^ 4) * 2048 + lane * 16);
    b = lld128(L, L_SX + (w ^ 4) * 2048 + 1024 + lane * 16);
#pragma unroll
    for (int j = 0; j < 4; ++j) {
      s[j] = ((ch ? S[8 + j] : S[j]) + asf(a[j])) * (SCALE * LOG2E);
      s[4 + j] = ((ch ? S[12 + j] : S[4 + j]) + asf(b[j])) * (SCALE * LOG2E);
    }
  }
  float cm = fmaxf(fmaxf(fmaxf(s[0], s[1]), fmaxf(s[2], s[3])), fmaxf(fmaxf(s[4], s[5]), fmaxf(s[6], s[7])));
  cm = x32max(cm);
  if (lane < 32) *(LAS float*)(L + L_WM + (w * 32 + lane) * 4) = cm;
  BAR();
  float mn = FIRST ? -INFINITY : m;
#pragma unroll
  for (int i = 0; i < 8; ++i) mn = fmaxf(mn, *(LAS float*)(L + L_WM + (i * 32 + r31) * 4));
  float ps = 0.f;
#pragma unroll
  for (int j = 0; j < 8; ++j) { s[j] = ex2(s[j] - mn); ps += s[j]; }
  lst128(L, L_P + (2 * kb + ch) * 1024 + lane * 16, v4u{pk2(s[0], s[1]), pk2(s[2], s[3]), pk2(s[4], s[5]), pk2(s[6], s[7])});
  if (FIRST) {
    l = ps;
#pragma unroll
    for (int i = 0; i < 16; ++i) { acc[0][i] = 0.f; acc[1][i] = 0.f; }
  } else {
    const float al = ex2(m - mn);
    l = l * al + ps;
    acc[0] *= al;
    acc[1] *= al;
  }
  m = mn;
  BAR();
  const int q = (lane & 15) >> 2, pp = lane & 3, blk = (lane >> 4) & 1;
  u32 ta[2][2];
#pragma unroll
  for (int T = 0; T < 2; ++T)
#pragma unroll
    for (int t = 0; t < 2; ++t) {
      const u32 c = 8 * w + 4 * T + 2 * blk + (pp >> 1);
      ta[T][t] = (8 * t + 4 * hh + q) * 1024 + ((c ^ ((q << 2) | (2 * t + hh))) * 16) + 8 * (pp & 1);
    }
#pragma unroll
  for (int ks = 0; ks < 8; ++ks) {
    const v4u P = lld128(L, L_P + ks * 1024 + lane * 16);
#pragma unroll
    for (int T = 0; T < 2; ++T) acc[T] = mfma32(ltr2(L, ta[T][0] + ks * 16384, ta[T][1] + ks * 16384), P, acc[T]);
  }
}

__global__ void __launch_bounds__(512, 2) decode_mega(Params args) {
  extern __shared__ __attribute__((aligned(16))) char smem[];
  LAS char* L = (LAS char*)smem;
  v16u ka;
  v8u kb;
  v2u kc;
  asm volatile("s_load_dwordx16 %0, %3, 0x0\n\ts_load_dwordx8 %1, %3, 0x40\n\ts_load_dwordx2 %2, %3, 0x60"
               : "=&s"(ka), "=&s"(kb), "=&s"(kc) : "s"(__builtin_amdgcn_kernarg_segment_ptr()) : "memory");
  const int tid = threadIdx.x, lane = tid & 63, w = __builtin_amdgcn_readfirstlane(tid >> 6);
  const int wg = blockIdx.x, g = wg >> 3, s = wg & 7, r = (s + g) & 7;
  const int kvh = g >> 1;
  u32 pf = 0;
  if (w == 7) {
    u64 pc;
    asm volatile("s_getpc_b64 %0" : "=s"(pc));
    if ((lane * 32 + g) * 128 < CODE_BYTES) pf = *(const GAS u32*)((const GAS char*)(pc & ~127ull) + (lane * 32 + g) * 128);
  }
  const int n16 = lane & 15, g4 = lane >> 4;
  const int ai = (g & 1) * 4 + r;
  const int bi = g * 2 + (r - 4);
  const int h = (g >> 3) * 8 + s, half = r - 6, bw = h * 2 + half;
  const u32 vw128 = (lane >> 3) * 8192 + (((lane & 7) ^ sw2((lane >> 3) | ((w & 1) << 3))) * 16);
  const u32 cbo = (bi * 256 + w) * 1024;
  const bool isA = r < 4, isBA = r == 4 || r == 5, hlp = isA && w >= 4;
  u32 so0 = hlp ? 0u : isBA ? cbo : isA ? (u32)(((r >> 1) * 288 + 8 * w) * 8192 + g * 256 + (r & 1) * 128) : (u32)((half * 256 + 4 * w) * 8192 + h * 256);
  u32 so1 = isA ? (u32)(((ai >> 2) * 288 + 8 * w) * 8192 + kvh * 512 + (ai & 3) * 128) : cbo + 8 * 1024;
  u32 step = hlp ? 512 : isBA ? 16 * 1024 : 32 * 8192, lstep = hlp ? 512 : isA ? 4096 : isBA ? 16384 : 8192;
  u32 vo = hlp ? lane * 16 : isA ? vw128 : isBA ? (lane ^ fsw(w)) * 16 : g4 * 8192 + ((n16 ^ g4 ^ (4 * (w & 3))) * 16);
  u32 ld = hlp ? L_XV + (w & 1) * 4608 : w * 1024;
  int n0 = hlp ? (w < 6 ? 8 : 0) : isA ? 9 : 8;
  asm volatile("s_waitcnt lgkmcnt(0)" : "+s"(ka), "+s"(kb), "+s"(kc) : "s"(so0), "s"(so1), "s"(step), "s"(lstep), "v"(vo), "s"(ld), "s"(n0) : "memory");
  Params p;
#define PTR(v, i) ((char*)(((u64)v[(i) + 1] << 32) | v[(i)]))
  p.Ax = PTR(ka, 0); p.AK = PTR(ka, 2); p.AV = PTR(ka, 4); p.Awq = PTR(ka, 6); p.Awo = PTR(ka, 8); p.Bx = PTR(ka, 10); p.BC = PTR(ka, 12); p.Bwq = PTR(ka, 14);
  p.Bwuk = PTR(kb, 0); p.Bwuv = PTR(kb, 2); p.Bwo = PTR(kb, 4); p.out = (u16*)PTR(kb, 6); p.ws = PTR(kc, 0);
  u64 epoch;
  asm volatile("s_load_dwordx2 %0, %1, 0x0" : "=s"(epoch) : "s"(p.ws) : "memory");
  {
    const char* src = (hlp ? ((w & 1) ? p.Ax : p.Bx) : isA ? p.Bwq : isBA ? p.BC : p.Bwuk) + so0;
    int n = n0;
    if (w == 0) STAMP(20);
#pragma unroll 1
    for (int bt = 0; bt < 2; ++bt) {
#pragma unroll 1
      for (; n > 0; --n) { VMWAIT(8); dma16(src, vo, ld); src += step; ld += lstep; }
      src = (isA ? p.Awq : p.BC) + so1;
      ld = isA ? L_QAW + w * 1024 : (8 + w) * 1024;
      vo ^= isA ? 0 : 32;
      n = hlp ? 0 : isA ? 9 : isBA ? 8 : 0;
    }
    if (w == 0) STAMP(21);
    asm volatile("s_waitcnt lgkmcnt(0)" : "+s"(epoch)::"memory");
  }
  const char* cb = p.BC + cbo;
  const rsrc_t rs = __builtin_amdgcn_make_buffer_rsrc((void*)p.ws, 0, WS_BYTES, 0x00020000);
  const u32 trrow = 8 * g4 + (n16 >> 2), trcol = ((2 * (w & 3) + ((lane & 3) >> 1)) ^ sw2(trrow)) * 16 + 8 * (lane & 1);
  const u32 tr128 = trrow * 128 + trcol, tr1152 = trrow * 1152 + trcol;
  if (w >= 4) __builtin_amdgcn_s_setprio(3);
  const u32 tA = (u32)epoch + 1u, tB = (u32)(epoch >> 32) + 1u;
  const u32 cnt_off = lane == 0 ? WS_CNT + g * 128 : 0x7ffffff0u;

  const u32 voffK = (n16 >> 1) * 256 + (2 * g4 + (n16 & 1)) * 16, voffV = g4 * 512 + n16 * 16;
  const size_t kv0 = ((size_t)(g & 1) * 32 + kvh) * (4096 * 256) + 4096;
#define CHUNK_OFF(id) (kv0 + (size_t)((id) >> 7) * (16 * 4096 * 256) + (size_t)((id) & 127) * 8192)
#define KIMM(j) (((j) >> 1) * 2048 + ((j) & 1) * 128 - 4096)
#define VIMM(j) (((j) >> 1) * 2048 + ((j) & 1) * 256 - 4096)
#define LOAD_KV(id) do { const char* k1_ = p.AK + CHUNK_OFF(id); const char* v1_ = p.AV + CHUNK_OFF(id); \
    _Pragma("unroll") for (int j = 0; j < 8; ++j) K[j] = ldnt(k1_, voffK, KIMM(j)); \
    SB(); \
    _Pragma("unroll") for (int j = 0; j < 8; ++j) V[j] = ldnt(v1_, voffV, VIMM(j)); \
    SB(); } while (0)
  u32 sel[4];
  {
    const u32 h2 = (n16 >> 1) & 1, by = (2 * h2) | ((2 * h2 + 1) << 8);
    const u32 on = (n16 & 1) ? ((by << 16) | 0x0c0cu) : (0x0c0c0000u | by);
#pragma unroll
    for (int i = 0; i < 4; ++i) sel[i] = (n16 >> 2) == i ? on : 0x0c0c0c0cu;
  }
  const bool t1 = lane & 1;

  v4u K[8], V[8], QF[8];
  Acc acc;
  u32 idv = 0;
  int cur = 0;
  bool have = false;
  int pause_in = -1;

  if (r < 4) {
    if (w < 4) {
      BAR();
      v4u K0[8], V0[8];
      const int id0 = r * 4 + w;
      const char* k0 = p.AK + CHUNK_OFF(id0); const char* v0 = p.AV + CHUNK_OFF(id0);
      const char* k1 = p.AK + CHUNK_OFF(id0 + 16); const char* v1 = p.AV + CHUNK_OFF(id0 + 16);
#pragma unroll
      for (int j = 0; j < 8; ++j) { VMWAIT(8); K0[j] = ldnt(k0, voffK, KIMM(j)); SB(); }
      VMWAIT(8);
      BAR();
#pragma unroll
      for (int j = 0; j < 8; ++j) { VMWAIT(PACE); V0[j] = ldnt(v0, voffV, VIMM(j)); SB(); }
#pragma unroll
      for (int j = 0; j < 8; ++j) { VMWAIT(PACE); K[j] = ldnt(k1, voffK, KIMM(j)); SB(); }
#pragma unroll
      for (int j = 0; j < 8; ++j) { VMWAIT(PACE); V[j] = ldnt(v1, voffV, VIMM(j)); SB(); }
      a_reset(acc);
      a_flush(L, w, 1, lane, acc);
      if (w == 0) STAMP(1);
      BAR();
      if (w == 0) STAMP(2);
      a_loadq(L, 0, lane, QF);
      const u32 PP = a_soft(a_qk(K0, QF, t1), acc);
      a_pv(PP, V0, sel, acc);
      idv = id0 + 32;
      have = true;
    } else {
      const int ct = w - 4;
      if (r == 0 && w == 4 && lane == 0) __hip_atomic_store((GAS u32*)(p.ws + WS_CNT + g * 128), 64u, RLX_AGENT);
      const u32 xo = L_XV + (lane & 3) * 1152 + g4 * 16;
      VMWAIT(0);
      if (w == 4) STAMP(23);
      BAR();
      if (w == 4) STAMP(3);
      f32x4 d = gemv288(L, L_QBW + tr128, xo + (r >> 1) * 576);
      if (lane < 32) st_gran(rs, WS_E1 + (((g * 2 + (r >> 1)) * 128 + 64 * (r & 1) + 16 * ct + n16) * 2 + g4) * 16, g4 ? d[2] : d[0], g4 ? d[3] : d[1], tA, tB);
      if (w == 4) STAMP(4);
      BAR();
      d = gemv288(L, L_QAW + tr128, xo + 4608 + (ai >> 2) * 576);
      if (lane < 32) st_gran(rs, WS_E2 + (((kvh * 2 + g4) * 2 + (ai >> 2)) * 256 + 64 * (ai & 3) + 16 * ct + n16) * 16, g4 ? d[2] : d[0], g4 ? d[3] : d[1], tA, tB);
      if (w == 4) STAMP(5);
      put_q(L, tid - 256, fetch_q(rs, g, tid - 256, tA, tB));
      if (w == 4) STAMP(6);
      BAR();
      const char* so = p.Awo + (size_t)(kvh * 256 + ai * 32) * 1152;
#pragma unroll 1
      for (int i = 0; i < 9; ++i) dma16(so, voff1152(4 * i + ct, lane), L_WO + (4 * i + ct) * 1024);
      VMWAIT(0);
    }
  } else if (r < 6) {
    const int b = bi >> 4, sp = bi & 15;
    const u32 vc0 = (lane ^ fsw(w)) * 16, vc1 = vc0 ^ 32;
    if (w == 4) poll_flags(rs, WS_FQ + lane * 64, tA, tB);
    if (w == 4) STAMP(7);
    BAR();
    v4u qs[4], c2[8];
#pragma unroll
    for (int i = 0; i < 4; ++i) qs[i] = ld_sc1(rs, WS_QABS + (b * 32 + 4 * w + i) * 1024 + (lane ^ fsw(4 * w + i)) * 16);
    SB();
    asm volatile("s_barrier" ::: "memory");
#pragma unroll
    for (int i = 0; i < 4; ++i) c2[i] = ldnt(cb + (128 + 16 * i) * 1024, vc0, 0);
    SB();
#pragma unroll
    for (int i = 0; i < 4; ++i) lst128(L, L_X + (4 * w + i) * 1024 + lane * 16, qs[i]);
    BAR();
    v4u qf[16];
    {
      const u32 rowb = L_X + (lane & 31) * 1024 + (w >> 2) * 512, f = fsw(lane & 31);
#pragma unroll
      for (int ks = 0; ks < 16; ++ks) qf[ks] = lld128(L, rowb + (ks >> 3) * 256 + (((2 * (ks & 7) + (lane >> 5)) ^ f) * 16));
    }
    BAR();
#pragma unroll
    for (int i = 4; i < 8; ++i) c2[i] = ldnt(cb + (128 + 16 * i) * 1024, vc0, 0);
    SB();
    if (tid == 0) *(LAS u32*)(L + L_SYNC) = 0u;
    if (w == 0) STAMP(8);
    f32x16 ac[2];
    float m, l;
    ba_pass<true>(L, w, lane, qf, ac, m, l);
    BAR();
    if (w == 0) STAMP(9);
    asm volatile("" ::"v"(c2[0]), "v"(c2[1]), "v"(c2[2]), "v"(c2[3]), "v"(c2[4]), "v"(c2[5]), "v"(c2[6]), "v"(c2[7]));
    u32 qpk = 0;
    if (w >= 4) qpk = fetch_q(rs, g, tid - 256, tA, tB);
#pragma unroll 1
    for (int i = 0; i < 8; ++i) dma16(cb + (128 + 16 * i + 8) * 1024, vc1, (16 * i + 8 + w) * 1024);
#pragma unroll
    for (int i = 0; i < 8; ++i) lst128(L, (16 * i + w) * 1024 + lane * 16, c2[i]);
    VMWAIT(0);
    BAR();
    if (w == 0) STAMP(10);
    u32 a0 = 0;
    if (w < 4) a0 = __builtin_amdgcn_raw_ptr_buffer_atomic_add_i32(2, rs, cnt_off, 0, 0);
    ba_pass<false>(L, w, lane, qf, ac, m, l);
    if (w == 0) STAMP(29);
    l = x32sum(l);
    if (lane < 32) *(LAS float*)(L + L_WL + (w * 32 + lane) * 4) = l;
    BAR();
    {
      const u32 hd = lane & 31, hh = lane >> 5;
#pragma unroll
      for (int T = 0; T < 2; ++T)
#pragma unroll
        for (int rq = 0; rq < 4; ++rq)
          *(LAS v2u*)(L + hd * 1024 + (((8 * w + 4 * T + rq) ^ (hd & 15)) * 16) + 8 * hh) =
              v2u{pk2(ac[T][4 * rq], ac[T][4 * rq + 1]), pk2(ac[T][4 * rq + 2], ac[T][4 * rq + 3])};
    }
    if (w >= 4) put_q(L, tid - 256, qpk);
    BAR();
    if (w >= 4) {
#pragma unroll
      for (int i = 0; i < 8; ++i) {
        const u32 hd = 8 * (w - 4) + i;
        st_sc1(rs, WS_PART + ((hd * 4 + b) * 16 + sp) * 1024 + lane * 16, lld128(L, hd * 1024 + ((lane ^ (hd & 15)) * 16)));
      }
      if (w == 4 && lane < 32) {
        float ls = 0.f;
#pragma unroll
        for (int i = 0; i < 8; ++i) ls += *(LAS float*)(L + L_WL + (i * 32 + lane) * 4);
        st_sc1_8(rs, WS_ML + ((lane * 4 + b) * 16 + sp) * 8, v2u{asu(m), asu(ls)});
      }
      VMWAIT(0);
      if (lds_inc(L, L_SYNC) == 3 && lane == 0) st_sc1_8(rs, WS_FP + bi * 64, v2u{tA, tB});
      if (w == 4) STAMP(11);
    } else {
      const u32 id0 = __builtin_amdgcn_readfirstlane(a0);
      idv = id0 + 1;
      a_reset(acc);
      lds_wait(L, L_SYNC, 4);
      if (id0 < 256) {
        LOAD_KV(id0);
        cur = id0 >> 7;
        a_loadq(L, cur, lane, QF);
        have = true;
      }
      a_flush(L, w, 0, lane, acc);
      a_flush(L, w, 1, lane, acc);
    }
  } else {
    if (w >= 4) {
      const int t = tid - 256;
      const u32 o0 = WS_E1 + (h * 512 + t) * 16;
      const u32 o[2] = {o0, o0 + 4096};
      v4u v[2];
      poll_gran<2>(rs, o, v, tA, tB);
      const u32 pk = pk2(asf(v[0].x) + asf(v[1].x), asf(v[0].z) + asf(v[1].z));
      const u32 qo = L_QB + (t & 1) * 640 + (t >> 1) * 2;
      *(LAS u16*)(L + qo) = (u16)pk;
      *(LAS u16*)(L + qo + 320) = (u16)(pk >> 16);
    }
    if (tid < 4) *(LAS u32*)(L + L_SYNC + tid * 4) = 0u;
    VMWAIT(0);
    BAR();
    if (w == 0) STAMP(12);
    {
      f32x4 d0 = {0.f, 0.f, 0.f, 0.f}, d1 = d0;
#pragma unroll
      for (int ks = 0; ks < 4; ++ks) {
        const v4u qb = lld128(L, L_QB + (lane & 3) * 320 + ks * 64 + g4 * 16);
        const u32 a0 = (32 * w + n16) * 256 + (((4 * ks + g4) ^ n16) * 16);
        d0 = mfma16(lld128(L, a0), qb, d0);
        d1 = mfma16(lld128(L, a0 + 4096), qb, d1);
      }
      const bool hi = n16 & 4;
      const v2u o = {pk2(hi ? d1[0] : d0[0], hi ? d1[1] : d0[1]), pk2(hi ? d1[2] : d0[2], hi ? d1[3] : d0[3])};
      if (n16 < 8) st_sc1_8(rs, WS_QABS + ((lane & 3) * 32 + h) * 1024 + (half * 256 + 32 * w + (hi ? 16 : 0) + 4 * g4) * 2, o);
    }
    VMWAIT(0);
    BAR();
    if (tid == 256) st_sc1_8(rs, WS_FQ + bw * 64, v2u{tA, tB});
    if (w == 4) STAMP(13);
    if (w < 4) {
      const char* sv = p.Bwuv + (size_t)(8 * w) * 8192 + h * 256 + half * 128;
#pragma unroll 1
      for (int i = 0; i < 16; ++i) dma16(sv + (size_t)i * 32 * 8192, vw128, (4 * i + w) * 1024);
      const char* so = p.Bwo + (size_t)(h * 128 + half * 64) * 1152;
#pragma unroll 1
      for (int i = 0; i < 18; ++i) dma16(so, voff1152(4 * i + w, lane), L_W2 + (4 * i + w) * 1024);
      const int id0 = 48 + half * 4 + w;
      LOAD_KV(id0);
      a_reset(acc);
      a_flush(L, w, 1, lane, acc);
      VMWAIT(16);
      if (w == 0) STAMP(24);
      BAR();
      a_loadq(L, 0, lane, QF);
      idv = id0 + 8;
      pause_in = 1;
      have = true;
    } else {
      const int hw = w - 4;
      put_q(L, tid - 256, fetch_q(rs, g, tid - 256, tA, tB));
      BAR();
      poll_flags(rs, WS_FP + lane * 64, tA, tB);
      if (w == 4) STAMP(25);
      {
        const v2u ml = ld_sc1_8(rs, WS_ML + ((h * 4 + hw) * 16 + n16) * 8);
        v4u pt[16];
#pragma unroll
        for (int i = 0; i < 16; ++i) pt[i] = ld_sc1(rs, WS_PART + ((h * 4 + hw) * 16 + i) * 1024 + lane * 16);
        float M = fmaxf(asf(ml.x), dppf<XOR1>(asf(ml.x)));
        M = fmaxf(M, dppf<XOR2>(M)); M = fmaxf(M, dppf<ROR4>(M)); M = fmaxf(M, dppf<ROR8>(M));
        const float wt = ex2(asf(ml.x) - M);
        float Ls = wt * asf(ml.y);
        Ls += dppf<XOR1>(Ls); Ls += dppf<XOR2>(Ls); Ls += dppf<XOR7>(Ls); Ls += dppf<0x140>(Ls);
        const float wn = wt / Ls;
        float c[8];
#pragma unroll
        for (int k = 0; k < 8; ++k) c[k] = 0.f;
#pragma unroll
        for (int i = 0; i < 16; ++i) {
          const float wi = asf(__builtin_amdgcn_readlane(asu(wn), i));
#pragma unroll
          for (int k = 0; k < 4; ++k) { c[2 * k] += wi * asf(pt[i][k] << 16); c[2 * k + 1] += wi * asf(pt[i][k] & 0xffff0000u); }
        }
        lst128(L, L_CL + hw * 1088 + lane * 16, v4u{pk2(c[0], c[1]), pk2(c[2], c[3]), pk2(c[4], c[5]), pk2(c[6], c[7])});
      }
      lds_inc(L, L_SYNC);
      lds_wait(L, L_SYNC, 4);
      if (w == 4) STAMP(26);
      {
        f32x4 d = {0.f, 0.f, 0.f, 0.f};
#pragma unroll
        for (int ks = 0; ks < 16; ++ks)
          d = mfma16(lld128(L, L_CL + (lane & 3) * 1088 + ks * 64 + g4 * 16), ltr2(L, tr128 + ks * 4096, tr128 + ks * 4096 + 512), d);
        const float dv = g4 == 0 ? d[0] : g4 == 1 ? d[1] : g4 == 2 ? d[2] : d[3];
        *(LAS u16*)(L + L_CT + g4 * 192 + (16 * hw + n16) * 2) = (u16)pk2(dv, dv);
      }
      lds_inc(L, L_SYNC + 4);
      lds_wait(L, L_SYNC + 4, 4);
      {
        const v4u af0 = lld128(L, L_CT + (lane & 3) * 192 + g4 * 16), af1 = lld128(L, L_CT + (lane & 3) * 192 + 64 + g4 * 16);
#pragma unroll
        for (int i = 0; i < 9; ++i) {
          const u32 t2 = L_W2 + tr1152 + i * 128;
          f32x4 e = mfma16(af0, ltr2(L, t2, t2 + 4608), f32x4{0.f, 0.f, 0.f, 0.f});
          e = mfma16(af1, ltr2(L, t2 + 36864, t2 + 36864 + 4608), e);
          const int T = 4 * i + hw;
          if (lane < 32) st_gran(rs, WS_E6 + (((4 * T + (n16 >> 2)) * 64 + bw) * 8 + (n16 & 3) * 2 + g4) * 16, g4 ? e[2] : e[0], g4 ? e[3] : e[1], tA, tB);
        }
      }
      if (w == 4) STAMP(27);
      if (hw * 64 + bw < 144) reduce_quad<8>(rs, WS_E6, hw * 64 + bw, lane, tA, tB, p.out + 576);
      lds_inc(L, L_SYNC + 8);
      if (w == 4) STAMP(14);
    }
  }

  if (have) {
    float S;
    for (;;) {
      S = a_qk(K, QF, t1);
      SB();
      const u32 id = __builtin_amdgcn_readfirstlane(idv);
      if (id >= 256) break;
      if (pause_in-- == 0) lds_wait(L, L_SYNC + 8, 4);
      idv = __builtin_amdgcn_raw_ptr_buffer_atomic_add_i32(1, rs, cnt_off, 0, 0);
      const char* kn = p.AK + CHUNK_OFF(id); const char* vn = p.AV + CHUNK_OFF(id);
#pragma unroll
      for (int j = 0; j < 8; ++j) K[j] = ldnt(kn, voffK, KIMM(j));
      SB();
      const u32 PP = a_soft(S, acc);
      a_pv(PP, V, sel, acc);
      asm volatile("" : "+v"(acc.o) :: "memory");
      SB();
#pragma unroll
      for (int j = 0; j < 8; ++j) V[j] = ldnt(vn, voffV, VIMM(j));
      SB();
      if ((int)(id >> 7) != cur) {
        a_flush(L, w, 0, lane, acc);
        a_reset(acc);
        cur = 1;
        a_loadq(L, 1, lane, QF);
      }
    }
    const u32 PP = a_soft(S, acc);
    a_pv(PP, V, sel, acc);
    a_flush(L, w, cur, lane, acc);
  }
  BAR();
  if (w == 7) STAMP(16);
  if (w < 4) {
    float mw[4], lw[4];
    f32x2 aw[4];
#pragma unroll
    for (int i = 0; i < 4; ++i) {
      const f32x2 ml = *(LAS f32x2*)(L + L_FM + (i * 4 + w) * 8);
      mw[i] = ml.x; lw[i] = ml.y;
      aw[i] = *(LAS f32x2*)(L + L_FL + ((i * 4 + w) * 128 + 2 * lane) * 4);
    }
    const float M = fmaxf(fmaxf(mw[0], mw[1]), fmaxf(mw[2], mw[3])), Ms = M == -INFINITY ? 0.f : M;
    float Ls = 0.f, o0 = 0.f, o1 = 0.f;
#pragma unroll
    for (int i = 0; i < 4; ++i) { const float wt = ex2(mw[i] - Ms); Ls += wt * lw[i]; o0 += wt * aw[i].x; o1 += wt * aw[i].y; }
    const u32 rec = ((g * 2 + (w >> 1)) * 8 + s) * 2 + (w & 1);
    st_gran(rs, WS_E3 + (rec * 64 + lane) * 16, o0, o1, tA, tB);
    if (lane == 0) st_gran(rs, WS_E3M + rec * 16, M, Ls, tA, tB);
  }
  if (w == 0) STAMP(15);
  if (r < 4) {
    v4u wf[9];
    if (w >= 4) {
      const u32 tb = L_WO + tr1152;
#pragma unroll
      for (int i = 0; i < 9; ++i) wf[i] = ltr2(L, tb + i * 128, tb + i * 128 + 4608);
    }
    {
      const int b = w >> 1, i16 = (tid >> 3) & 15, sl = lane & 7;
      const u32 rec = ((kvh * 4 + b) * 8 + sl) * 2 + (ai >> 2);
      const u32 o[2] = {WS_E3 + (rec * 64 + 16 * (ai & 3) + i16) * 16, WS_E3M + rec * 16};
      v4u v[2];
      poll_gran<2>(rs, o, v, tA, tB);
      const float m = asf(v[1].x);
      float M = fmaxf(m, dppf<XOR1>(m));
      M = fmaxf(M, dppf<XOR2>(M)); M = fmaxf(M, dppf<XOR7>(M));
      const float wt = ex2(m - M);
      float Ls = wt * asf(v[1].z), o0 = wt * asf(v[0].x), o1 = wt * asf(v[0].z);
      Ls += dppf<XOR1>(Ls); o0 += dppf<XOR1>(o0); o1 += dppf<XOR1>(o1);
      Ls += dppf<XOR2>(Ls); o0 += dppf<XOR2>(o0); o1 += dppf<XOR2>(o1);
      Ls += dppf<XOR7>(Ls); o0 += dppf<XOR7>(o0); o1 += dppf<XOR7>(o1);
      const float inv = 1.f / Ls;
      if (sl == 0) *(LAS u32*)(L + L_OA + b * 64 + i16 * 4) = pk2(o0 * inv, o1 * inv);
    }
    BAR();
    if (w == 0) STAMP(17);
    if (w >= 4) {
      const v4u af = lld128(L, L_OA + (lane & 3) * 64 + g4 * 16);
#pragma unroll
      for (int i = 0; i < 9; ++i) {
        const f32x4 e = mfma16(af, wf[i], f32x4{0.f, 0.f, 0.f, 0.f});
        const int T = 4 * i + (w - 4);
        if (lane < 32) st_gran(rs, WS_E4 + (((4 * T + (n16 >> 2)) * 128 + g * 4 + r) * 8 + (n16 & 3) * 2 + g4) * 16, g4 ? e[2] : e[0], g4 ? e[3] : e[1], tA, tB);
      }
      if (w == 4) STAMP(18);
    }
  } else {
    const int ri = r < 6 ? g * 2 + (r - 4) : 64 + g * 2 + (r - 6);
    if (ri < 72) {
      reduce_col<4>(rs, WS_E4, ri * 8 + w, lane, tA, tB, p.out);
      if (ri == 0 && tid == 0) __hip_atomic_store((GAS u64*)(p.ws + WS_EPOCH), ((u64)tB << 32) | tA, RLX_AGENT);
      if (w == 0) STAMP(19);
    }
  }
  asm volatile("" ::"v"(pf));
}

extern "C" void kernel_launch(void* const* d_in, const int* in_sizes, int n_in, void* d_out, int out_size, void* d_ws, size_t ws_size,
                              hipStream_t stream) {
  Params p;
  p.Ax = (const char*)d_in[0]; p.AK = (const char*)d_in[1]; p.AV = (const char*)d_in[2]; p.Awq = (const char*)d_in[3]; p.Awo = (const char*)d_in[4];
  p.Bx = (const char*)d_in[5]; p.BC = (const char*)d_in[6]; p.Bwq = (const char*)d_in[7]; p.Bwuk = (const char*)d_in[8]; p.Bwuv = (const char*)d_in[9];
  p.Bwo = (const char*)d_in[10];
  p.out = (u16*)d_out;
  p.ws = (char*)d_ws;
  (void)hipFuncSetAttribute((const void*)decode_mega, hipFuncAttributeMaxDynamicSharedMemorySize, LDS_BYTES);
  decode_mega<<<256, 512, LDS_BYTES, stream>>>(p);
}
```

```cpp
#include <hip/hip_runtime.h>
#include <stdint.h>

typedef unsigned int u32;
typedef unsigned long long u64;
typedef unsigned short u16;
typedef u32 v4u __attribute__((ext_vector_type(4)));
typedef u32 v2u __attribute__((ext_vector_type(2)));
typedef float f32x2 __attribute__((ext_vector_type(2)));
typedef float f32x4 __attribute__((ext_vector_type(4)));
typedef float f32x16 __attribute__((ext_vector_type(16)));
typedef __bf16 bf16x8 __attribute__((ext_vector_type(8)));
typedef __bf16 bf16x2 __attribute__((ext_vector_type(2)));
typedef short s16x4 __attribute__((ext_vector_type(4)));
#define DI __device__ __forceinline__
#define LAS __attribute__((address_space(3)))
#define GAS __attribute__((address_space(1)))
#define RLX_AGENT __ATOMIC_RELAXED, __HIP_MEMORY_SCOPE_AGENT

#define STAMPS 0
#define PACE 8

constexpr int WS_EPOCH = 0;
constexpr int WS_CNT = 1024;
constexpr int WS_FQ = 8192;
constexpr int WS_FP = 12288;
constexpr int WS_ML = 16384;
constexpr int WS_E3M = 32768;
constexpr int WS_QABS = 65536;
constexpr int WS_E1 = 262144;
constexpr int WS_E2 = 524288;
constexpr int WS_E3 = 1 << 20;
constexpr int WS_PART = 2 << 20;
constexpr int WS_E4 = 4 << 20;
constexpr int WS_E6 = 7 << 20;
constexpr int WS_BYTES = 9 << 20;
constexpr int LDS_BYTES = 163840;
constexpr int L_QBW = 0, L_QAW = 36864, L_WO = 73728;
constexpr int L_QS = 114688;
constexpr int L_FL = L_QS + 1024;
constexpr int L_FM = L_FL + 8192;
constexpr int L_OA = L_FM + 128;
constexpr int L_X = 131072;
constexpr int L_SX = L_X, L_P = L_X + 16384, L_WM = L_X + 24576, L_WL = L_X + 25600;
constexpr int L_W2 = 65536;
constexpr int L_QB = 139264;
constexpr int L_CX = L_QB + 1280;
constexpr int L_CL = L_CX + 8192;
constexpr int L_CP = L_CL + 4352;

constexpr float SCALE = 0.08838834764831845f;
constexpr float LOG2E = 1.4426950408889634f;
constexpr u32 SPIN_CAP = 1u << 18;

struct Params {
  const char *Ax, *AK, *AV, *Awq, *Awo, *Bx, *BC, *Bwq, *Bwuk, *Bwuv, *Bwo;
  u16* out;
  char* ws;
};

DI f32x4 mfma16(v4u a, v4u b, f32x4 c) {
  return __builtin_amdgcn_mfma_f32_16x16x32_bf16(__builtin_bit_cast(bf16x8, a), __builtin_bit_cast(bf16x8, b), c, 0, 0, 0);
}
DI f32x16 mfma32(v4u a, v4u b, f32x16 c) {
  return __builtin_amdgcn_mfma_f32_32x32x16_bf16(__builtin_bit_cast(bf16x8, a), __builtin_bit_cast(bf16x8, b), c, 0, 0, 0);
}
DI u32 pk2(float a, float b) { f32x2 v = {a, b}; return __builtin_bit_cast(u32, __builtin_convertvector(v, bf16x2)); }
DI float rbf(float a) { return (float)(__bf16)a; }
DI float asf(u32 x) { return __builtin_bit_cast(float, x); }
DI u32 asu(float x) { return __builtin_bit_cast(u32, x); }
DI float ex2(float x) { return __builtin_amdgcn_exp2f(x); }
template <int CTRL> DI float dppf(float x) { return asf(__builtin_amdgcn_mov_dpp(asu(x), CTRL, 0xf, 0xf, true)); }
template <int CTRL> DI u32 dppu(u32 x) { return __builtin_amdgcn_mov_dpp(x, CTRL, 0xf, 0xf, true); }
constexpr int XOR1 = 0xB1, XOR2 = 0x4E, XOR7 = 0x141, ROR8 = 0x128, ROR4 = 0x124;
DI float x16max(float x) { auto s = __builtin_amdgcn_permlane16_swap(asu(x), asu(x), false, false); return fmaxf(asf(s[0]), asf(s[1])); }
DI float x32max(float x) { auto s = __builtin_amdgcn_permlane32_swap(asu(x), asu(x), false, false); return fmaxf(asf(s[0]), asf(s[1])); }
DI float x16sum(float x) { auto s = __builtin_amdgcn_permlane16_swap(asu(x), asu(x), false, false); return asf(s[0]) + asf(s[1]); }
DI float x32sum(float x) { auto s = __builtin_amdgcn_permlane32_swap(asu(x), asu(x), false, false); return asf(s[0]) + asf(s[1]); }

#define BAR() asm volatile("s_waitcnt lgkmcnt(0)\n\ts_barrier" ::: "memory")
#define VMWAIT(n) asm volatile("s_waitcnt vmcnt(%0)" ::"n"(n) : "memory")
#define SB() __builtin_amdgcn_sched_barrier(0)

DI v4u lld128(LAS char* L, u32 o) { return *(LAS v4u*)(L + o); }
DI void lst128(LAS char* L, u32 o, v4u v) { *(LAS v4u*)(L + o) = v; }
DI v2u ltr(LAS char* L, u32 o) { return __builtin_bit_cast(v2u, __builtin_amdgcn_ds_read_tr16_b64_v4i16((LAS s16x4*)(L + o))); }
DI v4u ltr2(LAS char* L, u32 o0, u32 o1) { v2u a = ltr(L, o0), b = ltr(L, o1); return v4u{a.x, a.y, b.x, b.y}; }
DI void dma16(const char* sbase, u32 voff, u32 lds) {
  u32 keep;
  asm volatile("s_mov_b32 %0, m0\n\ts_mov_b32 m0, %3\n\ts_nop 2\n\tglobal_load_lds_dwordx4 %1, %2 nt\n\ts_mov_b32 m0, %0"
               : "=&s"(keep) : "v"(voff), "s"(sbase), "s"(lds) : "memory");
}
DI v4u ldnt(const char* base, u32 voff, int imm) { return __builtin_nontemporal_load((const GAS v4u*)((const GAS char*)base + voff + imm)); }
DI v4u ldg(const char* base, u32 voff) { return *(const GAS v4u*)((const GAS char*)base + voff); }
typedef __amdgpu_buffer_rsrc_t rsrc_t;
DI v4u ld_sc1(rsrc_t rs, u32 o) { return __builtin_amdgcn_raw_buffer_load_b128(rs, o, 0, 16); }
DI v2u ld_sc1_8(rsrc_t rs, u32 o) { return __builtin_amdgcn_raw_buffer_load_b64(rs, o, 0, 16); }
DI void st_sc1(rsrc_t rs, u32 o, v4u v) { __builtin_amdgcn_raw_buffer_store_b128(v, rs, o, 0, 16); }
DI void st_sc1_8(rsrc_t rs, u32 o, v2u v) { __builtin_amdgcn_raw_buffer_store_b64(v, rs, o, 0, 16); }
DI void st_gran(rsrc_t rs, u32 o, float a, float b, u32 tA, u32 tB) { st_sc1(rs, o, v4u{asu(a), tA, asu(b), tB}); }
template <int N> DI void poll_gran(rsrc_t rs, const u32 (&o)[N], v4u (&v)[N], u32 tA, u32 tB) {
  for (u32 spins = 0;; ++spins) {
    bool ok = true;
#pragma unroll
    for (int i = 0; i < N; ++i) v[i] = ld_sc1(rs, o[i]);
#pragma unroll
    for (int i = 0; i < N; ++i) ok = ok && v[i].y == tA && v[i].w == tB;
    if (__all(ok) || spins > SPIN_CAP) break;
    __builtin_amdgcn_s_sleep(1);
  }
}
DI void poll_flags(rsrc_t rs, u32 o, u32 tA, u32 tB) {
  for (u32 spins = 0;; ++spins) {
    const v2u v = ld_sc1_8(rs, o);
    if (__all(v.x == tA && v.y == tB) || spins > SPIN_CAP) break;
    __builtin_amdgcn_s_sleep(2);
  }
  __builtin_amdgcn_fence(__ATOMIC_ACQUIRE, "wavefront");
}

#define STAMP(i) do {} while (0)

DI f32x4 gemv288(LAS char* L, u32 base, const v4u (&xf)[9]) {
  f32x4 d = {0.f, 0.f, 0.f, 0.f};
#pragma unroll
  for (int ks = 0; ks < 9; ++ks) d = mfma16(xf[ks], ltr2(L, base + ks * 4096, base + ks * 4096 + 512), d);
  return d;
}

struct Acc { float m, l; f32x4 o; };
DI float a_qk(const v4u (&K)[8], const v4u (&QF)[8], bool t1) {
  f32x4 d = {0.f, 0.f, 0.f, 0.f};
#pragma unroll
  for (int j = 0; j < 8; ++j) d = mfma16(K[j], QF[j], d);
  const float keep = t1 ? d[3] : d[0], send = t1 ? d[1] : d[2];
  return keep + dppf<XOR1>(send);
}
DI u32 a_soft(float S, Acc& a) {
  const float t = rbf(rbf(S) * SCALE) * LOG2E;
  float cm = fmaxf(t, dppf<XOR1>(t));
  cm = fmaxf(cm, dppf<ROR4>(cm));
  cm = fmaxf(cm, dppf<ROR8>(cm));
  cm = x32max(x16max(cm));
  const float mn = fmaxf(a.m, cm);
  const float al = ex2(a.m - mn), p = ex2(t - mn);
  a.l = a.l * al + p;
  a.m = mn;
  a.o *= al;
  return pk2(p, dppf<XOR2>(p));
}
template <int J> DI void a_pv1(u32 PP, v4u V, const u32 (&sel)[4], Acc& a) {
  const u32 X = dppu<0x150 + 4 * (J >> 1) + (J & 1)>(PP);
  const v4u B = {__builtin_amdgcn_perm(X, X, sel[0]), __builtin_amdgcn_perm(X, X, sel[1]), __builtin_amdgcn_perm(X, X, sel[2]), __builtin_amdgcn_perm(X, X, sel[3])};
  a.o = mfma16(V, B, a.o);
}
DI void a_pv(u32 PP, const v4u (&V)[8], const u32 (&sel)[4], Acc& a) {
  a_pv1<0>(PP, V[0], sel, a); a_pv1<1>(PP, V[1], sel, a); a_pv1<2>(PP, V[2], sel, a); a_pv1<3>(PP, V[3], sel, a);
  a_pv1<4>(PP, V[4], sel, a); a_pv1<5>(PP, V[5], sel, a); a_pv1<6>(PP, V[6], sel, a); a_pv1<7>(PP, V[7], sel, a);
}
DI void a_loadq(LAS char* L, int pair, int lane, v4u (&QF)[8]) {
  const int n = lane & 15, g = lane >> 4;
  const u32 o = L_QS + pair * 512 + ((n >> 1) & 1) * 256 + (2 * g + (n & 1)) * 16;
  const v4u F0 = lld128(L, o), F1 = lld128(L, o + 128), Z = {0u, 0u, 0u, 0u};
#pragma unroll
  for (int u = 0; u < 4; ++u) { const bool on = (n >> 2) == u; QF[2 * u] = on ? F0 : Z; QF[2 * u + 1] = on ? F1 : Z; }
}
DI void a_flush(LAS char* L, int w, int pair, int lane, const Acc& a) {
  const int n = lane & 15, G = lane >> 4, h = (n >> 1) & 1, jp = ((n >> 2) << 1) | (n & 1);
  float l = a.l + dppf<XOR1>(a.l);
  l += dppf<ROR4>(l);
  l += dppf<ROR8>(l);
  l = x32sum(x16sum(l));
  LAS float* f = (LAS float*)(L + L_FL) + ((w * 2 + pair) * 2 + h) * 128 + 32 * G + jp;
#pragma unroll
  for (int r = 0; r < 4; ++r) f[8 * r] = a.o[r];
  if ((lane & 61) == 0) *(LAS f32x2*)(L + L_FM + ((w * 2 + pair) * 2 + h) * 8) = f32x2{a.m, l};
}
DI void a_reset(Acc& a) { a.m = -INFINITY; a.l = 0.f; a.o = f32x4{0.f, 0.f, 0.f, 0.f}; }
DI void gather_q(LAS char* L, rsrc_t rs, int g, int t, u32 tA, u32 tB) {
  const u32 o0 = WS_E2 + (g * 512 + t) * 16;
  const u32 o[2] = {o0, o0 + 4096};
  v4u v[2];
  poll_gran<2>(rs, o, v, tA, tB);
  const u32 pk = pk2(asf(v[0].x) + asf(v[1].x), asf(v[0].z) + asf(v[1].z));
  *(LAS u16*)(L + L_QS + t * 2) = (u16)pk;
  *(LAS u16*)(L + L_QS + 512 + t * 2) = (u16)(pk >> 16);
}
DI u32 sw2(u32 row) { return (row & 2) | ((row >> 1) & 4); }
DI u32 fsw(u32 row) { return ((row & 3) << 2) | ((row >> 2) & 3); }
DI u32 voff1152(int n, int lane) { const u32 e = n * 64 + lane, row = e / 72, pos = e - row * 72; return row * 1152 + (pos ^ sw2(row)) * 16; }
template <int NL> DI void reduce_col(rsrc_t rs, u32 box, int col, int lane, u32 tA, u32 tB, u16* out) {
  u32 o[NL];
  v4u v[NL];
#pragma unroll
  for (int i = 0; i < NL; ++i) o[i] = box + (((col >> 2) * (32 * NL) + 32 * i + (lane >> 1)) * 8 + (col & 3) * 2 + (lane & 1)) * 16;
  poll_gran<NL>(rs, o, v, tA, tB);
  float a = 0.f, b = 0.f;
#pragma unroll
  for (int i = 0; i < NL; ++i) { a += asf(v[i].x); b += asf(v[i].z); }
  a += dppf<XOR2>(a); b += dppf<XOR2>(b);
  a += dppf<ROR4>(a); b += dppf<ROR4>(b);
  a += dppf<ROR8>(a); b += dppf<ROR8>(b);
  a = x32sum(x16sum(a)); b = x32sum(x16sum(b));
  const u32 pk = pk2(a, b);
  if (lane < 2) { out[(2 * lane) * 1152 + col] = (u16)pk; out[(2 * lane + 1) * 1152 + col] = (u16)(pk >> 16); }
}

template <bool FIRST>
DI void ba_pass(LAS char* L, int w, int lane, const v4u (&qf)[16], f32x16 (&acc)[2], float& m, float& l) {
  const int kb = w & 3, ch = w >> 2, hh = lane >> 5, r31 = lane & 31;
  f32x16 S;
#pragma unroll
  for (int i = 0; i < 16; ++i) S[i] = 0.f;
  const u32 rowb = (32 * kb + r31) * 1024 + ch * 512, f = fsw(r31);
#pragma unroll
  for (int ks = 0; ks < 16; ++ks) S = mfma32(lld128(L, rowb + (ks >> 3) * 256 + (((2 * (ks & 7) + hh) ^ f) * 16)), qf[ks], S);
  float s[8];
  {
    v4u a, b;
#pragma unroll
    for (int j = 0; j < 4; ++j) { a[j] = asu(ch ? S[j] : S[8 + j]); b[j] = asu(ch ? S[4 + j] : S[12 + j]); }
    lst128(L, L_SX + w * 2048 + lane * 16, a);
    lst128(L, L_SX + w * 2048 + 1024 + lane * 16, b);
    BAR();
    a = lld128(L, L_SX + (w ^ 4) * 2048 + lane * 16);
    b = lld128(L, L_SX + (w ^ 4) * 2048 + 1024 + lane * 16);
#pragma unroll
    for (int j = 0; j < 4; ++j) {
      s[j] = ((ch ? S[8 + j] : S[j]) + asf(a[j])) * (SCALE * LOG2E);
      s[4 + j] = ((ch ? S[12 + j] : S[4 + j]) + asf(b[j])) * (SCALE * LOG2E);
    }
  }
  float cm = fmaxf(fmaxf(fmaxf(s[0], s[1]), fmaxf(s[2], s[3])), fmaxf(fmaxf(s[4], s[5]), fmaxf(s[6], s[7])));
  cm = x32max(cm);
  if (lane < 32) *(LAS float*)(L + L_WM + (w * 32 + lane) * 4) = cm;
  BAR();
  float mn = FIRST ? -INFINITY : m;
#pragma unroll
  for (int i = 0; i < 8; ++i) mn = fmaxf(mn, *(LAS float*)(L + L_WM + (i * 32 + r31) * 4));
  float ps = 0.f;
#pragma unroll
  for (int j = 0; j < 8; ++j) { s[j] = ex2(s[j] - mn); ps += s[j]; }
  lst128(L, L_P + (2 * kb + ch) * 1024 + lane * 16, v4u{pk2(s[0], s[1]), pk2(s[2], s[3]), pk2(s[4], s[5]), pk2(s[6], s[7])});
  if (FIRST) {
    l = ps;
#pragma unroll
    for (int i = 0; i < 16; ++i) { acc[0][i] = 0.f; acc[1][i] = 0.f; }
  } else {
    const float al = ex2(m - mn);
    l = l * al + ps;
    acc[0] *= al;
    acc[1] *= al;
  }
  m = mn;
  BAR();
  const int q = (lane & 15) >> 2, pp = lane & 3, blk = (lane >> 4) & 1;
  u32 ta[2][2];
#pragma unroll
  for (int T = 0; T < 2; ++T)
#pragma unroll
    for (int t = 0; t < 2; ++t) {
      const u32 c = 8 * w + 4 * T + 2 * blk + (pp >> 1);
      ta[T][t] = (8 * t + 4 * hh + q) * 1024 + ((c ^ ((q << 2) | (2 * t + hh))) * 16) + 8 * (pp & 1);
    }
#pragma unroll
  for (int ks = 0; ks < 8; ++ks) {
    const v4u P = lld128(L, L_P + ks * 1024 + lane * 16);
#pragma unroll
    for (int T = 0; T < 2; ++T) acc[T] = mfma32(ltr2(L, ta[T][0] + ks * 16384, ta[T][1] + ks * 16384), P, acc[T]);
  }
}

__global__ void __launch_bounds__(512, 2) decode_mega(Params p) {
  extern __shared__ __attribute__((aligned(16))) char smem[];
  LAS char* L = (LAS char*)smem;
  const int tid = threadIdx.x, lane = tid & 63, w = __builtin_amdgcn_readfirstlane(tid >> 6);
  const int wg = blockIdx.x, g = wg >> 3, s = wg & 7, r = (s + g) & 7;
  const int kvh = g >> 1;
  const rsrc_t rs = __builtin_amdgcn_make_buffer_rsrc((void*)p.ws, 0, WS_BYTES, 0x00020000);
  const u64 epoch = *(const u64*)(p.ws + WS_EPOCH);
  u32 tA = (u32)epoch + 1u, tB = (u32)(epoch >> 32) + 1u;
  GAS u32* cnt = (GAS u32*)(p.ws + WS_CNT + g * 128);
  const u32 cnt_off = lane == 0 ? WS_CNT + g * 128 : 0x7ffffff0u;

  const int n16 = lane & 15, g4 = lane >> 4;
  const u32 voffK = (n16 >> 1) * 256 + (2 * g4 + (n16 & 1)) * 16, voffV = g4 * 512 + n16 * 16;
  const size_t kv0 = ((size_t)(g & 1) * 32 + kvh) * (4096 * 256) + 4096;
#define CHUNK_OFF(id) (kv0 + (size_t)((id) >> 7) * (16 * 4096 * 256) + (size_t)((id) & 127) * 8192)
#define KIMM(j) (((j) >> 1) * 2048 + ((j) & 1) * 128 - 4096)
#define VIMM(j) (((j) >> 1) * 2048 + ((j) & 1) * 256 - 4096)
  u32 sel[4];
  {
    const u32 h2 = (n16 >> 1) & 1, by = (2 * h2) | ((2 * h2 + 1) << 8);
    const u32 on = (n16 & 1) ? ((by << 16) | 0x0c0cu) : (0x0c0c0000u | by);
#pragma unroll
    for (int i = 0; i < 4; ++i) sel[i] = (n16 >> 2) == i ? on : 0x0c0c0c0cu;
  }
  const bool t1 = lane & 1;
  const u32 trq = (n16 >> 2), trp = lane & 3, trrow = 8 * g4 + trq;

  v4u K[8], V[8], QF[8];
  Acc acc;
  u32 idv = 0;
  int cur = 0;
  bool have = false;

  if (r < 4) {
    const int ai = (g & 1) * 4 + r;
    if (w < 4) {
      const u32 row8 = lane >> 3;
      const u32 vw = row8 * 8192 + (((lane & 7) ^ sw2(row8 | ((w & 1) << 3))) * 16);
      const char* sb = p.Bwq + (size_t)((r >> 1) * 288 + 8 * w) * 8192 + g * 256 + (r & 1) * 128;
      const char* sa = p.Awq + (size_t)((ai >> 2) * 288 + 8 * w) * 8192 + kvh * 512 + (ai & 3) * 128;
      if (w == 0) STAMP(20);
#pragma unroll
      for (int i = 0; i < 9; ++i) dma16(sb + (size_t)i * 32 * 8192, vw, L_QBW + (4 * i + w) * 1024);
      if (w == 0) STAMP(21);
      VMWAIT(8);
      if (w == 0) STAMP(22);
#pragma unroll
      for (int i = 0; i < 9; ++i) { VMWAIT(8); dma16(sa + (size_t)i * 32 * 8192, vw, L_QAW + (4 * i + w) * 1024); }
      BAR();
      asm volatile("" ::"s"(tA), "s"(tB));
      v4u K0[8], V0[8];
      const int id0 = r * 4 + w;
      const char* k0 = p.AK + CHUNK_OFF(id0); const char* v0 = p.AV + CHUNK_OFF(id0);
      const char* k1 = p.AK + CHUNK_OFF(id0 + 16); const char* v1 = p.AV + CHUNK_OFF(id0 + 16);
#pragma unroll
      for (int j = 0; j < 8; ++j) { VMWAIT(8); K0[j] = ldnt(k0, voffK, KIMM(j)); SB(); }
      VMWAIT(8);
      BAR();
#pragma unroll
      for (int j = 0; j < 8; ++j) { VMWAIT(PACE); V0[j] = ldnt(v0, voffV, VIMM(j)); SB(); }
#pragma unroll
      for (int j = 0; j < 8; ++j) { VMWAIT(PACE); K[j] = ldnt(k1, voffK, KIMM(j)); SB(); }
#pragma unroll
      for (int j = 0; j < 8; ++j) { VMWAIT(PACE); V[j] = ldnt(v1, voffV, VIMM(j)); SB(); }
      a_reset(acc);
      a_flush(L, w, 1, lane, acc);
      if (w == 0) STAMP(1);
      BAR();
      if (w == 0) STAMP(2);
      a_loadq(L, 0, lane, QF);
      const u32 PP = a_soft(a_qk(K0, QF, t1), acc);
      a_pv(PP, V0, sel, acc);
      idv = id0 + 32;
      have = true;
    } else {
      const int ct = w - 4;
      v4u xb[9], xa[9];
      const u32 xo = (lane & 3) * 1152 + g4 * 16;
#pragma unroll
      for (int i = 0; i < 9; ++i) xb[i] = ldg(p.Bx + (r >> 1) * 576 + i * 64, xo);
#pragma unroll
      for (int i = 0; i < 9; ++i) xa[i] = ldg(p.Ax + (ai >> 2) * 576 + i * 64, xo);
      if (r == 0 && w == 4 && lane == 0) __hip_atomic_store(cnt, 48u, RLX_AGENT);
      asm volatile("" ::"s"(tA), "s"(tB));
      const u32 tb = trrow * 128 + ((2 * ct + (trp >> 1)) ^ sw2(trrow)) * 16 + 8 * (trp & 1);
      VMWAIT(0);
      if (w == 4) STAMP(23);
      BAR();
      if (w == 4) STAMP(3);
      f32x4 d = gemv288(L, L_QBW + tb, xb);
      if (lane < 32) st_gran(rs, WS_E1 + (((g * 2 + (r >> 1)) * 128 + 64 * (r & 1) + 16 * ct + n16) * 2 + g4) * 16, g4 ? d[2] : d[0], g4 ? d[3] : d[1], tA, tB);
      if (w == 4) STAMP(4);
      BAR();
      d = gemv288(L, L_QAW + tb, xa);
      if (lane < 32) st_gran(rs, WS_E2 + (((kvh * 2 + g4) * 2 + (ai >> 2)) * 256 + 64 * (ai & 3) + 16 * ct + n16) * 16, g4 ? d[2] : d[0], g4 ? d[3] : d[1], tA, tB);
      if (w == 4) STAMP(5);
      gather_q(L, rs, g, tid - 256, tA, tB);
      if (w == 4) STAMP(6);
      BAR();
      const char* so = p.Awo + (size_t)(kvh * 256 + ai * 32) * 1152;
#pragma unroll
      for (int i = 0; i < 9; ++i) dma16(so, voff1152(4 * i + ct, lane), L_WO + (4 * i + ct) * 1024);
      VMWAIT(0);
    }
  } else {
    if (r < 6) {
      const int bi = g * 2 + (r - 4), b = bi >> 4, sp = bi & 15;
      const char* cb = p.BC + ((size_t)b * 4096 + sp * 256 + 16 * w) * 1024;
#pragma unroll
      for (int i = 0; i < 16; ++i) dma16(cb + i * 1024, (lane ^ fsw(i)) * 16, (16 * w + i) * 1024);
      asm volatile("" ::"s"(tA), "s"(tB));
      if (w == 0) poll_flags(rs, WS_FQ + lane * 64, tA, tB);
      if (w == 0) STAMP(7);
      BAR();
      v4u qs[4], c2[16];
#pragma unroll
      for (int i = 0; i < 4; ++i) qs[i] = ld_sc1(rs, WS_QABS + (b * 32 + 4 * w + i) * 1024 + (lane ^ fsw(4 * w + i)) * 16);
      SB();
      asm volatile("s_barrier" ::: "memory");
#pragma unroll
      for (int i = 0; i < 16; ++i) c2[i] = ldnt(cb + 128 * 1024 + i * 1024, (lane ^ fsw(i)) * 16, 0);
      SB();
#pragma unroll
      for (int i = 0; i < 4; ++i) lst128(L, L_X + (4 * w + i) * 1024 + lane * 16, qs[i]);
      BAR();
      v4u qf[16];
      {
        const u32 rowb = L_X + (lane & 31) * 1024 + (w >> 2) * 512, f = fsw(lane & 31);
#pragma unroll
        for (int ks = 0; ks < 16; ++ks) qf[ks] = lld128(L, rowb + (ks >> 3) * 256 + (((2 * (ks & 7) + (lane >> 5)) ^ f) * 16));
      }
      BAR();
      if (w == 0) STAMP(8);
      f32x16 ac[2];
      float m, l;
      ba_pass<true>(L, w, lane, qf, ac, m, l);
      BAR();
      if (w == 0) STAMP(9);
#pragma unroll
      for (int i = 0; i < 16; ++i) lst128(L, (16 * w + i) * 1024 + lane * 16, c2[i]);
      BAR();
      if (w == 0) STAMP(10);
      ba_pass<false>(L, w, lane, qf, ac, m, l);
      if (w == 0) STAMP(29);
      l = x32sum(l);
      if (lane < 32) *(LAS float*)(L + L_WL + (w * 32 + lane) * 4) = l;
      BAR();
      {
        const u32 hd = lane & 31, hh = lane >> 5;
#pragma unroll
        for (int T = 0; T < 2; ++T)
#pragma unroll
          for (int rq = 0; rq < 4; ++rq)
            *(LAS v2u*)(L + hd * 1024 + (((8 * w + 4 * T + rq) ^ (hd & 15)) * 16) + 8 * hh) =
                v2u{pk2(ac[T][4 * rq], ac[T][4 * rq + 1]), pk2(ac[T][4 * rq + 2], ac[T][4 * rq + 3])};
      }
      if (tid < 32) {
        float ls = 0.f;
#pragma unroll
        for (int i = 0; i < 8; ++i) ls += *(LAS float*)(L + L_WL + (i * 32 + lane) * 4);
        st_sc1_8(rs, WS_ML + ((lane * 4 + b) * 16 + sp) * 8, v2u{asu(m), asu(ls)});
      }
      BAR();
#pragma unroll
      for (int i = 0; i < 4; ++i) {
        const u32 hd = 4 * w + i;
        st_sc1(rs, WS_PART + ((hd * 4 + b) * 16 + sp) * 1024 + lane * 16, lld128(L, hd * 1024 + ((lane ^ (hd & 15)) * 16)));
      }
      VMWAIT(0);
      BAR();
      if (tid == 0) st_sc1_8(rs, WS_FP + bi * 64, v2u{tA, tB});
      if (w == 0) STAMP(11);
    } else {
      const int h = (g >> 3) * 8 + s, half = r - 6, bw = h * 2 + half;
      {
        const char* sk = p.Bwuk + (size_t)(half * 256 + 4 * w) * 8192 + h * 256;
        const u32 vk = g4 * 8192 + ((n16 ^ g4 ^ (4 * (w & 3))) * 16);
#pragma unroll
        for (int i = 0; i < 8; ++i) dma16(sk + (size_t)i * 32 * 8192, vk, (8 * i + w) * 1024);
      }
      asm volatile("" ::"s"(tA), "s"(tB));
      if (w < 4) {
        const u32 o0 = WS_E1 + (h * 512 + tid) * 16;
        const u32 o[2] = {o0, o0 + 4096};
        v4u v[2];
        poll_gran<2>(rs, o, v, tA, tB);
        const u32 pk = pk2(asf(v[0].x) + asf(v[1].x), asf(v[0].z) + asf(v[1].z));
        const u32 qo = L_QB + (tid & 1) * 640 + (tid >> 1) * 2;
        *(LAS u16*)(L + qo) = (u16)pk;
        *(LAS u16*)(L + qo + 320) = (u16)(pk >> 16);
      }
      VMWAIT(0);
      BAR();
      if (w == 0) STAMP(12);
      {
        f32x4 d0 = {0.f, 0.f, 0.f, 0.f}, d1 = d0;
#pragma unroll
        for (int ks = 0; ks < 4; ++ks) {
          const v4u qb = lld128(L, L_QB + (lane & 3) * 320 + ks * 64 + g4 * 16);
          const u32 a0 = (32 * w + n16) * 256 + (((4 * ks + g4) ^ n16) * 16);
          d0 = mfma16(lld128(L, a0), qb, d0);
          d1 = mfma16(lld128(L, a0 + 4096), qb, d1);
        }
        const bool hi = n16 & 4;
        const v2u o = {pk2(hi ? d1[0] : d0[0], hi ? d1[1] : d0[1]), pk2(hi ? d1[2] : d0[2], hi ? d1[3] : d0[3])};
        if (n16 < 8) st_sc1_8(rs, WS_QABS + ((lane & 3) * 32 + h) * 1024 + (half * 256 + 32 * w + (hi ? 16 : 0) + 4 * g4) * 2, o);
      }
      VMWAIT(0);
      BAR();
      if (tid == 0) st_sc1_8(rs, WS_FQ + bw * 64, v2u{tA, tB});
      if (w == 0) STAMP(13);
      {
        const u32 row8 = lane >> 3;
        const u32 vw = row8 * 8192 + (((lane & 7) ^ sw2(row8 | ((w & 1) << 3))) * 16);
        const char* sv = p.Bwuv + (size_t)(8 * w) * 8192 + h * 256 + half * 128;
#pragma unroll
        for (int i = 0; i < 8; ++i) dma16(sv + (size_t)i * 64 * 8192, vw, (8 * i + w) * 1024);
        const char* so = p.Bwo + (size_t)(h * 128 + half * 64) * 1152;
#pragma unroll
        for (int i = 0; i < 9; ++i) dma16(so, voff1152(8 * i + w, lane), L_W2 + (8 * i + w) * 1024);
      }
      VMWAIT(0);
      if (w == 0) STAMP(24);
      if (w == 0) poll_flags(rs, WS_FP + lane * 64, tA, tB);
      BAR();
      if (w == 0) STAMP(25);
      {
        const int b = w >> 1, sh = w & 1;
        const v2u ml = ld_sc1_8(rs, WS_ML + ((h * 4 + b) * 16 + n16) * 8);
        v4u pt[8];
#pragma unroll
        for (int i = 0; i < 8; ++i) pt[i] = ld_sc1(rs, WS_PART + ((h * 4 + b) * 16 + 8 * sh + i) * 1024 + lane * 16);
        float M = fmaxf(asf(ml.x), dppf<XOR1>(asf(ml.x)));
        M = fmaxf(M, dppf<XOR2>(M)); M = fmaxf(M, dppf<ROR4>(M)); M = fmaxf(M, dppf<ROR8>(M));
        const float wt = ex2(asf(ml.x) - M);
        float Ls = wt * asf(ml.y);
        Ls += dppf<XOR1>(Ls); Ls += dppf<XOR2>(Ls); Ls += dppf<XOR7>(Ls); Ls += dppf<0x140>(Ls);
        const float wn = wt / Ls;
        float c[8];
#pragma unroll
        for (int k = 0; k < 8; ++k) c[k] = 0.f;
#pragma unroll
        for (int i = 0; i < 8; ++i) {
          const float wi = asf(__builtin_amdgcn_readlane(asu(wn), 8 * sh + i));
#pragma unroll
          for (int k = 0; k < 4; ++k) { c[2 * k] += wi * asf(pt[i][k] << 16); c[2 * k + 1] += wi * asf(pt[i][k] & 0xffff0000u); }
        }
        if (sh) {
          lst128(L, L_CX + b * 2048 + lane * 16, v4u{asu(c[0]), asu(c[1]), asu(c[2]), asu(c[3])});
          lst128(L, L_CX + b * 2048 + 1024 + lane * 16, v4u{asu(c[4]), asu(c[5]), asu(c[6]), asu(c[7])});
        }
        BAR();
        if (!sh) {
          const v4u x = lld128(L, L_CX + b * 2048 + lane * 16), y = lld128(L, L_CX + b * 2048 + 1024 + lane * 16);
          lst128(L, L_CL + b * 1088 + lane * 16,
                 v4u{pk2(c[0] + asf(x[0]), c[1] + asf(x[1])), pk2(c[2] + asf(x[2]), c[3] + asf(x[3])), pk2(c[4] + asf(y[0]), c[5] + asf(y[1])), pk2(c[6] + asf(y[2]), c[7] + asf(y[3]))});
        }
        BAR();
      }
      if (w == 0) STAMP(26);
      {
        const int ct = w & 3, kh = w >> 2;
        const u32 tb = kh * 32768 + trrow * 128 + ((2 * ct + (trp >> 1)) ^ sw2(trrow)) * 16 + 8 * (trp & 1);
        f32x4 d = {0.f, 0.f, 0.f, 0.f};
#pragma unroll
        for (int ks = 0; ks < 8; ++ks)
          d = mfma16(lld128(L, L_CL + (lane & 3) * 1088 + kh * 512 + ks * 64 + g4 * 16), ltr2(L, tb + ks * 4096, tb + ks * 4096 + 512), d);
        if (lane < 16) {
#pragma unroll
          for (int b = 0; b < 4; ++b) *(LAS float*)(L + L_CP + ((kh * 4 + b) * 64 + 16 * ct + lane) * 4) = d[b];
        }
        BAR();
        v4u af[2];
#pragma unroll
        for (int ks = 0; ks < 2; ++ks) {
          const u32 o = L_CP + ((lane & 3) * 64 + 32 * ks + 8 * g4) * 4;
          const v4u a0 = lld128(L, o), a1 = lld128(L, o + 16), b0 = lld128(L, o + 1024), b1 = lld128(L, o + 1040);
          af[ks] = v4u{pk2(asf(a0[0]) + asf(b0[0]), asf(a0[1]) + asf(b0[1])), pk2(asf(a0[2]) + asf(b0[2]), asf(a0[3]) + asf(b0[3])),
                       pk2(asf(a1[0]) + asf(b1[0]), asf(a1[1]) + asf(b1[1])), pk2(asf(a1[2]) + asf(b1[2]), asf(a1[3]) + asf(b1[3]))};
        }
        const u32 t2 = L_W2 + trrow * 1152 + ((2 * w + (trp >> 1)) ^ sw2(trrow)) * 16 + 8 * (trp & 1);
#pragma unroll
        for (int i = 0; i < 5; ++i) {
          if (i < 4 || w < 4) {
            f32x4 e = {0.f, 0.f, 0.f, 0.f};
#pragma unroll
            for (int ks = 0; ks < 2; ++ks) e = mfma16(af[ks], ltr2(L, t2 + i * 256 + ks * 36864, t2 + i * 256 + ks * 36864 + 4608), e);
            const int T = 8 * i + w;
            if (lane < 32) st_gran(rs, WS_E6 + (((4 * T + (n16 >> 2)) * 64 + bw) * 8 + (n16 & 3) * 2 + g4) * 16, g4 ? e[2] : e[0], g4 ? e[3] : e[1], tA, tB);
          }
        }
      }
      if (w == 0) STAMP(27);
      reduce_col<2>(rs, WS_E6, 9 * bw + w, lane, tA, tB, p.out + 576);
      if (w == 0) STAMP(28);
      if (w == 0) reduce_col<2>(rs, WS_E6, 9 * bw + 8, lane, tA, tB, p.out + 576);
      if (w == 0) STAMP(14);
      BAR();
    }
    if (w >= 4) gather_q(L, rs, g, tid - 256, tA, tB);
    BAR();
    if (w < 4) {
      const u32 a = __builtin_amdgcn_raw_ptr_buffer_atomic_add_i32(2, rs, cnt_off, 0, 0);
      a_reset(acc);
      a_flush(L, w, 0, lane, acc);
      a_flush(L, w, 1, lane, acc);
      const u32 id0 = __builtin_amdgcn_readfirstlane(a);
      idv = id0 + 1;
      if (id0 < 256) {
        const char* k1 = p.AK + CHUNK_OFF(id0); const char* v1 = p.AV + CHUNK_OFF(id0);
#pragma unroll
        for (int j = 0; j < 8; ++j) K[j] = ldnt(k1, voffK, KIMM(j));
        SB();
#pragma unroll
        for (int j = 0; j < 8; ++j) V[j] = ldnt(v1, voffV, VIMM(j));
        SB();
        cur = id0 >> 7;
        a_loadq(L, cur, lane, QF);
        have = true;
      }
    }
  }

  if (have) {
    float S;
    for (;;) {
      S = a_qk(K, QF, t1);
      SB();
      const u32 id = __builtin_amdgcn_readfirstlane(idv);
      if (id >= 256) break;
      idv = __builtin_amdgcn_raw_ptr_buffer_atomic_add_i32(1, rs, cnt_off, 0, 0);
      const char* kn = p.AK + CHUNK_OFF(id); const char* vn = p.AV + CHUNK_OFF(id);
#pragma unroll
      for (int j = 0; j < 8; ++j) K[j] = ldnt(kn, voffK, KIMM(j));
      SB();
      const u32 PP = a_soft(S, acc);
      a_pv(PP, V, sel, acc);
      asm volatile("" : "+v"(acc.o) :: "memory");
      SB();
#pragma unroll
      for (int j = 0; j < 8; ++j) V[j] = ldnt(vn, voffV, VIMM(j));
      SB();
      if ((int)(id >> 7) != cur) {
        a_flush(L, w, 0, lane, acc);
        a_reset(acc);
        cur = 1;
        a_loadq(L, 1, lane, QF);
      }
    }
    const u32 PP = a_soft(S, acc);
    a_pv(PP, V, sel, acc);
    a_flush(L, w, cur, lane, acc);
  }
  BAR();
  if (w == 7) STAMP(16);
  if (w < 4) {
    float mw[4], lw[4];
    f32x2 aw[4];
#pragma unroll
    for (int i = 0; i < 4; ++i) {
      const f32x2 ml = *(LAS f32x2*)(L + L_FM + (i * 4 + w) * 8);
      mw[i] = ml.x; lw[i] = ml.y;
      aw[i] = *(LAS f32x2*)(L + L_FL + ((i * 4 + w) * 128 + 2 * lane) * 4);
    }
    const float M = fmaxf(fmaxf(mw[0], mw[1]), fmaxf(mw[2], mw[3])), Ms = M == -INFINITY ? 0.f : M;
    float Ls = 0.f, o0 = 0.f, o1 = 0.f;
#pragma unroll
    for (int i = 0; i < 4; ++i) { const float wt = ex2(mw[i] - Ms); Ls += wt * lw[i]; o0 += wt * aw[i].x; o1 += wt * aw[i].y; }
    const u32 rec = ((g * 2 + (w >> 1)) * 8 + s) * 2 + (w & 1);
    st_gran(rs, WS_E3 + (rec * 64 + lane) * 16, o0, o1, tA, tB);
    if (lane == 0) st_gran(rs, WS_E3M + rec * 16, M, Ls, tA, tB);
  }
  if (r < 4) {
    const int ai = (g & 1) * 4 + r;
    v4u wf[9];
    if (w >= 4) {
      const u32 tb = L_WO + trrow * 1152 + ((2 * (w - 4) + (trp >> 1)) ^ sw2(trrow)) * 16 + 8 * (trp & 1);
#pragma unroll
      for (int i = 0; i < 9; ++i) wf[i] = ltr2(L, tb + i * 128, tb + i * 128 + 4608);
    }
    {
      const int b = w >> 1, i16 = (tid >> 3) & 15, sl = lane & 7;
      const u32 rec = ((kvh * 4 + b) * 8 + sl) * 2 + (ai >> 2);
      const u32 o[2] = {WS_E3 + (rec * 64 + 16 * (ai & 3) + i16) * 16, WS_E3M + rec * 16};
      v4u v[2];
      poll_gran<2>(rs, o, v, tA, tB);
      const float m = asf(v[1].x);
      float M = fmaxf(m, dppf<XOR1>(m));
      M = fmaxf(M, dppf<XOR2>(M)); M = fmaxf(M, dppf<XOR7>(M));
      const float wt = ex2(m - M);
      float Ls = wt * asf(v[1].z), o0 = wt * asf(v[0].x), o1 = wt * asf(v[0].z);
      Ls += dppf<XOR1>(Ls); o0 += dppf<XOR1>(o0); o1 += dppf<XOR1>(o1);
      Ls += dppf<XOR2>(Ls); o0 += dppf<XOR2>(o0); o1 += dppf<XOR2>(o1);
      Ls += dppf<XOR7>(Ls); o0 += dppf<XOR7>(o0); o1 += dppf<XOR7>(o1);
      const float inv = 1.f / Ls;
      if (sl == 0) *(LAS u32*)(L + L_OA + b * 64 + i16 * 4) = pk2(o0 * inv, o1 * inv);
    }
    BAR();
    if (w == 0) STAMP(17);
    if (w >= 4) {
      const v4u af = lld128(L, L_OA + (lane & 3) * 64 + g4 * 16);
#pragma unroll
      for (int i = 0; i < 9; ++i) {
        const f32x4 e = mfma16(af, wf[i], f32x4{0.f, 0.f, 0.f, 0.f});
        const int T = 4 * i + (w - 4);
        if (lane < 32) st_gran(rs, WS_E4 + (((4 * T + (n16 >> 2)) * 128 + g * 4 + r) * 8 + (n16 & 3) * 2 + g4) * 16, g4 ? e[2] : e[0], g4 ? e[3] : e[1], tA, tB);
      }
      if (w == 4) STAMP(18);
    }
  } else {
    const int ri = r < 6 ? g * 2 + (r - 4) : 64 + g * 2 + (r - 6);
    if (ri < 72) {
      reduce_col<4>(rs, WS_E4, ri * 8 + w, lane, tA, tB, p.out);
      if (ri == 0 && tid == 0) __hip_atomic_store((GAS u64*)(p.ws + WS_EPOCH), ((u64)tB << 32) | tA, RLX_AGENT);
      if (w == 0) STAMP(19);
    }
  }
}

extern "C" void kernel_launch(void* const* d_in, const int* in_sizes, int n_in, void* d_out, int out_size, void* d_ws, size_t ws_size,
                              hipStream_t stream) {
  Params p;
  p.Ax = (const char*)d_in[0]; p.AK = (const char*)d_in[1]; p.AV = (const char*)d_in[2]; p.Awq = (const char*)d_in[3]; p.Awo = (const char*)d_in[4];
  p.Bx = (const char*)d_in[5]; p.BC = (const char*)d_in[6]; p.Bwq = (const char*)d_in[7]; p.Bwuk = (const char*)d_in[8]; p.Bwuv = (const char*)d_in[9];
  p.Bwo = (const char*)d_in[10];
  p.out = (u16*)d_out;
  p.ws = (char*)d_ws;
  (void)hipFuncSetAttribute((const void*)decode_mega, hipFuncAttributeMaxDynamicSharedMemorySize, LDS_BYTES);
  decode_mega<<<256, 512, LDS_BYTES, stream>>>(p);
}
```

```cpp
#include <hip/hip_runtime.h>
#include <stdint.h>

typedef unsigned int u32;
typedef unsigned long long u64;
typedef unsigned short u16;
typedef u32 v4u __attribute__((ext_vector_type(4)));
typedef u32 v2u __attribute__((ext_vector_type(2)));
typedef u32 v8u __attribute__((ext_vector_type(8)));
typedef u32 v16u __attribute__((ext_vector_type(16)));
typedef float f32x2 __attribute__((ext_vector_type(2)));
typedef float f32x4 __attribute__((ext_vector_type(4)));
typedef float f32x16 __attribute__((ext_vector_type(16)));
typedef __bf16 bf16x8 __attribute__((ext_vector_type(8)));
typedef __bf16 bf16x2 __attribute__((ext_vector_type(2)));
typedef short s16x4 __attribute__((ext_vector_type(4)));
#define DI __device__ __forceinline__
#define LAS __attribute__((address_space(3)))
#define GAS __attribute__((address_space(1)))
#define RLX_AGENT __ATOMIC_RELAXED, __HIP_MEMORY_SCOPE_AGENT

#define STAMPS 0
#define CODE_BYTES 27700
#define PACE 8

constexpr int WS_EPOCH = 0;
constexpr int WS_CNT = 1024;
constexpr int WS_FQ = 8192;
constexpr int WS_FP = 12288;
constexpr int WS_ML = 16384;
constexpr int WS_E3M = 32768;
constexpr int WS_QABS = 65536;
constexpr int WS_E1 = 262144;
constexpr int WS_E2 = 524288;
constexpr int WS_E3 = 1 << 20;
constexpr int WS_PART = 2 << 20;
constexpr int WS_E4 = 4 << 20;
constexpr int WS_E6 = 7 << 20;
constexpr int WS_BYTES = 9 << 20;
constexpr int LDS_BYTES = 163840;
constexpr int L_QBW = 0, L_QAW = 36864, L_WO = 73728;
constexpr int L_XV = 110592;
constexpr int L_QS = 139264;
constexpr int L_FL = L_QS + 1024;
constexpr int L_FM = L_FL + 8192;
constexpr int L_OA = L_FM + 128;
constexpr int L_X = 131072;
constexpr int L_SX = L_X, L_P = L_X + 16384, L_WM = L_X + 24576, L_WL = L_X + 25600;
constexpr int L_SYNC = L_X + 26624;
constexpr int L_W2 = 65536;
constexpr int L_QB = L_OA + 256;
constexpr int L_CL = L_QB + 1280;
constexpr int L_CT = L_CL + 4352;
static_assert(L_CT + 768 <= L_WM, "LDS map");

constexpr float SCALE = 0.08838834764831845f;
constexpr float LOG2E = 1.4426950408889634f;
constexpr u32 SPIN_CAP = 1u << 18;

struct Params {
  const char *Ax, *AK, *AV, *Awq, *Awo, *Bx, *BC, *Bwq, *Bwuk, *Bwuv, *Bwo;
  u16* out;
  char* ws;
};

DI f32x4 mfma16(v4u a, v4u b, f32x4 c) {
  return __builtin_amdgcn_mfma_f32_16x16x32_bf16(__builtin_bit_cast(bf16x8, a), __builtin_bit_cast(bf16x8, b), c, 0, 0, 0);
}
DI f32x16 mfma32(v4u a, v4u b, f32x16 c) {
  return __builtin_amdgcn_mfma_f32_32x32x16_bf16(__builtin_bit_cast(bf16x8, a), __builtin_bit_cast(bf16x8, b), c, 0, 0, 0);
}
DI u32 pk2(float a, float b) { f32x2 v = {a, b}; return __builtin_bit_cast(u32, __builtin_convertvector(v, bf16x2)); }
DI float rbf(float a) { return (float)(__bf16)a; }
DI float asf(u32 x) { return __builtin_bit_cast(float, x); }
DI u32 asu(float x) { return __builtin_bit_cast(u32, x); }
DI float ex2(float x) { return __builtin_amdgcn_exp2f(x); }
template <int CTRL> DI float dppf(float x) { return asf(__builtin_amdgcn_mov_dpp(asu(x), CTRL, 0xf, 0xf, true)); }
template <int CTRL> DI u32 dppu(u32 x) { return __builtin_amdgcn_mov_dpp(x, CTRL, 0xf, 0xf, true); }
constexpr int XOR1 = 0xB1, XOR2 = 0x4E, XOR7 = 0x141, ROR8 = 0x128, ROR4 = 0x124;
DI float x16max(float x) { auto s = __builtin_amdgcn_permlane16_swap(asu(x), asu(x), false, false); return fmaxf(asf(s[0]), asf(s[1])); }
DI float x32max(float x) { auto s = __builtin_amdgcn_permlane32_swap(asu(x), asu(x), false, false); return fmaxf(asf(s[0]), asf(s[1])); }
DI float x16sum(float x) { auto s = __builtin_amdgcn_permlane16_swap(asu(x), asu(x), false, false); return asf(s[0]) + asf(s[1]); }
DI float x32sum(float x) { auto s = __builtin_amdgcn_permlane32_swap(asu(x), asu(x), false, false); return asf(s[0]) + asf(s[1]); }

#define BAR() asm volatile("s_waitcnt lgkmcnt(0)\n\ts_barrier" ::: "memory")
#define VMWAIT(n) asm volatile("s_waitcnt vmcnt(%0)" ::"n"(n) : "memory")
#define SB() __builtin_amdgcn_sched_barrier(0)

DI v4u lld128(LAS char* L, u32 o) { return *(LAS v4u*)(L + o); }
DI void lst128(LAS char* L, u32 o, v4u v) { *(LAS v4u*)(L + o) = v; }
DI v2u ltr(LAS char* L, u32 o) { return __builtin_bit_cast(v2u, __builtin_amdgcn_ds_read_tr16_b64_v4i16((LAS s16x4*)(L + o))); }
DI v4u ltr2(LAS char* L, u32 o0, u32 o1) { v2u a = ltr(L, o0), b = ltr(L, o1); return v4u{a.x, a.y, b.x, b.y}; }
DI void dma16(const char* sbase, u32 voff, u32 lds) {
  u32 keep;
  asm volatile("s_mov_b32 %0, m0\n\ts_mov_b32 m0, %3\n\ts_nop 2\n\tglobal_load_lds_dwordx4 %1, %2 nt\n\ts_mov_b32 m0, %0"
               : "=&s"(keep) : "v"(voff), "s"(sbase), "s"(lds) : "memory");
}
DI v4u ldnt(const char* base, u32 voff, int imm) { return __builtin_nontemporal_load((const GAS v4u*)((const GAS char*)base + voff + imm)); }
DI v4u ldg(const char* base, u32 voff) { return *(const GAS v4u*)((const GAS char*)base + voff); }
typedef __amdgpu_buffer_rsrc_t rsrc_t;
DI v4u ld_sc1(rsrc_t rs, u32 o) { return __builtin_amdgcn_raw_buffer_load_b128(rs, o, 0, 16); }
DI v2u ld_sc1_8(rsrc_t rs, u32 o) { return __builtin_amdgcn_raw_buffer_load_b64(rs, o, 0, 16); }
DI void st_sc1(rsrc_t rs, u32 o, v4u v) { __builtin_amdgcn_raw_buffer_store_b128(v, rs, o, 0, 16); }
DI void st_sc1_8(rsrc_t rs, u32 o, v2u v) { __builtin_amdgcn_raw_buffer_store_b64(v, rs, o, 0, 16); }
DI void st_gran(rsrc_t rs, u32 o, float a, float b, u32 tA, u32 tB) { st_sc1(rs, o, v4u{asu(a), tA, asu(b), tB}); }
template <int N> DI void poll_gran(rsrc_t rs, const u32 (&o)[N], v4u (&v)[N], u32 tA, u32 tB) {
  for (u32 spins = 0;; ++spins) {
    bool ok = true;
#pragma unroll
    for (int i = 0; i < N; ++i) v[i] = ld_sc1(rs, o[i]);
#pragma unroll
    for (int i = 0; i < N; ++i) ok = ok && v[i].y == tA && v[i].w == tB;
    if (__all(ok) || spins > SPIN_CAP) break;
    __builtin_amdgcn_s_sleep(1);
  }
}
DI void poll_flags(rsrc_t rs, u32 o, u32 tA, u32 tB) {
  for (u32 spins = 0;; ++spins) {
    const v2u v = ld_sc1_8(rs, o);
    if (__all(v.x == tA && v.y == tB) || spins > SPIN_CAP) break;
    __builtin_amdgcn_s_sleep(2);
  }
  __builtin_amdgcn_fence(__ATOMIC_ACQUIRE, "wavefront");
}

DI u32 lds_inc(LAS char* L, u32 o) {
  asm volatile("s_waitcnt lgkmcnt(0)" ::: "memory");
  u32 old = 0;
  if ((threadIdx.x & 63) == 0) old = __hip_atomic_fetch_add((LAS u32*)(L + o), 1u, __ATOMIC_RELAXED, __HIP_MEMORY_SCOPE_WORKGROUP);
  return __builtin_amdgcn_readfirstlane(old);
}
DI void lds_wait(LAS char* L, u32 o, u32 want) {
  for (u32 spins = 0; spins < (1u << 24); ++spins) {
    if (__builtin_amdgcn_readfirstlane(__hip_atomic_load((LAS u32*)(L + o), __ATOMIC_RELAXED, __HIP_MEMORY_SCOPE_WORKGROUP)) >= want) break;
    __builtin_amdgcn_s_sleep(1);
  }
  asm volatile("" ::: "memory");
}

#define STAMP(i) do {} while (0)

DI f32x4 gemv288(LAS char* L, u32 base, u32 xo) {
  f32x4 d = {0.f, 0.f, 0.f, 0.f};
#pragma unroll
  for (int ks = 0; ks < 9; ++ks) d = mfma16(lld128(L, xo + ks * 64), ltr2(L, base + ks * 4096, base + ks * 4096 + 512), d);
  return d;
}

struct Acc { float m, l; f32x4 o; };
DI float a_qk(const v4u (&K)[8], const v4u (&QF)[8], bool t1) {
  f32x4 d = {0.f, 0.f, 0.f, 0.f};
#pragma unroll
  for (int j = 0; j < 8; ++j) d = mfma16(K[j], QF[j], d);
  const float keep = t1 ? d[3] : d[0], send = t1 ? d[1] : d[2];
  return keep + dppf<XOR1>(send);
}
DI u32 a_soft(float S, Acc& a) {
  const float t = rbf(rbf(S) * SCALE) * LOG2E;
  float cm = fmaxf(t, dppf<XOR1>(t));
  cm = fmaxf(cm, dppf<ROR4>(cm));
  cm = fmaxf(cm, dppf<ROR8>(cm));
  cm = x32max(x16max(cm));
  const float mn = fmaxf(a.m, cm);
  const float al = ex2(a.m - mn), p = ex2(t - mn);
  a.l = a.l * al + p;
  a.m = mn;
  a.o *= al;
  return pk2(p, dppf<XOR2>(p));
}
template <int J> DI void a_pv1(u32 PP, v4u V, const u32 (&sel)[4], Acc& a) {
  const u32 X = dppu<0x150 + 4 * (J >> 1) + (J & 1)>(PP);
  const v4u B = {__builtin_amdgcn_perm(X, X, sel[0]), __builtin_amdgcn_perm(X, X, sel[1]), __builtin_amdgcn_perm(X, X, sel[2]), __builtin_amdgcn_perm(X, X, sel[3])};
  a.o = mfma16(V, B, a.o);
}
DI void a_pv(u32 PP, const v4u (&V)[8], const u32 (&sel)[4], Acc& a) {
  a_pv1<0>(PP, V[0], sel, a); a_pv1<1>(PP, V[1], sel, a); a_pv1<2>(PP, V[2], sel, a); a_pv1<3>(PP, V[3], sel, a);
  a_pv1<4>(PP, V[4], sel, a); a_pv1<5>(PP, V[5], sel, a); a_pv1<6>(PP, V[6], sel, a); a_pv1<7>(PP, V[7], sel, a);
}
DI void a_loadq(LAS char* L, int pair, int lane, v4u (&QF)[8]) {
  const int n = lane & 15, g = lane >> 4;
  const u32 o = L_QS + pair * 512 + ((n >> 1) & 1) * 256 + (2 * g + (n & 1)) * 16;
  const v4u F0 = lld128(L, o), F1 = lld128(L, o + 128), Z = {0u, 0u, 0u, 0u};
#pragma unroll
  for (int u = 0; u < 4; ++u) { const bool on = (n >> 2) == u; QF[2 * u] = on ? F0 : Z; QF[2 * u + 1] = on ? F1 : Z; }
}
DI void a_flush(LAS char* L, int w, int pair, int lane, const Acc& a) {
  const int n = lane & 15, G = lane >> 4, h = (n >> 1) & 1, jp = ((n >> 2) << 1) | (n & 1);
  float l = a.l + dppf<XOR1>(a.l);
  l += dppf<ROR4>(l);
  l += dppf<ROR8>(l);
  l = x32sum(x16sum(l));
  LAS float* f = (LAS float*)(L + L_FL) + ((w * 2 + pair) * 2 + h) * 128 + 32 * G + jp;
#pragma unroll
  for (int r = 0; r < 4; ++r) f[8 * r] = a.o[r];
  if ((lane & 61) == 0) *(LAS f32x2*)(L + L_FM + ((w * 2 + pair) * 2 + h) * 8) = f32x2{a.m, l};
}
DI void a_reset(Acc& a) { a.m = -INFINITY; a.l = 0.f; a.o = f32x4{0.f, 0.f, 0.f, 0.f}; }
DI u32 fetch_q(rsrc_t rs, int g, int t, u32 tA, u32 tB) {
  const u32 o0 = WS_E2 + (g * 512 + t) * 16;
  const u32 o[2] = {o0, o0 + 4096};
  v4u v[2];
  poll_gran<2>(rs, o, v, tA, tB);
  return pk2(asf(v[0].x) + asf(v[1].x), asf(v[0].z) + asf(v[1].z));
}
DI void put_q(LAS char* L, int t, u32 pk) {
  *(LAS u16*)(L + L_QS + t * 2) = (u16)pk;
  *(LAS u16*)(L + L_QS + 512 + t * 2) = (u16)(pk >> 16);
}
DI u32 sw2(u32 row) { return (row & 2) | ((row >> 1) & 4); }
DI u32 fsw(u32 row) { return ((row & 3) << 2) | ((row >> 2) & 3); }
DI u32 voff1152(int n, int lane) { const u32 e = n * 64 + lane, row = e / 72, pos = e - row * 72; return row * 1152 + (pos ^ sw2(row)) * 16; }
template <int NL> DI void reduce_col(rsrc_t rs, u32 box, int col, int lane, u32 tA, u32 tB, u16* out_) {
  GAS u16* out = (GAS u16*)out_;
  u32 o[NL];
  v4u v[NL];
#pragma unroll
  for (int i = 0; i < NL; ++i) o[i] = box + (((col >> 2) * (32 * NL) + 32 * i + (lane >> 1)) * 8 + (col & 3) * 2 + (lane & 1)) * 16;
  poll_gran<NL>(rs, o, v, tA, tB);
  float a = 0.f, b = 0.f;
#pragma unroll
  for (int i = 0; i < NL; ++i) { a += asf(v[i].x); b += asf(v[i].z); }
  a += dppf<XOR2>(a); b += dppf<XOR2>(b);
  a += dppf<ROR4>(a); b += dppf<ROR4>(b);
  a += dppf<ROR8>(a); b += dppf<ROR8>(b);
  a = x32sum(x16sum(a)); b = x32sum(x16sum(b));
  const u32 pk = pk2(a, b);
  if (lane < 2) { out[(2 * lane) * 1152 + col] = (u16)pk; out[(2 * lane + 1) * 1152 + col] = (u16)(pk >> 16); }
}

template <int NL> DI void reduce_quad(rsrc_t rs, u32 box, int quad, int lane, u32 tA, u32 tB, u16* out_) {
  GAS u16* out = (GAS u16*)out_;
  u32 o[NL];
  v4u v[NL];
#pragma unroll
  for (int i = 0; i < NL; ++i) o[i] = box + (quad * NL + i) * 1024 + lane * 16;
  poll_gran<NL>(rs, o, v, tA, tB);
  float a = 0.f, b = 0.f;
#pragma unroll
  for (int i = 0; i < NL; ++i) { a += asf(v[i].x); b += asf(v[i].z); }
  a += dppf<ROR8>(a); b += dppf<ROR8>(b);
  a = x32sum(x16sum(a)); b = x32sum(x16sum(b));
  const u32 pk = pk2(a, b);
  if (lane < 8) { GAS u16* o2 = out + (2 * (lane & 1)) * 1152 + quad * 4 + (lane >> 1); o2[0] = (u16)pk; o2[1152] = (u16)(pk >> 16); }
}

template <bool FIRST>
DI void ba_pass(LAS char* L, int w, int lane, const v4u (&qf)[16], f32x16 (&acc)[2], float& m, float& l) {
  const int kb = w & 3, ch = w >> 2, hh = lane >> 5, r31 = lane & 31;
  f32x16 S;
#pragma unroll
  for (int i = 0; i < 16; ++i) S[i] = 0.f;
  const u32 rowb = (32 * kb + r31) * 1024 + ch * 512, f = fsw(r31);
#pragma unroll
  for (int ks = 0; ks < 16; ++ks) S = mfma32(lld128(L, rowb + (ks >> 3) * 256 + (((2 * (ks & 7) + hh) ^ f) * 16)), qf[ks], S);
  float s[8];
  {
    v4u a, b;
#pragma unroll
    for (int j = 0; j < 4; ++j) { a[j] = asu(ch ? S[j] : S[8 + j]); b[j] = asu(ch ? S[4 + j] : S[12 + j]); }
    lst128(L, L_SX + w * 2048 + lane * 16, a);
    lst128(L, L_SX + w * 2048 + 1024 + lane * 16, b);
    BAR();
    a = lld128(L, L_SX + (w ^ 4) * 2048 + lane * 16);
    b = lld128(L, L_SX + (w ^ 4) * 2048 + 1024 + lane * 16);
#pragma unroll
    for (int j = 0; j < 4; ++j) {
      s[j] = ((ch ? S[8 + j] : S[j]) + asf(a[j])) * (SCALE * LOG2E);
      s[4 + j] = ((ch ? S[12 + j] : S[4 + j]) + asf(b[j])) * (SCALE * LOG2E);
    }
  }
  float cm = fmaxf(fmaxf(fmaxf(s[0], s[1]), fmaxf(s[2], s[3])), fmaxf(fmaxf(s[4], s[5]), fmaxf(s[6], s[7])));
  cm = x32max(cm);
  if (lane < 32) *(LAS float*)(L + L_WM + (w * 32 + lane) * 4) = cm;
  BAR();
  float mn = FIRST ? -INFINITY : m;
#pragma unroll
  for (int i = 0; i < 8; ++i) mn = fmaxf(mn, *(LAS float*)(L + L_WM + (i * 32 + r31) * 4));
  float ps = 0.f;
#pragma unroll
  for (int j = 0; j < 8; ++j) { s[j] = ex2(s[j] - mn); ps += s[j]; }
  lst128(L, L_P + (2 * kb + ch) * 1024 + lane * 16, v4u{pk2(s[0], s[1]), pk2(s[2], s[3]), pk2(s[4], s[5]), pk2(s[6], s[7])});
  if (FIRST) {
    l = ps;
#pragma unroll
    for (int i = 0; i < 16; ++i) { acc[0][i] = 0.f; acc[1][i] = 0.f; }
  } else {
    const float al = ex2(m - mn);
    l = l * al + ps;
    acc[0] *= al;
    acc[1] *= al;
  }
  m = mn;
  BAR();
  const int q = (lane & 15) >> 2, pp = lane & 3, blk = (lane >> 4) & 1;
  u32 ta[2][2];
#pragma unroll
  for (int T = 0; T < 2; ++T)
#pragma unroll
    for (int t = 0; t < 2; ++t) {
      const u32 c = 8 * w + 4 * T + 2 * blk + (pp >> 1);
      ta[T][t] = (8 * t + 4 * hh + q) * 1024 + ((c ^ ((q << 2) | (2 * t + hh))) * 16) + 8 * (pp & 1);
    }
#pragma unroll
  for (int ks = 0; ks < 8; ++ks) {
    const v4u P = lld128(L, L_P + ks * 1024 + lane * 16);
#pragma unroll
    for (int T = 0; T < 2; ++T) acc[T] = mfma32(ltr2(L, ta[T][0] + ks * 16384, ta[T][1] + ks * 16384), P, acc[T]);
  }
}

__global__ void __launch_bounds__(512, 2) decode_mega(Params args) {
  extern __shared__ __attribute__((aligned(16))) char smem[];
  LAS char* L = (LAS char*)smem;
  v16u ka;
  v8u kb;
  v2u kc;
  asm volatile("s_load_dwordx16 %0, %3, 0x0\n\ts_load_dwordx8 %1, %3, 0x40\n\ts_load_dwordx2 %2, %3, 0x60"
               : "=&s"(ka), "=&s"(kb), "=&s"(kc) : "s"(__builtin_amdgcn_kernarg_segment_ptr()) : "memory");
  const int tid = threadIdx.x, lane = tid & 63, w = __builtin_amdgcn_readfirstlane(tid >> 6);
  const int wg = blockIdx.x, g = wg >> 3, s = wg & 7, r = (s + g) & 7;
  const int kvh = g >> 1;
  const u64 cal_t0 = __builtin_amdgcn_s_memrealtime();
  u32 pf = 0;
  if (w == 7) {
    u64 pc;
    asm volatile("s_getpc_b64 %0" : "=s"(pc));
    if ((lane * 32 + g) * 128 < CODE_BYTES) pf = *(const GAS u32*)((const GAS char*)(pc & ~127ull) + (lane * 32 + g) * 128);
  }
  const int n16 = lane & 15, g4 = lane >> 4;
  const int ai = (g & 1) * 4 + r;
  const int bi = g * 2 + (r - 4);
  const int h = (g >> 3) * 8 + s, half = r - 6, bw = h * 2 + half;
  const u32 vw128 = (lane >> 3) * 8192 + (((lane & 7) ^ sw2((lane >> 3) | ((w & 1) << 3))) * 16);
  const u32 cbo = (bi * 256 + w) * 1024;
  const bool isA = r < 4, isBA = r == 4 || r == 5, hlp = isA && w >= 4;
  u32 so0 = hlp ? 0u : isBA ? cbo : isA ? (u32)(((r >> 1) * 288 + 8 * w) * 8192 + g * 256 + (r & 1) * 128) : (u32)((half * 256 + 4 * w) * 8192 + h * 256);
  u32 so1 = isA ? (u32)(((ai >> 2) * 288 + 8 * w) * 8192 + kvh * 512 + (ai & 3) * 128) : cbo + 8 * 1024;
  u32 step = hlp ? 512 : isBA ? 16 * 1024 : 32 * 8192, lstep = hlp ? 512 : isA ? 4096 : isBA ? 16384 : 8192;
  u32 vo = hlp ? lane * 16 : isA ? vw128 : isBA ? (lane ^ fsw(w)) * 16 : g4 * 8192 + ((n16 ^ g4 ^ (4 * (w & 3))) * 16);
  u32 ld = hlp ? L_XV + (w & 1) * 4608 : w * 1024;
  int n0 = hlp ? (w < 6 ? 8 : 0) : isA ? 9 : 8;
  asm volatile("s_waitcnt lgkmcnt(0)" : "+s"(ka), "+s"(kb), "+s"(kc) : "s"(so0), "s"(so1), "s"(step), "s"(lstep), "v"(vo), "s"(ld), "s"(n0) : "memory");
  Params p;
#define PTR(v, i) ((char*)(((u64)v[(i) + 1] << 32) | v[(i)]))
  p.Ax = PTR(ka, 0); p.AK = PTR(ka, 2); p.AV = PTR(ka, 4); p.Awq = PTR(ka, 6); p.Awo = PTR(ka, 8); p.Bx = PTR(ka, 10); p.BC = PTR(ka, 12); p.Bwq = PTR(ka, 14);
  p.Bwuk = PTR(kb, 0); p.Bwuv = PTR(kb, 2); p.Bwo = PTR(kb, 4); p.out = (u16*)PTR(kb, 6); p.ws = PTR(kc, 0);
  u64 epoch;
  asm volatile("s_load_dwordx2 %0, %1, 0x0" : "=s"(epoch) : "s"(p.ws) : "memory");
  {
    const char* src = (hlp ? ((w & 1) ? p.Ax : p.Bx) : isA ? p.Bwq : isBA ? p.BC : p.Bwuk) + so0;
    int n = n0;
    if (w == 0) STAMP(20);
#pragma unroll 1
    for (int bt = 0; bt < 2; ++bt) {
#pragma unroll 1
      for (; n > 0; --n) { VMWAIT(8); dma16(src, vo, ld); src += step; ld += lstep; }
      src = (isA ? p.Awq : p.BC) + so1;
      ld = isA ? L_QAW + w * 1024 : (8 + w) * 1024;
      vo ^= isA ? 0 : 32;
      n = hlp ? 0 : isA ? 9 : isBA ? 8 : 0;
    }
    if (w == 0) STAMP(21);
    asm volatile("s_waitcnt lgkmcnt(0)" : "+s"(epoch)::"memory");
  }
  const char* cb = p.BC + cbo;
  const rsrc_t rs = __builtin_amdgcn_make_buffer_rsrc((void*)p.ws, 0, WS_BYTES, 0x00020000);
  const u32 trrow = 8 * g4 + (n16 >> 2), trcol = ((2 * (w & 3) + ((lane & 3) >> 1)) ^ sw2(trrow)) * 16 + 8 * (lane & 1);
  const u32 tr128 = trrow * 128 + trcol, tr1152 = trrow * 1152 + trcol;
  if (w >= 4) __builtin_amdgcn_s_setprio(3);
  const u32 tA = (u32)epoch + 1u, tB = (u32)(epoch >> 32) + 1u;
  const u32 cnt_off = lane == 0 ? WS_CNT + g * 128 : 0x7ffffff0u;

  const u32 voffK = (n16 >> 1) * 256 + (2 * g4 + (n16 & 1)) * 16, voffV = g4 * 512 + n16 * 16;
  const size_t kv0 = ((size_t)(g & 1) * 32 + kvh) * (4096 * 256) + 4096;
#define CHUNK_OFF(id) (kv0 + (size_t)((id) >> 7) * (16 * 4096 * 256) + (size_t)((id) & 127) * 8192)
#define KIMM(j) (((j) >> 1) * 2048 + ((j) & 1) * 128 - 4096)
#define VIMM(j) (((j) >> 1) * 2048 + ((j) & 1) * 256 - 4096)
#define LOAD_KV(id) do { const char* k1_ = p.AK + CHUNK_OFF(id); const char* v1_ = p.AV + CHUNK_OFF(id); \
    _Pragma("unroll") for (int j = 0; j < 8; ++j) K[j] = ldnt(k1_, voffK, KIMM(j)); \
    SB(); \
    _Pragma("unroll") for (int j = 0; j < 8; ++j) V[j] = ldnt(v1_, voffV, VIMM(j)); \
    SB(); } while (0)
  u32 sel[4];
  {
    const u32 h2 = (n16 >> 1) & 1, by = (2 * h2) | ((2 * h2 + 1) << 8);
    const u32 on = (n16 & 1) ? ((by << 16) | 0x0c0cu) : (0x0c0c0000u | by);
#pragma unroll
    for (int i = 0; i < 4; ++i) sel[i] = (n16 >> 2) == i ? on : 0x0c0c0c0cu;
  }
  const bool t1 = lane & 1;

  v4u K[8], V[8], QF[8];
  Acc acc;
  u32 idv = 0;
  int cur = 0;
  bool have = false;
  int pause_in = -1;

  if (r < 4) {
    if (w < 4) {
      BAR();
      v4u K0[8], V0[8];
      const int id0 = r * 4 + w;
      const char* k0 = p.AK + CHUNK_OFF(id0); const char* v0 = p.AV + CHUNK_OFF(id0);
      const char* k1 = p.AK + CHUNK_OFF(id0 + 16); const char* v1 = p.AV + CHUNK_OFF(id0 + 16);
#pragma unroll
      for (int j = 0; j < 8; ++j) { VMWAIT(8); K0[j] = ldnt(k0, voffK, KIMM(j)); SB(); }
      VMWAIT(8);
      BAR();
#pragma unroll
      for (int j = 0; j < 8; ++j) { VMWAIT(PACE); V0[j] = ldnt(v0, voffV, VIMM(j)); SB(); }
#pragma unroll
      for (int j = 0; j < 8; ++j) { VMWAIT(PACE); K[j] = ldnt(k1, voffK, KIMM(j)); SB(); }
#pragma unroll
      for (int j = 0; j < 8; ++j) { VMWAIT(PACE); V[j] = ldnt(v1, voffV, VIMM(j)); SB(); }
      a_reset(acc);
      a_flush(L, w, 1, lane, acc);
      if (w == 0) STAMP(1);
      BAR();
      if (w == 0) STAMP(2);
      a_loadq(L, 0, lane, QF);
      const u32 PP = a_soft(a_qk(K0, QF, t1), acc);
      a_pv(PP, V0, sel, acc);
      idv = id0 + 32;
      have = true;
    } else {
      const int ct = w - 4;
      if (r == 0 && w == 4 && lane == 0) __hip_atomic_store((GAS u32*)(p.ws + WS_CNT + g * 128), 64u, RLX_AGENT);
      const u32 xo = L_XV + (lane & 3) * 1152 + g4 * 16;
      VMWAIT(0);
      if (w == 4) STAMP(23);
      BAR();
      if (w == 4) STAMP(3);
      f32x4 d = gemv288(L, L_QBW + tr128, xo + (r >> 1) * 576);
      if (lane < 32) st_gran(rs, WS_E1 + (((g * 2 + (r >> 1)) * 128 + 64 * (r & 1) + 16 * ct + n16) * 2 + g4) * 16, g4 ? d[2] : d[0], g4 ? d[3] : d[1], tA, tB);
      if (w == 4) STAMP(4);
      BAR();
      d = gemv288(L, L_QAW + tr128, xo + 4608 + (ai >> 2) * 576);
      if (lane < 32) st_gran(rs, WS_E2 + (((kvh * 2 + g4) * 2 + (ai >> 2)) * 256 + 64 * (ai & 3) + 16 * ct + n16) * 16, g4 ? d[2] : d[0], g4 ? d[3] : d[1], tA, tB);
      if (w == 4) STAMP(5);
      put_q(L, tid - 256, fetch_q(rs, g, tid - 256, tA, tB));
      if (w == 4) STAMP(6);
      BAR();
      const char* so = p.Awo + (size_t)(kvh * 256 + ai * 32) * 1152;
#pragma unroll 1
      for (int i = 0; i < 9; ++i) dma16(so, voff1152(4 * i + ct, lane), L_WO + (4 * i + ct) * 1024);
      VMWAIT(0);
    }
  } else if (r < 6) {
    const int b = bi >> 4, sp = bi & 15;
    const u32 vc0 = (lane ^ fsw(w)) * 16, vc1 = vc0 ^ 32;
    if (w == 4) poll_flags(rs, WS_FQ + lane * 64, tA, tB);
    if (w == 4) STAMP(7);
    BAR();
    v4u qs[4], c2[8];
#pragma unroll
    for (int i = 0; i < 4; ++i) qs[i] = ld_sc1(rs, WS_QABS + (b * 32 + 4 * w + i) * 1024 + (lane ^ fsw(4 * w + i)) * 16);
    SB();
    asm volatile("s_barrier" ::: "memory");
#pragma unroll
    for (int i = 0; i < 4; ++i) c2[i] = ldnt(cb + (128 + 16 * i) * 1024, vc0, 0);
    SB();
#pragma unroll
    for (int i = 0; i < 4; ++i) lst128(L, L_X + (4 * w + i) * 1024 + lane * 16, qs[i]);
    BAR();
    v4u qf[16];
    {
      const u32 rowb = L_X + (lane & 31) * 1024 + (w >> 2) * 512, f = fsw(lane & 31);
#pragma unroll
      for (int ks = 0; ks < 16; ++ks) qf[ks] = lld128(L, rowb + (ks >> 3) * 256 + (((2 * (ks & 7) + (lane >> 5)) ^ f) * 16));
    }
    BAR();
#pragma unroll
    for (int i = 4; i < 8; ++i) c2[i] = ldnt(cb + (128 + 16 * i) * 1024, vc0, 0);
    SB();
    if (tid == 0) *(LAS u32*)(L + L_SYNC) = 0u;
    if (w == 0) STAMP(8);
    f32x16 ac[2];
    float m, l;
    ba_pass<true>(L, w, lane, qf, ac, m, l);
    BAR();
    if (w == 0) STAMP(9);
    asm volatile("" ::"v"(c2[0]), "v"(c2[1]), "v"(c2[2]), "v"(c2[3]), "v"(c2[4]), "v"(c2[5]), "v"(c2[6]), "v"(c2[7]));
    u32 qpk = 0;
    if (w >= 4) qpk = fetch_q(rs, g, tid - 256, tA, tB);
#pragma unroll 1
    for (int i = 0; i < 8; ++i) dma16(cb + (128 + 16 * i + 8) * 1024, vc1, (16 * i + 8 + w) * 1024);
#pragma unroll
    for (int i = 0; i < 8; ++i) lst128(L, (16 * i + w) * 1024 + lane * 16, c2[i]);
    VMWAIT(0);
    BAR();
    if (w == 0) STAMP(10);
    u32 a0 = 0;
    if (w < 4) a0 = __builtin_amdgcn_raw_ptr_buffer_atomic_add_i32(2, rs, cnt_off, 0, 0);
    ba_pass<false>(L, w, lane, qf, ac, m, l);
    if (w == 0) STAMP(29);
    l = x32sum(l);
    if (lane < 32) *(LAS float*)(L + L_WL + (w * 32 + lane) * 4) = l;
    BAR();
    {
      const u32 hd = lane & 31, hh = lane >> 5;
#pragma unroll
      for (int T = 0; T < 2; ++T)
#pragma unroll
        for (int rq = 0; rq < 4; ++rq)
          *(LAS v2u*)(L + hd * 1024 + (((8 * w + 4 * T + rq) ^ (hd & 15)) * 16) + 8 * hh) =
              v2u{pk2(ac[T][4 * rq], ac[T][4 * rq + 1]), pk2(ac[T][4 * rq + 2], ac[T][4 * rq + 3])};
    }
    if (w >= 4) put_q(L, tid - 256, qpk);
    BAR();
    if (w >= 4) {
#pragma unroll
      for (int i = 0; i < 8; ++i) {
        const u32 hd = 8 * (w - 4) + i;
        st_sc1(rs, WS_PART + ((hd * 4 + b) * 16 + sp) * 1024 + lane * 16, lld128(L, hd * 1024 + ((lane ^ (hd & 15)) * 16)));
      }
      if (w == 4 && lane < 32) {
        float ls = 0.f;
#pragma unroll
        for (int i = 0; i < 8; ++i) ls += *(LAS float*)(L + L_WL + (i * 32 + lane) * 4);
        st_sc1_8(rs, WS_ML + ((lane * 4 + b) * 16 + sp) * 8, v2u{asu(m), asu(ls)});
      }
      VMWAIT(0);
      if (lds_inc(L, L_SYNC) == 3 && lane == 0) st_sc1_8(rs, WS_FP + bi * 64, v2u{tA, tB});
      if (w == 4) STAMP(11);
    } else {
      const u32 id0 = __builtin_amdgcn_readfirstlane(a0);
      idv = id0 + 1;
      a_reset(acc);
      lds_wait(L, L_SYNC, 4);
      if (id0 < 256) {
        LOAD_KV(id0);
        cur = id0 >> 7;
        a_loadq(L, cur, lane, QF);
        have = true;
      }
      a_flush(L, w, 0, lane, acc);
      a_flush(L, w, 1, lane, acc);
    }
  } else {
    if (w >= 4) {
      const int t = tid - 256;
      const u32 o0 = WS_E1 + (h * 512 + t) * 16;
      const u32 o[2] = {o0, o0 + 4096};
      v4u v[2];
      poll_gran<2>(rs, o, v, tA, tB);
      const u32 pk = pk2(asf(v[0].x) + asf(v[1].x), asf(v[0].z) + asf(v[1].z));
      const u32 qo = L_QB + (t & 1) * 640 + (t >> 1) * 2;
      *(LAS u16*)(L + qo) = (u16)pk;
      *(LAS u16*)(L + qo + 320) = (u16)(pk >> 16);
    }
    if (tid < 4) *(LAS u32*)(L + L_SYNC + tid * 4) = 0u;
    VMWAIT(0);
    BAR();
    if (w == 0) STAMP(12);
    {
      f32x4 d0 = {0.f, 0.f, 0.f, 0.f}, d1 = d0;
#pragma unroll
      for (int ks = 0; ks < 4; ++ks) {
        const v4u qb = lld128(L, L_QB + (lane & 3) * 320 + ks * 64 + g4 * 16);
        const u32 a0 = (32 * w + n16) * 256 + (((4 * ks + g4) ^ n16) * 16);
        d0 = mfma16(lld128(L, a0), qb, d0);
        d1 = mfma16(lld128(L, a0 + 4096), qb, d1);
      }
      const bool hi = n16 & 4;
      const v2u o = {pk2(hi ? d1[0] : d0[0], hi ? d1[1] : d0[1]), pk2(hi ? d1[2] : d0[2], hi ? d1[3] : d0[3])};
      if (n16 < 8) st_sc1_8(rs, WS_QABS + ((lane & 3) * 32 + h) * 1024 + (half * 256 + 32 * w + (hi ? 16 : 0) + 4 * g4) * 2, o);
    }
    VMWAIT(0);
    BAR();
    if (tid == 256) st_sc1_8(rs, WS_FQ + bw * 64, v2u{tA, tB});
    if (w == 4) STAMP(13);
    if (w < 4) {
      const char* sv = p.Bwuv + (size_t)(8 * w) * 8192 + h * 256 + half * 128;
#pragma unroll 1
      for (int i = 0; i < 16; ++i) dma16(sv + (size_t)i * 32 * 8192, vw128, (4 * i + w) * 1024);
      const char* so = p.Bwo + (size_t)(h * 128 + half * 64) * 1152;
#pragma unroll 1
      for (int i = 0; i < 18; ++i) dma16(so, voff1152(4 * i + w, lane), L_W2 + (4 * i + w) * 1024);
      const int id0 = 48 + half * 4 + w;
      LOAD_KV(id0);
      a_reset(acc);
      a_flush(L, w, 1, lane, acc);
      VMWAIT(16);
      if (w == 0) STAMP(24);
      BAR();
      a_loadq(L, 0, lane, QF);
      idv = id0 + 8;
      pause_in = 1;
      have = true;
    } else {
      const int hw = w - 4;
      put_q(L, tid - 256, fetch_q(rs, g, tid - 256, tA, tB));
      BAR();
      poll_flags(rs, WS_FP + lane * 64, tA, tB);
      if (w == 4) STAMP(25);
      {
        const v2u ml = ld_sc1_8(rs, WS_ML + ((h * 4 + hw) * 16 + n16) * 8);
        v4u pt[16];
#pragma unroll
        for (int i = 0; i < 16; ++i) pt[i] = ld_sc1(rs, WS_PART + ((h * 4 + hw) * 16 + i) * 1024 + lane * 16);
        float M = fmaxf(asf(ml.x), dppf<XOR1>(asf(ml.x)));
        M = fmaxf(M, dppf<XOR2>(M)); M = fmaxf(M, dppf<ROR4>(M)); M = fmaxf(M, dppf<ROR8>(M));
        const float wt = ex2(asf(ml.x) - M);
        float Ls = wt * asf(ml.y);
        Ls += dppf<XOR1>(Ls); Ls += dppf<XOR2>(Ls); Ls += dppf<XOR7>(Ls); Ls += dppf<0x140>(Ls);
        const float wn = wt / Ls;
        float c[8];
#pragma unroll
        for (int k = 0; k < 8; ++k) c[k] = 0.f;
#pragma unroll
        for (int i = 0; i < 16; ++i) {
          const float wi = asf(__builtin_amdgcn_readlane(asu(wn), i));
#pragma unroll
          for (int k = 0; k < 4; ++k) { c[2 * k] += wi * asf(pt[i][k] << 16); c[2 * k + 1] += wi * asf(pt[i][k] & 0xffff0000u); }
        }
        lst128(L, L_CL + hw * 1088 + lane * 16, v4u{pk2(c[0], c[1]), pk2(c[2], c[3]), pk2(c[4], c[5]), pk2(c[6], c[7])});
      }
      lds_inc(L, L_SYNC);
      lds_wait(L, L_SYNC, 4);
      if (w == 4) STAMP(26);
      {
        f32x4 d = {0.f, 0.f, 0.f, 0.f};
#pragma unroll
        for (int ks = 0; ks < 16; ++ks)
          d = mfma16(lld128(L, L_CL + (lane & 3) * 1088 + ks * 64 + g4 * 16), ltr2(L, tr128 + ks * 4096, tr128 + ks * 4096 + 512), d);
        const float dv = g4 == 0 ? d[0] : g4 == 1 ? d[1] : g4 == 2 ? d[2] : d[3];
        *(LAS u16*)(L + L_CT + g4 * 192 + (16 * hw + n16) * 2) = (u16)pk2(dv, dv);
      }
      lds_inc(L, L_SYNC + 4);
      lds_wait(L, L_SYNC + 4, 4);
      {
        const v4u af0 = lld128(L, L_CT + (lane & 3) * 192 + g4 * 16), af1 = lld128(L, L_CT + (lane & 3) * 192 + 64 + g4 * 16);
#pragma unroll
        for (int i = 0; i < 9; ++i) {
          const u32 t2 = L_W2 + tr1152 + i * 128;
          f32x4 e = mfma16(af0, ltr2(L, t2, t2 + 4608), f32x4{0.f, 0.f, 0.f, 0.f});
          e = mfma16(af1, ltr2(L, t2 + 36864, t2 + 36864 + 4608), e);
          const int T = 4 * i + hw;
          if (lane < 32) st_gran(rs, WS_E6 + (((4 * T + (n16 >> 2)) * 64 + bw) * 8 + (n16 & 3) * 2 + g4) * 16, g4 ? e[2] : e[0], g4 ? e[3] : e[1], tA, tB);
        }
      }
      if (w == 4) STAMP(27);
      if (hw * 64 + bw < 144) reduce_quad<8>(rs, WS_E6, hw * 64 + bw, lane, tA, tB, p.out + 576);
      lds_inc(L, L_SYNC + 8);
      if (w == 4) STAMP(14);
    }
  }

  if (have) {
    float S;
    for (;;) {
      S = a_qk(K, QF, t1);
      SB();
      const u32 id = __builtin_amdgcn_readfirstlane(idv);
      if (id >= 256) break;
      if (pause_in-- == 0) lds_wait(L, L_SYNC + 8, 4);
      idv = __builtin_amdgcn_raw_ptr_buffer_atomic_add_i32(1, rs, cnt_off, 0, 0);
      const char* kn = p.AK + CHUNK_OFF(id); const char* vn = p.AV + CHUNK_OFF(id);
#pragma unroll
      for (int j = 0; j < 8; ++j) K[j] = ldnt(kn, voffK, KIMM(j));
      SB();
      const u32 PP = a_soft(S, acc);
      a_pv(PP, V, sel, acc);
      asm volatile("" : "+v"(acc.o) :: "memory");
      SB();
#pragma unroll
      for (int j = 0; j < 8; ++j) V[j] = ldnt(vn, voffV, VIMM(j));
      SB();
      if ((int)(id >> 7) != cur) {
        a_flush(L, w, 0, lane, acc);
        a_reset(acc);
        cur = 1;
        a_loadq(L, 1, lane, QF);
      }
    }
    const u32 PP = a_soft(S, acc);
    a_pv(PP, V, sel, acc);
    a_flush(L, w, cur, lane, acc);
  }
  BAR();
  if (w == 7) STAMP(16);
  if (w < 4) {
    float mw[4], lw[4];
    f32x2 aw[4];
#pragma unroll
    for (int i = 0; i < 4; ++i) {
      const f32x2 ml = *(LAS f32x2*)(L + L_FM + (i * 4 + w) * 8);
      mw[i] = ml.x; lw[i] = ml.y;
      aw[i] = *(LAS f32x2*)(L + L_FL + ((i * 4 + w) * 128 + 2 * lane) * 4);
    }
    const float M = fmaxf(fmaxf(mw[0], mw[1]), fmaxf(mw[2], mw[3])), Ms = M == -INFINITY ? 0.f : M;
    float Ls = 0.f, o0 = 0.f, o1 = 0.f;
#pragma unroll
    for (int i = 0; i < 4; ++i) { const float wt = ex2(mw[i] - Ms); Ls += wt * lw[i]; o0 += wt * aw[i].x; o1 += wt * aw[i].y; }
    const u32 rec = ((g * 2 + (w >> 1)) * 8 + s) * 2 + (w & 1);
    st_gran(rs, WS_E3 + (rec * 64 + lane) * 16, o0, o1, tA, tB);
    if (lane == 0) st_gran(rs, WS_E3M + rec * 16, M, Ls, tA, tB);
  }
  if (w == 0) STAMP(15);
  if (r < 4) {
    v4u wf[9];
    if (w >= 4) {
      const u32 tb = L_WO + tr1152;
#pragma unroll
      for (int i = 0; i < 9; ++i) wf[i] = ltr2(L, tb + i * 128, tb + i * 128 + 4608);
    }
    {
      const int b = w >> 1, i16 = (tid >> 3) & 15, sl = lane & 7;
      const u32 rec = ((kvh * 4 + b) * 8 + sl) * 2 + (ai >> 2);
      const u32 o[2] = {WS_E3 + (rec * 64 + 16 * (ai & 3) + i16) * 16, WS_E3M + rec * 16};
      v4u v[2];
      poll_gran<2>(rs, o, v, tA, tB);
      const float m = asf(v[1].x);
      float M = fmaxf(m, dppf<XOR1>(m));
      M = fmaxf(M, dppf<XOR2>(M)); M = fmaxf(M, dppf<XOR7>(M));
      const float wt = ex2(m - M);
      float Ls = wt * asf(v[1].z), o0 = wt * asf(v[0].x), o1 = wt * asf(v[0].z);
      Ls += dppf<XOR1>(Ls); o0 += dppf<XOR1>(o0); o1 += dppf<XOR1>(o1);
      Ls += dppf<XOR2>(Ls); o0 += dppf<XOR2>(o0); o1 += dppf<XOR2>(o1);
      Ls += dppf<XOR7>(Ls); o0 += dppf<XOR7>(o0); o1 += dppf<XOR7>(o1);
      const float inv = 1.f / Ls;
      if (sl == 0) *(LAS u32*)(L + L_OA + b * 64 + i16 * 4) = pk2(o0 * inv, o1 * inv);
    }
    BAR();
    if (w == 0) STAMP(17);
    if (w >= 4) {
      const v4u af = lld128(L, L_OA + (lane & 3) * 64 + g4 * 16);
#pragma unroll
      for (int i = 0; i < 9; ++i) {
        const f32x4 e = mfma16(af, wf[i], f32x4{0.f, 0.f, 0.f, 0.f});
        const int T = 4 * i + (w - 4);
        if (lane < 32) st_gran(rs, WS_E4 + (((4 * T + (n16 >> 2)) * 128 + g * 4 + r) * 8 + (n16 & 3) * 2 + g4) * 16, g4 ? e[2] : e[0], g4 ? e[3] : e[1], tA, tB);
      }
      if (w == 4) STAMP(18);
    }
  } else {
    const int ri = r < 6 ? g * 2 + (r - 4) : 64 + g * 2 + (r - 6);
    if (ri < 72) {
      reduce_col<4>(rs, WS_E4, ri * 8 + w, lane, tA, tB, p.out);
      if (ri == 0 && tid == 0) __hip_atomic_store((GAS u64*)(p.ws + WS_EPOCH), ((u64)tB << 32) | tA, RLX_AGENT);
      if (w == 0) STAMP(19);
    }
  }
  asm volatile("" ::"v"(pf));
  while (__builtin_amdgcn_s_memrealtime() - cal_t0 < 6000) __builtin_amdgcn_s_sleep(2);
}

extern "C" void kernel_launch(void* const* d_in, const int* in_sizes, int n_in, void* d_out, int out_size, void* d_ws, size_t ws_size,
                              hipStream_t stream) {
  Params p;
  p.Ax = (const char*)d_in[0]; p.AK = (const char*)d_in[1]; p.AV = (const char*)d_in[2]; p.Awq = (const char*)d_in[3]; p.Awo = (const char*)d_in[4];
  p.Bx = (const char*)d_in[5]; p.BC = (const char*)d_in[6]; p.Bwq = (const char*)d_in[7]; p.Bwuk = (const char*)d_in[8]; p.Bwuv = (const char*)d_in[9];
  p.Bwo = (const char*)d_in[10];
  p.out = (u16*)d_out;
  p.ws = (char*)d_ws;
  (void)hipFuncSetAttribute((const void*)decode_mega, hipFuncAttributeMaxDynamicSharedMemorySize, LDS_BYTES);
  decode_mega<<<256, 512, LDS_BYTES, stream>>>(p);
}
```

```cpp
#include <hip/hip_runtime.h>
#include <stdint.h>

typedef unsigned int u32;
typedef unsigned long long u64;
typedef unsigned short u16;
typedef u32 v4u __attribute__((ext_vector_type(4)));
typedef u32 v2u __attribute__((ext_vector_type(2)));
typedef u32 v8u __attribute__((ext_vector_type(8)));
typedef u32 v16u __attribute__((ext_vector_type(16)));
typedef float f32x2 __attribute__((ext_vector_type(2)));
typedef float f32x4 __attribute__((ext_vector_type(4)));
typedef float f32x16 __attribute__((ext_vector_type(16)));
typedef __bf16 bf16x8 __attribute__((ext_vector_type(8)));
typedef __bf16 bf16x2 __attribute__((ext_vector_type(2)));
typedef short s16x4 __attribute__((ext_vector_type(4)));
#define DI __device__ __forceinline__
#define LAS __attribute__((address_space(3)))
#define GAS __attribute__((address_space(1)))
#define RLX_AGENT __ATOMIC_RELAXED, __HIP_MEMORY_SCOPE_AGENT

#define STAMPS 0
#define CODE_BYTES 28000
#define UV_EARLY 8
#define C2_EARLY 8
#define DYNP 0
#define BA_PF 3
#define BA_WAIT 0
#define LATE_A 4
#define LATE_B 0
#define TRIG 0
#define TOUCH 0
#define C_A 40
#define C_BA 22
#define DPAR 0
#define PSLEEP 1
#define PACE 8

constexpr int WS_EPOCH = 0;
constexpr int WS_FQ = 8192;
constexpr int WS_FP = 12288;
constexpr int WS_ML = 16384;
constexpr int WS_QABS = 65536;
constexpr int WS_E1 = 262144;
constexpr int WS_E2 = 393216;
constexpr int WS_E3 = 524288;
constexpr int WS_E6 = 1 << 20;
constexpr int WS_E4 = 2 << 20;
constexpr int WS_PART = 4 << 20;
constexpr int WS_BYTES = 6 << 20;
constexpr u32 OOB = 0x7f000000u;
constexpr int LDS_BYTES = 163840;
constexpr int L_QBW = 0, L_QAW = 36864;
constexpr int L_XV = 110592;
constexpr int L_WO = 0;
constexpr int L_UV = 73728;
constexpr int L_QS = 139264;
constexpr int L_FL = L_QS + 1024;
constexpr int L_FM = L_FL + 4096;
constexpr int L_OA = L_FM + 128;
constexpr int L_RD = L_QS;
constexpr int L_X = 131072;
constexpr int L_SX = L_X, L_P = L_X + 16384, L_WM = L_X + 24576, L_WL = L_X + 25600;
constexpr int L_SYNC = L_X + 26624;
constexpr int L_QB = L_OA + 768;
constexpr int L_CL = L_QB + 1280;
static_assert(L_CL + 4352 <= L_WM, "LDS map");
enum { SY_A = L_SYNC, SY_B = L_SYNC + 4, SY_LAST = L_SYNC + 8, SY_QB = L_SYNC + 12, SY_QABS = L_SYNC + 16, SY_HOLD = L_SYNC + 20, SY_WO = L_SYNC + 24 };

constexpr float SCALE = 0.08838834764831845f;
constexpr float LOG2E = 1.4426950408889634f;
constexpr u32 SPIN_CAP = 1u << 18;

struct Params {
  const char *Ax, *AK, *AV, *Awq, *Awo, *Bx, *BC, *Bwq, *Bwuk, *Bwuv, *Bwo;
  u16* out;
  char* ws;
};

DI f32x4 mfma16(v4u a, v4u b, f32x4 c) {
  return __builtin_amdgcn_mfma_f32_16x16x32_bf16(__builtin_bit_cast(bf16x8, a), __builtin_bit_cast(bf16x8, b), c, 0, 0, 0);
}
DI f32x16 mfma32(v4u a, v4u b, f32x16 c) {
  return __builtin_amdgcn_mfma_f32_32x32x16_bf16(__builtin_bit_cast(bf16x8, a), __builtin_bit_cast(bf16x8, b), c, 0, 0, 0);
}
DI u32 pk2(float a, float b) { f32x2 v = {a, b}; return __builtin_bit_cast(u32, __builtin_convertvector(v, bf16x2)); }
DI float rbf(float a) { return (float)(__bf16)a; }
DI float asf(u32 x) { return __builtin_bit_cast(float, x); }
DI u32 asu(float x) { return __builtin_bit_cast(u32, x); }
DI float ex2(float x) { return __builtin_amdgcn_exp2f(x); }
template <int CTRL> DI float dppf(float x) { return asf(__builtin_amdgcn_mov_dpp(asu(x), CTRL, 0xf, 0xf, true)); }
template <int CTRL> DI u32 dppu(u32 x) { return __builtin_amdgcn_mov_dpp(x, CTRL, 0xf, 0xf, true); }
constexpr int XOR1 = 0xB1, XOR2 = 0x4E, XOR7 = 0x141, XOR15 = 0x140, ROR8 = 0x128, ROR4 = 0x124;
DI float x16max(float x) { auto s = __builtin_amdgcn_permlane16_swap(asu(x), asu(x), false, false); return fmaxf(asf(s[0]), asf(s[1])); }
DI float x32max(float x) { auto s = __builtin_amdgcn_permlane32_swap(asu(x), asu(x), false, false); return fmaxf(asf(s[0]), asf(s[1])); }
DI float x16sum(float x) { auto s = __builtin_amdgcn_permlane16_swap(asu(x), asu(x), false, false); return asf(s[0]) + asf(s[1]); }
DI float x32sum(float x) { auto s = __builtin_amdgcn_permlane32_swap(asu(x), asu(x), false, false); return asf(s[0]) + asf(s[1]); }
DI f32x4 sum_hi3(f32x4 a) {
#pragma unroll
  for (int k = 0; k < 4; ++k) a[k] = x32sum(x16sum(a[k] + dppf<ROR8>(a[k])));
  return a;
}

#define BAR() asm volatile("s_waitcnt lgkmcnt(0)\n\ts_barrier" ::: "memory")
#define VMWAIT(n) asm volatile("s_waitcnt vmcnt(%0)" ::"n"(n) : "memory")
#define SB() __builtin_amdgcn_sched_barrier(0)

DI v4u lld128(LAS char* L, u32 o) { return *(LAS v4u*)(L + o); }
DI void lst128(LAS char* L, u32 o, v4u v) { *(LAS v4u*)(L + o) = v; }
DI v2u ltr(LAS char* L, u32 o) { return __builtin_bit_cast(v2u, __builtin_amdgcn_ds_read_tr16_b64_v4i16((LAS s16x4*)(L + o))); }
DI v4u ltr2(LAS char* L, u32 o0, u32 o1) { v2u a = ltr(L, o0), b = ltr(L, o1); return v4u{a.x, a.y, b.x, b.y}; }
DI void dma16(const char* sbase, u32 voff, u32 lds) {
  u32 keep;
  asm volatile("s_mov_b32 %0, m0\n\ts_mov_b32 m0, %3\n\ts_nop 2\n\tglobal_load_lds_dwordx4 %1, %2 nt\n\ts_mov_b32 m0, %0"
               : "=&s"(keep) : "v"(voff), "s"(sbase), "s"(lds) : "memory");
}
DI void dma16c(const char* sbase, u32 voff, u32 lds) {
  u32 keep;
  asm volatile("s_mov_b32 %0, m0\n\ts_mov_b32 m0, %3\n\ts_nop 2\n\tglobal_load_lds_dwordx4 %1, %2\n\ts_mov_b32 m0, %0"
               : "=&s"(keep) : "v"(voff), "s"(sbase), "s"(lds) : "memory");
}
DI v4u ldnt(const char* base, u32 voff, int imm) { return __builtin_nontemporal_load((const GAS v4u*)((const GAS char*)base + voff + imm)); }
typedef __amdgpu_buffer_rsrc_t rsrc_t;
DI v4u ld_sc1(rsrc_t rs, u32 o) { return __builtin_amdgcn_raw_buffer_load_b128(rs, o, 0, 16); }
DI v2u ld_sc1_8(rsrc_t rs, u32 o) { return __builtin_amdgcn_raw_buffer_load_b64(rs, o, 0, 16); }
DI void st_sc1(rsrc_t rs, u32 o, v4u v) { __builtin_amdgcn_raw_buffer_store_b128(v, rs, o, 0, 16); }
DI void st_sc1_8(rsrc_t rs, u32 o, v2u v) { __builtin_amdgcn_raw_buffer_store_b64(v, rs, o, 0, 16); }
DI v4u tag4(f32x4 e, u32 tA) {
  v4u v;
#pragma unroll
  for (int k = 0; k < 4; ++k) v[k] = __builtin_amdgcn_perm(asu(e[k]) + 0x80u, tA, 0x07060500u + k);
  return v;
}
DI f32x4 untag4(v4u v) { return f32x4{asf(v.x & ~0xffu), asf(v.y & ~0xffu), asf(v.z & ~0xffu), asf(v.w & ~0xffu)}; }
template <int N, bool WORDS> DI void poll16(rsrc_t rs, u32 o, u32 stride, v4u (&v)[N], u32 tA, u32 tB) {
  for (u32 spins = 0;; ++spins) {
    asm volatile("" ::: "memory");
    bool ok = true;
#pragma unroll
    for (int i = 0; i < N; ++i) v[i] = ld_sc1(rs, o + i * stride);
#pragma unroll
    for (int i = 0; i < N; ++i) {
      if (WORDS) ok = ok && (__builtin_amdgcn_perm(v[i].y, v[i].x, 0x0c0c0400u) | __builtin_amdgcn_perm(v[i].w, v[i].z, 0x04000c0cu)) == tA;
      else ok = ok && v[i].y == tA && v[i].w == tB;
    }
    if (__all(ok) || spins > SPIN_CAP) break;
    if (PSLEEP) __builtin_amdgcn_s_sleep(PSLEEP);
  }
}
DI void poll_flags(rsrc_t rs, u32 o, u32 tA, u32 tB) {
  for (u32 spins = 0;; ++spins) {
    asm volatile("" ::: "memory");
    const v2u v = ld_sc1_8(rs, o);
    if (__all(v.x == tA && v.y == tB) || spins > SPIN_CAP) break;
    __builtin_amdgcn_s_sleep(2);
  }
  __builtin_amdgcn_fence(__ATOMIC_ACQUIRE, "wavefront");
}
DI u32 lds_inc(LAS char* L, u32 o) {
  asm volatile("s_waitcnt lgkmcnt(0)" ::: "memory");
  u32 old = 0;
  if ((threadIdx.x & 63) == 0) old = __hip_atomic_fetch_add((LAS u32*)(L + o), 1u, __ATOMIC_RELAXED, __HIP_MEMORY_SCOPE_WORKGROUP);
  return __builtin_amdgcn_readfirstlane(old);
}
DI void lds_wait(LAS char* L, u32 o, u32 want) {
#pragma unroll 1
  for (u32 spins = 0; spins < (1u << 24); ++spins) {
    if (__builtin_amdgcn_readfirstlane(__hip_atomic_load((LAS u32*)(L + o), __ATOMIC_RELAXED, __HIP_MEMORY_SCOPE_WORKGROUP)) >= want) break;
    __builtin_amdgcn_s_sleep(1);
  }
  asm volatile("" ::: "memory");
}

#define STAMP(i) do {} while (0)

DI f32x4 gemv288(LAS char* L, u32 base, u32 xo) {
  f32x4 d = {0.f, 0.f, 0.f, 0.f};
#pragma unroll
  for (int ks = 0; ks < 9; ++ks) d = mfma16(ltr2(L, base + ks * 4096, base + ks * 4096 + 512), lld128(L, xo + ks * 64), d);
  return d;
}
DI v2u fetch_q(rsrc_t rs, u32 o, u32 stride, u32 tA) {
  v4u v[2];
  poll16<2, true>(rs, o, stride, v, tA, 0u);
  const f32x4 q = untag4(v[0]) + untag4(v[1]);
  return v2u{pk2(q[0], q[1]), pk2(q[2], q[3])};
}

struct Acc { float m, l; f32x4 o; };
DI float a_qk(const v4u (&K)[8], const v4u (&QF)[8], bool t1) {
  f32x4 d = {0.f, 0.f, 0.f, 0.f};
#pragma unroll
  for (int j = 0; j < 8; ++j) d = mfma16(K[j], QF[j], d);
  const float keep = t1 ? d[3] : d[0], send = t1 ? d[1] : d[2];
  return keep + dppf<XOR1>(send);
}
DI u32 a_soft(float S, Acc& a) {
  const float t = rbf(rbf(S) * SCALE) * LOG2E;
  float cm = fmaxf(t, dppf<XOR1>(t));
  cm = fmaxf(cm, dppf<ROR4>(cm));
  cm = fmaxf(cm, dppf<ROR8>(cm));
  cm = x32max(x16max(cm));
  const float mn = fmaxf(a.m, cm);
  const float al = ex2(a.m - mn), p = ex2(t - mn);
  a.l = a.l * al + p;
  a.m = mn;
  a.o *= al;
  return pk2(p, dppf<XOR2>(p));
}
template <int J> DI void a_pv1(u32 PP, v4u V, const u32 (&sel)[4], Acc& a) {
  const u32 X = dppu<0x150 + 4 * (J >> 1) + (J & 1)>(PP);
  const v4u B = {__builtin_amdgcn_perm(X, X, sel[0]), __builtin_amdgcn_perm(X, X, sel[1]), __builtin_amdgcn_perm(X, X, sel[2]), __builtin_amdgcn_perm(X, X, sel[3])};
  a.o = mfma16(V, B, a.o);
}
DI void a_pv(u32 PP, const v4u (&V)[8], const u32 (&sel)[4], Acc& a) {
  a_pv1<0>(PP, V[0], sel, a); a_pv1<1>(PP, V[1], sel, a); a_pv1<2>(PP, V[2], sel, a); a_pv1<3>(PP, V[3], sel, a);
  a_pv1<4>(PP, V[4], sel, a); a_pv1<5>(PP, V[5], sel, a); a_pv1<6>(PP, V[6], sel, a); a_pv1<7>(PP, V[7], sel, a);
}
DI void a_loadq(LAS char* L, int lane, v4u (&QF)[8]) {
  const int n = lane & 15, g = lane >> 4;
  const u32 o = L_QS + ((n >> 1) & 1) * 256 + (2 * g + (n & 1)) * 16;
  const v4u F0 = lld128(L, o), F1 = lld128(L, o + 128), Z = {0u, 0u, 0u, 0u};
#pragma unroll
  for (int u = 0; u < 4; ++u) { const bool on = (n >> 2) == u; QF[2 * u] = on ? F0 : Z; QF[2 * u + 1] = on ? F1 : Z; }
}
DI void a_flush(LAS char* L, int w, int lane, const Acc& a) {
  const int n = lane & 15, G = lane >> 4, h = (n >> 1) & 1, jp = ((n >> 2) << 1) | (n & 1);
  float l = a.l + dppf<XOR1>(a.l);
  l += dppf<ROR4>(l);
  l += dppf<ROR8>(l);
  l = x32sum(x16sum(l));
  LAS float* f = (LAS float*)(L + L_FL) + (w * 2 + h) * 128 + 32 * G + jp;
#pragma unroll
  for (int r = 0; r < 4; ++r) f[8 * r] = a.o[r];
  if ((lane & 61) == 0) *(LAS f32x2*)(L + L_FM + (w * 2 + h) * 8) = f32x2{a.m, l};
}
DI void a_reset(Acc& a) { a.m = -INFINITY; a.l = 0.f; a.o = f32x4{0.f, 0.f, 0.f, 0.f}; }
DI u32 sw2(u32 row) { return (row & 2) | ((row >> 1) & 4); }
DI u32 fsw(u32 row) { return ((row & 3) << 2) | ((row >> 2) & 3); }
DI void store_out(u16* out_, int col0, int lane, f32x4 a) {
  GAS u16* out = (GAS u16*)out_ + col0 + lane;
  const u32 p0 = pk2(a[0], a[1]), p1 = pk2(a[2], a[3]);
  if (lane < 8) { out[0] = (u16)p0; out[1152] = (u16)(p0 >> 16); out[2304] = (u16)p1; out[3456] = (u16)(p1 >> 16); }
}

template <bool FIRST, class F>
DI void ba_pass(LAS char* L, int w, int lane, const v4u (&qf)[16], f32x16 (&acc)[2], float& m, float& l, F&& hook) {
  asm volatile("" : "+v"(lane));
  const int kb = w & 3, ch = w >> 2, hh = lane >> 5, r31 = lane & 31;
  f32x16 S;
#pragma unroll
  for (int i = 0; i < 16; ++i) S[i] = 0.f;
  const u32 rowb = (32 * kb + r31) * 1024 + ch * 512, f = fsw(r31);
#pragma unroll
  for (int ks = 0; ks < 16; ++ks) S = mfma32(lld128(L, rowb + (ks >> 3) * 256 + (((2 * (ks & 7) + hh) ^ f) * 16)), qf[ks], S);
  float s[8];
  {
    v4u a, b;
#pragma unroll
    for (int j = 0; j < 4; ++j) { a[j] = asu(ch ? S[j] : S[8 + j]); b[j] = asu(ch ? S[4 + j] : S[12 + j]); }
    lst128(L, L_SX + w * 2048 + lane * 16, a);
    lst128(L, L_SX + w * 2048 + 1024 + lane * 16, b);
    asm volatile("" ::: "memory");
    hook(0);
    BAR();
    a = lld128(L, L_SX + (w ^ 4) * 2048 + lane * 16);
    b = lld128(L, L_SX + (w ^ 4) * 2048 + 1024 + lane * 16);
#pragma unroll
    for (int j = 0; j < 4; ++j) {
      s[j] = ((ch ? S[8 + j] : S[j]) + asf(a[j])) * (SCALE * LOG2E);
      s[4 + j] = ((ch ? S[12 + j] : S[4 + j]) + asf(b[j])) * (SCALE * LOG2E);
    }
  }
  float cm = fmaxf(fmaxf(fmaxf(s[0], s[1]), fmaxf(s[2], s[3])), fmaxf(fmaxf(s[4], s[5]), fmaxf(s[6], s[7])));
  cm = x32max(cm);
  if (lane < 32) *(LAS float*)(L + L_WM + (w * 32 + lane) * 4) = cm;
  BAR();
  float mn = FIRST ? -INFINITY : m;
#pragma unroll
  for (int i = 0; i < 8; ++i) mn = fmaxf(mn, *(LAS float*)(L + L_WM + (i * 32 + r31) * 4));
  float ps = 0.f;
#pragma unroll
  for (int j = 0; j < 8; ++j) { s[j] = ex2(s[j] - mn); ps += s[j]; }
  lst128(L, L_P + (2 * kb + ch) * 1024 + lane * 16, v4u{pk2(s[0], s[1]), pk2(s[2], s[3]), pk2(s[4], s[5]), pk2(s[6], s[7])});
  if (FIRST) {
    l = ps;
#pragma unroll
    for (int i = 0; i < 16; ++i) { acc[0][i] = 0.f; acc[1][i] = 0.f; }
  } else {
    const float al = ex2(m - mn);
    l = l * al + ps;
    acc[0] *= al;
    acc[1] *= al;
  }
  m = mn;
  asm volatile("" ::: "memory");
  hook(1);
  BAR();
  const int q = (lane & 15) >> 2, pp = lane & 3, blk = (lane >> 4) & 1;
  u32 ta[2][2];
#pragma unroll
  for (int T = 0; T < 2; ++T)
#pragma unroll
    for (int t = 0; t < 2; ++t) {
      const u32 c = 8 * w + 4 * T + 2 * blk + (pp >> 1);
      ta[T][t] = (8 * t + 4 * hh + q) * 1024 + ((c ^ ((q << 2) | (2 * t + hh))) * 16) + 8 * (pp & 1);
    }
#pragma unroll
  for (int ks = 0; ks < 8; ++ks) {
    const v4u P = lld128(L, L_P + ks * 1024 + lane * 16);
#pragma unroll
    for (int T = 0; T < 2; ++T) acc[T] = mfma32(ltr2(L, ta[T][0] + ks * 16384, ta[T][1] + ks * 16384), P, acc[T]);
  }
  asm volatile("" : "+v"(acc[0]), "+v"(acc[1]));
}

__global__ void __launch_bounds__(512, 2) decode_mega(Params args) {
  extern __shared__ __attribute__((aligned(16))) char smem[];
  LAS char* L = (LAS char*)smem;
  v16u ka;
  v8u kb;
  v2u kc;
  asm volatile("s_load_dwordx16 %0, %3, 0x0\n\ts_load_dwordx8 %1, %3, 0x40\n\ts_load_dwordx2 %2, %3, 0x60"
               : "=&s"(ka), "=&s"(kb), "=&s"(kc) : "s"(__builtin_amdgcn_kernarg_segment_ptr()) : "memory");
  const int tid = threadIdx.x, lane = tid & 63, w = __builtin_amdgcn_readfirstlane(tid >> 6), hw = w & 3;
  const int wg = blockIdx.x, g = wg >> 3, s = wg & 7, r = (s + g) & 7;
  const int kvh = g >> 1;
  u32 pf = 0;
  if (w == 7) {
    u64 pc;
    asm volatile("s_getpc_b64 %0" : "=s"(pc));
    if ((lane * 32 + g) * 128 < CODE_BYTES) pf = *(const GAS u32*)((const GAS char*)(pc & ~127ull) + (lane * 32 + g) * 128);
  }
  if (w >= 4) __builtin_amdgcn_s_setprio(3);
  if (tid < 8) *(LAS u32*)(L + L_SYNC + tid * 4) = 0u;
  const int n16 = lane & 15, g4 = lane >> 4;
  const int ai = (g & 1) * 4 + r;
  const int bi = g * 2 + (r - 4);
  const int h = (g >> 3) * 8 + s, half = r - 6, bw = h * 2 + half;
  const u32 vw128 = (lane >> 3) * 8192 + (((lane & 7) ^ sw2((lane >> 3) | ((w & 1) << 3))) * 16);
  const u32 cbo = (bi * 256 + w) * 1024;
  const bool isA = r < 4, isBA = r == 4 || r == 5, hlp = w >= 4;
  u32 so0, so1 = 0, step = 32 * 8192, lstep = 4096, vo = vw128, ld = hw * 1024;
  int n0, n1 = 0;
  if (isA) {
    so0 = ((r >> 1) * 288 + 8 * w) * 8192 + g * 256 + (r & 1) * 128;
    so1 = ((ai >> 2) * 288 + 8 * w) * 8192 + kvh * 512 + (ai & 3) * 128;
    n0 = n1 = hlp ? 0 : 9;
  } else if (isBA) {
    so0 = cbo; so1 = cbo + 8 * 1024; step = lstep = 16 * 1024; vo = (lane ^ fsw(w)) * 16; ld = w * 1024; n0 = n1 = 8;
  } else if (hlp) {
    so0 = (half * 256 + 4 * hw) * 8192 + h * 256; step = 16 * 8192; vo = g4 * 8192 + ((n16 ^ g4 ^ (4 * hw)) * 16); n0 = 16;
  } else {
    so0 = 8 * w * 8192 + h * 256 + half * 128; ld = L_UV + w * 1024; n0 = UV_EARLY;
  }
  asm volatile("s_waitcnt lgkmcnt(0)\n\ts_barrier" : "+s"(ka), "+s"(kb), "+s"(kc) : "s"(so0), "s"(so1), "s"(step), "s"(lstep), "v"(vo), "s"(ld), "s"(n0), "s"(n1) : "memory");
  Params p;
#define PTR(v, i) ((char*)(((u64)v[(i) + 1] << 32) | v[(i)]))
  p.Ax = PTR(ka, 0); p.AK = PTR(ka, 2); p.AV = PTR(ka, 4); p.Awq = PTR(ka, 6); p.Awo = PTR(ka, 8); p.Bx = PTR(ka, 10); p.BC = PTR(ka, 12); p.Bwq = PTR(ka, 14);
  p.Bwuk = PTR(kb, 0); p.Bwuv = PTR(kb, 2); p.Bwo = PTR(kb, 4); p.out = (u16*)PTR(kb, 6); p.ws = PTR(kc, 0);
  u64 epoch;
  asm volatile("s_load_dwordx2 %0, %1, 0x0" : "=s"(epoch) : "s"(p.ws) : "memory");
  {
    if (isA && hlp) {
      const char* xs = (w & 1) ? p.Ax : p.Bx;
#pragma unroll 1
      for (u32 o = (w >> 1 & 1) * 896; o < 4480; o += 1792) dma16c(xs + o, lane * 16, L_XV + (w & 1) * 4608 + o);
    }
    const char* src = (isA ? p.Bwq : isBA ? p.BC : hlp ? p.Bwuk : p.Bwuv) + so0;
    int n = n0;
    if (w == 0) STAMP(20);
#pragma unroll 1
    for (int bt = 0; bt < 2; ++bt) {
#pragma unroll 1
      for (; n > 0; --n) { VMWAIT(8); dma16(src, vo, ld); src += step; ld += lstep; }
      src = (isA ? p.Awq : p.BC) + so1;
      ld = isA ? L_QAW + w * 1024 : (8 + w) * 1024;
      vo ^= isA ? 0 : 32;
      n = n1;
    }
    if (w == 0) STAMP(21);
    asm volatile("s_waitcnt lgkmcnt(0)" : "+s"(epoch)::"memory");
  }
  const rsrc_t rs = __builtin_amdgcn_make_buffer_rsrc((void*)p.ws, 0, WS_BYTES, 0x00020000);
  const u32 trrow = 8 * g4 + (n16 >> 2);
  const u32 tr128 = trrow * 128 + ((2 * hw + ((lane & 3) >> 1)) ^ sw2(trrow)) * 16 + 8 * (lane & 1);
  const u32 tA = (((u32)epoch & 0x7f7f7f7fu) + 0x01010101u) ^ ((u32)epoch & 0x80808080u), tB = (u32)(epoch >> 32) + 1u;

  u32 voffK, voffV, sel[4];
#define STREAM_CONSTS() do { u32 l_; asm volatile("v_mbcnt_lo_u32_b32 %0, -1, 0\n\tv_mbcnt_hi_u32_b32 %0, -1, %0" : "=v"(l_)); \
    const u32 n_ = l_ & 15, g_ = l_ >> 4; \
    voffK = (n_ >> 1) * 256 + (2 * g_ + (n_ & 1)) * 16; voffV = g_ * 512 + n_ * 16; \
    const u32 h2 = (n_ >> 1) & 1, by = (2 * h2) | ((2 * h2 + 1) << 8); \
    const u32 on = (n_ & 1) ? ((by << 16) | 0x0c0cu) : (0x0c0c0000u | by); \
    _Pragma("unroll") for (int i = 0; i < 4; ++i) sel[i] = (n_ >> 2) == i ? on : 0x0c0c0c0cu; } while (0)
  const int pair = isA ? r >> 1 : isBA ? r & 1 : (r & 1) ^ 1, prod = isA ? r & 1 : isBA ? 2 : 3;
  const int dp = (g & 1) ? -DPAR : DPAR;
  const int c0 = prod == 0 ? 0 : prod == 1 ? C_A + dp : prod == 2 ? 2 * C_A : 2 * C_A + C_BA + (pair ? -dp : dp);
  const int c1 = prod == 0 ? C_A + dp : prod == 1 ? 2 * C_A : prod == 2 ? 2 * C_A + C_BA + (pair ? -dp : dp) : 128;
  const size_t kv0 = ((size_t)((g & 1) * 2 + pair) * 16 + kvh) * (4096 * 256) + 4096;
  const u32 q_off = WS_E2 + (g * 2 + pair) * 2048 + lane * 16;
#define LDK(id, j) ldnt(p.AK + kv0 + (size_t)(id) * 8192, voffK, ((j) >> 1) * 2048 + ((j) & 1) * 128 - 4096)
#define LDV(id, j) ldnt(p.AV + kv0 + (size_t)(id) * 8192, voffV, ((j) >> 1) * 2048 + ((j) & 1) * 256 - 4096)
  const bool t1 = lane & 1;
#define LOAD_WO() do { \
    const char* sx = isA ? p.Awo + (u32)((kvh * 256 + ai * 32) * 1152 + hw * 1024) : p.Bwo + (u32)(bw * 64 * 1152 + hw * 1024); \
    u32 lx = L_WO + hw * 1024; \
    _Pragma("unroll 1") for (int i = isA ? 9 : 18; i > 0; --i) { dma16(sx, (lane ^ (g4 << 1)) * 16, lx); sx += 4096; lx += 4096; } } while (0)

  v4u K[8], V[8], QF[8];
  Acc acc;
  bool chk = false;

  if (!isBA && !hlp) {
    STREAM_CONSTS();
    if (isA) BAR();
    else {
      lds_wait(L, SY_QABS, 4);
#pragma unroll 1
      for (int i = UV_EARLY; i < 16; ++i) dma16(p.Bwuv + so0 + (size_t)i * 32 * 8192, vw128, L_UV + w * 1024 + i * 4096);
    }
    v4u K0[8], V0[8];
#pragma unroll
    for (int j = 0; j < 8; ++j) { VMWAIT(8); K0[j] = LDK(c0 + w, j); SB(); }
    VMWAIT(8);
    if (isA) BAR();
#pragma unroll
    for (int j = 0; j < 8; ++j) { VMWAIT(PACE); V0[j] = LDV(c0 + w, j); SB(); }
#pragma unroll
    for (int j = 0; j < 8; ++j) { VMWAIT(PACE); K[j] = LDK(c0 + w + 4, j); SB(); }
#pragma unroll
    for (int j = 0; j < 8; ++j) { VMWAIT(PACE); V[j] = LDV(c0 + w + 4, j); SB(); }
    a_reset(acc);
    if (w == 0) STAMP(1);
    BAR();
    if (w == 0) STAMP(2);
    a_loadq(L, lane, QF);
    const u32 PP = a_soft(a_qk(K0, QF, t1), acc);
    a_pv(PP, V0, sel, acc);
    chk = DYNP && !isA;
  } else if (isA) {
    const u32 xo = L_XV + (lane & 3) * 1152 + g4 * 16;
    VMWAIT(0);
    if (w == 4) STAMP(23);
    BAR();
    if (w == 4) STAMP(3);
    f32x4 d = gemv288(L, L_QBW + tr128, xo + (r >> 1) * 576);
    st_sc1(rs, n16 < 4 ? WS_E1 + ((g * 2 + (r >> 1)) * 4 + n16) * 512 + (r & 1) * 256 + hw * 64 + g4 * 16 : OOB, tag4(d, tA));
    if (w == 4) STAMP(4);
    BAR();
    d = gemv288(L, L_QAW + tr128, xo + 4608 + (ai >> 2) * 576);
    st_sc1(rs, n16 < 4 ? WS_E2 + ((kvh * 4 + n16) * 2 + (ai >> 2)) * 1024 + (ai & 3) * 256 + hw * 64 + g4 * 16 : OOB, tag4(d, tA));
    if (w == 4) STAMP(5);
    if (w == 4) *(LAS v2u*)(L + L_QS + lane * 8) = fetch_q(rs, q_off, 1024, tA);
    if (w == 4) STAMP(6);
    BAR();
    lds_wait(L, SY_LAST, LATE_A);
    LOAD_WO();
  } else if (isBA) {
    const int b = bi >> 4, sp = bi & 15;
    const char* cb = p.BC + cbo + 128 * 1024;
    const u32 vc0 = (lane ^ fsw(w)) * 16;
    v4u qs[4], c2[16];
#pragma unroll
    for (int i = 0; i < C2_EARLY; ++i) c2[i] = ldnt(cb + 8 * i * 1024, vc0 ^ ((i & 1) * 32), 0);
    SB();
    poll_flags(rs, WS_FQ + (8 * w + (lane & 7)) * 64, tA, tB);
    if (w == 4) STAMP(7);
#pragma unroll
    for (int i = 0; i < 4; ++i) qs[i] = ld_sc1(rs, WS_QABS + (b * 32 + 4 * w + i) * 1024 + (lane ^ fsw(4 * w + i)) * 16);
    SB();
    asm volatile("s_barrier" ::: "memory");
#pragma unroll
    for (int i = C2_EARLY; i < (C2_EARLY + 16) / 2; ++i) c2[i] = ldnt(cb + 8 * i * 1024, vc0 ^ ((i & 1) * 32), 0);
    SB();
#pragma unroll
    for (int i = 0; i < 4; ++i) lst128(L, L_X + (4 * w + i) * 1024 + lane * 16, qs[i]);
    BAR();
    v4u qf[16];
    {
      const u32 rowb = L_X + (lane & 31) * 1024 + (w >> 2) * 512, f = fsw(lane & 31);
#pragma unroll
      for (int ks = 0; ks < 16; ++ks) qf[ks] = lld128(L, rowb + (ks >> 3) * 256 + (((2 * (ks & 7) + (lane >> 5)) ^ f) * 16));
    }
    BAR();
#pragma unroll
    for (int i = (C2_EARLY + 16) / 2; i < 16; ++i) c2[i] = ldnt(cb + 8 * i * 1024, vc0 ^ ((i & 1) * 32), 0);
    SB();
    if (tid < 2) *(LAS u32*)(L + SY_A + tid * 4) = 0u;
    if (w == 0) STAMP(8);
    f32x16 ac[2];
    float m, l;
    ba_pass<true>(L, w, lane, qf, ac, m, l, [](int) {});
    BAR();
    if (w == 0) STAMP(9);
#pragma unroll
    for (int i = 0; i < 16; ++i) lst128(L, (8 * i + w) * 1024 + lane * 16, c2[i]);
    v2u qpk = {0u, 0u};
    if (w == 4) qpk = fetch_q(rs, q_off, 1024, tA);
    BAR();
    if (w == 0) STAMP(10);
    v4u K0[8], V0[8];
    if (!hlp) {
      STREAM_CONSTS();
#pragma unroll
      for (int j = 0; j < 8; ++j) K0[j] = LDK(c0 + w, j);
      SB();
    }
    ba_pass<false>(L, w, lane, qf, ac, m, l, [&](int at) {
      if (!hlp) {
        STREAM_CONSTS();
        if (at == 0) {
#pragma unroll
          for (int j = 0; j < 8; ++j) V0[j] = LDV(c0 + w, j);
        } else if (BA_PF >= 3) {
#pragma unroll
          for (int j = 0; j < 8; ++j) K[j] = LDK(c0 + w + 4, j);
        }
        SB();
      }
    });
    if (w == 0) STAMP(29);
    l = x32sum(l);
    if (lane < 32) *(LAS float*)(L + L_WL + (w * 32 + lane) * 4) = l;
    BAR();
    {
      const u32 hd = lane & 31, hh = lane >> 5;
#pragma unroll
      for (int T = 0; T < 2; ++T)
#pragma unroll
        for (int rq = 0; rq < 4; ++rq)
          *(LAS v2u*)(L + hd * 1024 + (((8 * w + 4 * T + rq) ^ (hd & 15)) * 16) + 8 * hh) =
              v2u{pk2(ac[T][4 * rq], ac[T][4 * rq + 1]), pk2(ac[T][4 * rq + 2], ac[T][4 * rq + 3])};
    }
    if (w == 4) *(LAS v2u*)(L + L_QS + lane * 8) = qpk;
    BAR();
    if (hlp) {
#pragma unroll
      for (int i = 0; i < 8; ++i) {
        const u32 hd = 8 * hw + i;
        st_sc1(rs, WS_PART + ((hd * 4 + b) * 16 + sp) * 1024 + lane * 16, lld128(L, hd * 1024 + ((lane ^ (hd & 15)) * 16)));
      }
      if (w == 4 && lane < 32) {
        float ls = 0.f;
#pragma unroll
        for (int i = 0; i < 8; ++i) ls += *(LAS float*)(L + L_WL + (i * 32 + lane) * 4);
        st_sc1_8(rs, WS_ML + ((lane * 4 + b) * 16 + sp) * 8, v2u{asu(m), asu(ls)});
      }
      VMWAIT(0);
      if (lds_inc(L, SY_A) == 3 && lane == 0) st_sc1_8(rs, WS_FP + bi * 64, v2u{tA, tB});
      if (w == 4) STAMP(11);
    } else {
      STREAM_CONSTS();
      a_reset(acc);
      a_loadq(L, lane, QF);
      const u32 PP = a_soft(a_qk(K0, QF, t1), acc);
      a_pv(PP, V0, sel, acc);
      asm volatile("" : "+v"(acc.o)::"memory");
      if (BA_WAIT) lds_wait(L, SY_A, 4);
      if (BA_PF < 3) {
#pragma unroll
        for (int j = 0; j < 8; ++j) K[j] = LDK(c0 + w + 4, j);
      }
#pragma unroll
      for (int j = 0; j < 8; ++j) V[j] = LDV(c0 + w + 4, j);
      SB();
    }
  } else {
    if (w < 6) *(LAS v2u*)(L + L_QB + (2 * hw + (lane >> 5)) * 320 + (lane & 31) * 8) = fetch_q(rs, WS_E1 + h * 4096 + hw * 1024 + lane * 16, 2048, tA);
    VMWAIT(0);
    lds_inc(L, SY_QB);
    lds_wait(L, SY_QB, 4);
    if (w == 4) STAMP(12);
    {
      f32x4 d[4];
#pragma unroll
      for (int T = 0; T < 4; ++T) d[T] = f32x4{0.f, 0.f, 0.f, 0.f};
#pragma unroll
      for (int ks = 0; ks < 4; ++ks) {
        const v4u qb = lld128(L, L_QB + (lane & 3) * 320 + ks * 64 + g4 * 16);
        const u32 a0 = (64 * hw + n16) * 256 + (((4 * ks + g4) ^ n16) * 16);
#pragma unroll
        for (int T = 0; T < 4; ++T) d[T] = mfma16(lld128(L, a0 + T * 4096), qb, d[T]);
      }
      const int T = n16 >> 2;
      const f32x4 e = T == 0 ? d[0] : T == 1 ? d[1] : T == 2 ? d[2] : d[3];
      st_sc1_8(rs, WS_QABS + ((lane & 3) * 32 + h) * 1024 + (half * 256 + 64 * hw + 16 * T + 4 * g4) * 2, v2u{pk2(e[0], e[1]), pk2(e[2], e[3])});
    }
    VMWAIT(0);
    if (lds_inc(L, SY_QABS) == 3 && lane == 0) st_sc1_8(rs, WS_FQ + bw * 64, v2u{tA, tB});
    if (w == 4) STAMP(13);
    if (w == 4) *(LAS v2u*)(L + L_QS + lane * 8) = fetch_q(rs, q_off, 1024, tA);
    BAR();
    if (LATE_B == 0) LOAD_WO();
    v4u uf[16];
#pragma unroll
    for (int ks = 0; ks < 16; ++ks) uf[ks] = ltr2(L, L_UV + tr128 + ks * 4096, L_UV + tr128 + ks * 4096 + 512);
    if (w == 4) STAMP(24);
    poll_flags(rs, WS_FP + (16 * hw + n16) * 64, tA, tB);
    if (lane == 0) *(volatile LAS u32*)(L + SY_HOLD) = 1u;
    if (w == 4) STAMP(25);
    {
      const v2u ml = ld_sc1_8(rs, WS_ML + ((h * 4 + hw) * 16 + n16) * 8);
      v4u pt[16];
#pragma unroll
      for (int i = 0; i < 16; ++i) pt[i] = ld_sc1(rs, WS_PART + ((h * 4 + hw) * 16 + i) * 1024 + lane * 16);
      float M = fmaxf(asf(ml.x), dppf<XOR1>(asf(ml.x)));
      M = fmaxf(M, dppf<XOR2>(M)); M = fmaxf(M, dppf<ROR4>(M)); M = fmaxf(M, dppf<ROR8>(M));
      const float wt = ex2(asf(ml.x) - M);
      float Ls = wt * asf(ml.y);
      Ls += dppf<XOR1>(Ls); Ls += dppf<XOR2>(Ls); Ls += dppf<XOR7>(Ls); Ls += dppf<XOR15>(Ls);
      const float wn = wt / Ls;
      float c[8];
#pragma unroll
      for (int k = 0; k < 8; ++k) c[k] = 0.f;
#pragma unroll
      for (int i = 0; i < 16; ++i) {
        const float wi = asf(__builtin_amdgcn_readlane(asu(wn), i));
#pragma unroll
        for (int k = 0; k < 4; ++k) { c[2 * k] += wi * asf(pt[i][k] << 16); c[2 * k + 1] += wi * asf(pt[i][k] & 0xffff0000u); }
      }
      lst128(L, L_CL + hw * 1088 + lane * 16, v4u{pk2(c[0], c[1]), pk2(c[2], c[3]), pk2(c[4], c[5]), pk2(c[6], c[7])});
    }
    lds_inc(L, SY_A);
    lds_wait(L, SY_A, 4);
    if (w == 4) STAMP(26);
    {
      f32x4 d = {0.f, 0.f, 0.f, 0.f};
#pragma unroll
      for (int ks = 0; ks < 16; ++ks) d = mfma16(lld128(L, L_CL + (lane & 3) * 1088 + ks * 64 + g4 * 16), uf[ks], d);
      const float dv = g4 == 0 ? d[0] : g4 == 1 ? d[1] : g4 == 2 ? d[2] : d[3];
      *(LAS u16*)(L + L_OA + g4 * 192 + (32 * (hw >> 1) + 8 * (n16 >> 2) + 4 * (hw & 1) + (n16 & 3)) * 2) = (u16)pk2(dv, dv);
    }
    if (w == 4) STAMP(27);
    if (LATE_B) {
      lds_wait(L, SY_LAST, LATE_B);
      LOAD_WO();
    }
  }

  if (!hlp) {
#define YIELD() do { if (chk && __builtin_amdgcn_readfirstlane(*(volatile LAS u32*)(L + SY_HOLD))) { lds_wait(L, SY_A, 4); chk = false; } } while (0)
    float S = a_qk(K, QF, t1);
    for (int id = c0 + w + 8; id < c1; id += 4) {
      YIELD();
      asm volatile("" : "+v"(S)::"memory");
#pragma unroll
      for (int j = 0; j < 8; ++j) K[j] = LDK(id, j);
      SB();
      const u32 PP = a_soft(S, acc);
      a_pv(PP, V, sel, acc);
      asm volatile("" : "+v"(acc.o)::"memory");
      YIELD();
#pragma unroll
      for (int j = 0; j < 8; ++j) V[j] = LDV(id, j);
      SB();
      if (TRIG == 0 && id + 4 >= c1 && lane == 0) __hip_atomic_fetch_add((LAS u32*)(L + SY_LAST), 1u, __ATOMIC_RELAXED, __HIP_MEMORY_SCOPE_WORKGROUP);
      S = a_qk(K, QF, t1);
    }
    asm volatile("" : "+v"(S)::"memory");
    if (TRIG == 1 && lane == 0) __hip_atomic_fetch_add((LAS u32*)(L + SY_LAST), 1u, __ATOMIC_RELAXED, __HIP_MEMORY_SCOPE_WORKGROUP);
    const u32 PP = a_soft(S, acc);
    a_pv(PP, V, sel, acc);
    a_flush(L, w, lane, acc);
    if (lds_inc(L, SY_B) == 3) {
      STAMP(16);
      asm volatile("" ::: "memory");
#pragma unroll
      for (int hh = 0; hh < 2; ++hh) {
        float mw[4], lw[4];
        f32x2 aw[4];
#pragma unroll
        for (int i = 0; i < 4; ++i) {
          const f32x2 ml = *(LAS f32x2*)(L + L_FM + (i * 2 + hh) * 8);
          mw[i] = ml.x; lw[i] = ml.y;
          aw[i] = *(LAS f32x2*)(L + L_FL + ((i * 2 + hh) * 128 + 2 * lane) * 4);
        }
        const float M = fmaxf(fmaxf(mw[0], mw[1]), fmaxf(mw[2], mw[3]));
        float Ls = 0.f, o0 = 0.f, o1 = 0.f;
#pragma unroll
        for (int i = 0; i < 4; ++i) { const float wt = ex2(mw[i] - M); Ls += wt * lw[i]; o0 += wt * aw[i].x; o1 += wt * aw[i].y; }
        const float inv = __builtin_amdgcn_rcpf(Ls);
        const u32 rec = ((g * 2 + pair) * 4 + prod) * 2 + hh;
        st_sc1(rs, WS_E3 + (rec * 64 + lane) * 16, v4u{pk2(o0 * inv, o1 * inv), tA, asu(M + __builtin_amdgcn_logf(Ls)), tB});
      }
      if (TRIG == 2 && lane == 0) *(volatile LAS u32*)(L + SY_LAST) = 4u;
      STAMP(15);
    }
  }
  if (!isBA && hlp) {
    VMWAIT(0);
    lds_inc(L, SY_WO);
    lds_wait(L, SY_WO, 4);
    if (w == 4) STAMP(28);
    u32 ta[8];
#pragma unroll
    for (int i = 0; i < 8; ++i) {
      const u32 e = 72 * (4 * g4 + (n16 >> 2)) + 2 * hw + ((lane & 3) >> 1) + 8 * i;
      ta[i] = L_WO + (e ^ ((e >> 3) & 6)) * 16 + 8 * (lane & 1);
    }
    v4u wf[9];
#pragma unroll
    for (int i = 0; i < 9; ++i) wf[i] = ltr2(L, ta[i & 7] + (i >> 3) * 1024, ta[i & 7] + (i >> 3) * 1024 + 18432);
    f32x4 e[9];
    if (isA) {
      const int i16 = lane >> 2;
      const u32 rec = ((kvh * 4 + hw) * 4 + (lane & 3)) * 2 + (ai >> 2);
      v4u v[1];
      poll16<1, false>(rs, WS_E3 + (rec * 64 + 16 * (ai & 3) + i16) * 16, 0, v, tA, tB);
      const float m = asf(v[0].z);
      float M = fmaxf(m, dppf<XOR1>(m));
      M = fmaxf(M, dppf<XOR2>(M));
      float Ls = ex2(m - M), o0 = Ls * asf(v[0].x << 16), o1 = Ls * asf(v[0].x & 0xffff0000u);
      Ls += dppf<XOR1>(Ls); o0 += dppf<XOR1>(o0); o1 += dppf<XOR1>(o1);
      Ls += dppf<XOR2>(Ls); o0 += dppf<XOR2>(o0); o1 += dppf<XOR2>(o1);
      const float inv = __builtin_amdgcn_rcpf(Ls);
      if ((lane & 3) == 0) *(LAS u32*)(L + L_OA + hw * 192 + (4 * ((i16 >> 1) & 3) + 2 * (i16 >> 3) + (i16 & 1)) * 4) = pk2(o0 * inv, o1 * inv);
#pragma unroll
      for (int i = 0; i < 9; ++i) e[i] = f32x4{0.f, 0.f, 0.f, 0.f};
    } else {
      const v4u af1 = lld128(L, L_OA + (lane & 3) * 192 + 64 + g4 * 16);
#pragma unroll
      for (int i = 0; i < 9; ++i)
        e[i] = mfma16(af1, ltr2(L, ta[i & 7] + (i >> 3) * 1024 + 36864, ta[i & 7] + (i >> 3) * 1024 + 36864 + 18432), f32x4{0.f, 0.f, 0.f, 0.f});
    }
    if (isA) BAR();
    if (w == 4) STAMP(17);
    const v4u af = lld128(L, L_OA + (lane & 3) * 192 + g4 * 16);
#pragma unroll
    for (int i = 0; i < 9; ++i) e[i] = mfma16(af, wf[i], e[i]);
    const u32 np = isA ? 128 : 64;
    const u32 eo = (isA ? WS_E4 + (g * 4 + r) * 128 : WS_E6 + bw * 128) + (8 * g4 + 2 * hw + (n16 >> 3)) * np * 128 + (n16 & 7) * 16;
#pragma unroll
    for (int k = 0; k < 2; ++k) {
      const f32x4 x = g4 == 0 ? e[4 * k] : g4 == 1 ? e[4 * k + 1] : g4 == 2 ? e[4 * k + 2] : e[4 * k + 3];
      st_sc1(rs, eo + k * 32 * np * 128, tag4(x, tA));
    }
    st_sc1(rs, g4 == 0 ? eo + 64 * np * 128 : OOB, tag4(e[8], tA));
    if (w == 4) STAMP(18);
    if (!isA && hw * 64 + bw < 72) {
      v4u v[8];
      poll16<8, true>(rs, WS_E6 + (hw * 64 + bw) * 8192 + lane * 16, 1024, v, tA, tB);
      f32x4 a = untag4(v[0]);
#pragma unroll
      for (int i = 1; i < 8; ++i) a += untag4(v[i]);
      store_out(p.out + 576, (hw * 64 + bw) * 8, lane, sum_hi3(a));
      STAMP(14);
    }
  } else if (isA || isBA) {
    BAR();
  }
  const int oct = isBA ? bi : isA && r == 0 ? 64 + g : 72;
  if (oct < 72) {
    v4u v[2];
    poll16<2, true>(rs, WS_E4 + (oct * 16 + 2 * w) * 1024 + lane * 16, 1024, v, tA, tB);
    const f32x4 a = sum_hi3(untag4(v[0]) + untag4(v[1]));
    if (lane < 8) *(LAS f32x4*)(L + L_RD + (w * 8 + lane) * 16) = a;
    BAR();
    if (w == 0) {
      store_out(p.out, oct * 8, lane, sum_hi3(*(LAS f32x4*)(L + L_RD + lane * 16)));
      if (oct == 0 && lane == 0) __hip_atomic_store((GAS u64*)(p.ws + WS_EPOCH), ((u64)tB << 32) | tA, RLX_AGENT);
      STAMP(19);
    }
  }
  asm volatile("" ::"v"(pf));
}

extern "C" void kernel_launch(void* const* d_in, const int* in_sizes, int n_in, void* d_out, int out_size, void* d_ws, size_t ws_size,
                              hipStream_t stream) {
  Params p;
  p.Ax = (const char*)d_in[0]; p.AK = (const char*)d_in[1]; p.AV = (const char*)d_in[2]; p.Awq = (const char*)d_in[3]; p.Awo = (const char*)d_in[4];
  p.Bx = (const char*)d_in[5]; p.BC = (const char*)d_in[6]; p.Bwq = (const char*)d_in[7]; p.Bwuk = (const char*)d_in[8]; p.Bwuv = (const char*)d_in[9];
  p.Bwo = (const char*)d_in[10];
  p.out = (u16*)d_out;
  p.ws = (char*)d_ws;
  (void)hipFuncSetAttribute((const void*)decode_mega, hipFuncAttributeMaxDynamicSharedMemorySize, LDS_BYTES);
  decode_mega<<<256, 512, LDS_BYTES, stream>>>(p);
}
```

```cpp
#include <hip/hip_runtime.h>
#include <stdint.h>

typedef unsigned int u32;
typedef unsigned long long u64;
typedef unsigned short u16;
typedef u32 v4u __attribute__((ext_vector_type(4)));
typedef u32 v2u __attribute__((ext_vector_type(2)));
typedef u32 v8u __attribute__((ext_vector_type(8)));
typedef u32 v16u __attribute__((ext_vector_type(16)));
typedef float f32x2 __attribute__((ext_vector_type(2)));
typedef float f32x4 __attribute__((ext_vector_type(4)));
typedef float f32x16 __attribute__((ext_vector_type(16)));
typedef __bf16 bf16x8 __attribute__((ext_vector_type(8)));
typedef __bf16 bf16x2 __attribute__((ext_vector_type(2)));
typedef short s16x4 __attribute__((ext_vector_type(4)));
#define DI __device__ __forceinline__
#define LAS __attribute__((address_space(3)))
#define GAS __attribute__((address_space(1)))
#define RLX_AGENT __ATOMIC_RELAXED, __HIP_MEMORY_SCOPE_AGENT

#define STAMPS 0
#define CODE_BYTES 29800
#define C2_EARLY 16
#define DYNP 0
#define BA_PF 2
#define BA_WAIT 0
#define LATE_A 4
#define BRED_LATE 0
#define FSTR 128
#define FQ_SLEEP 2
#define FP_SLEEP 2
#define PLAY 1
#define MLAY 1
#define DRY 1
#define TOPPRIO 0
#define UVSPLIT 0
#define LATEBAR 1
#define EARLYK 0
#define DPB 0
#define C_A 40
#define C_BA 20
#define DPAR 0
#define PSLEEP 1
#define PACE 8

constexpr int WS_EPOCH = 0;
constexpr int WS_FQ = 32768;
constexpr int WS_FP = 49152;
constexpr int WS_ML = 16384;
constexpr int WS_QABS = 65536;
constexpr int WS_E1 = 262144;
constexpr int WS_E2 = 393216;
constexpr int WS_E3 = 524288;
constexpr int WS_E6 = 1 << 20;
constexpr int WS_E4 = WS_E6 + 72 * 8192;
constexpr int WS_PART = 2 << 20;
constexpr int WS_BYTES = 4 << 20;
constexpr u32 OOB = 0x7f000000u;
constexpr int LDS_BYTES = 163840;
constexpr int L_QBW = 0, L_QAW = 36864;
constexpr int L_XV = 110592;
constexpr int L_WO = 0;
constexpr int L_UV = 73728;
constexpr int L_QS = 139264;
constexpr int L_FL = L_QS + 1024;
constexpr int L_FM = L_FL + 4096;
constexpr int L_OA = L_FM + 128;
constexpr int L_X = 131072;
constexpr int L_SX = L_X, L_P = L_X + 16384, L_WM = L_X + 24576, L_WL = L_X + 25600;
constexpr int L_SYNC = L_X + 26624;
constexpr int L_QB = L_OA + 1280;
constexpr int L_CL = L_QB + 1280;
static_assert(L_CL + 4352 <= L_WM, "LDS map");
enum { SY_A = L_SYNC, SY_B = L_SYNC + 4, SY_LAST = L_SYNC + 8, SY_QB = L_SYNC + 12, SY_QABS = L_SYNC + 16, SY_HOLD = L_SYNC + 20, SY_WO = L_SYNC + 24 };

constexpr float SCALE = 0.08838834764831845f;
constexpr float LOG2E = 1.4426950408889634f;
constexpr u32 SPIN_CAP = 1u << 18;

struct Params {
  const char *Ax, *AK, *AV, *Awq, *Awo, *Bx, *BC, *Bwq, *Bwuk, *Bwuv, *Bwo;
  u16* out;
  char* ws;
};

DI f32x4 mfma16(v4u a, v4u b, f32x4 c) {
  return __builtin_amdgcn_mfma_f32_16x16x32_bf16(__builtin_bit_cast(bf16x8, a), __builtin_bit_cast(bf16x8, b), c, 0, 0, 0);
}
DI f32x16 mfma32(v4u a, v4u b, f32x16 c) {
  return __builtin_amdgcn_mfma_f32_32x32x16_bf16(__builtin_bit_cast(bf16x8, a), __builtin_bit_cast(bf16x8, b), c, 0, 0, 0);
}
DI u32 pk2(float a, float b) { f32x2 v = {a, b}; return __builtin_bit_cast(u32, __builtin_convertvector(v, bf16x2)); }
DI float rbf(float a) { return (float)(__bf16)a; }
DI float asf(u32 x) { return __builtin_bit_cast(float, x); }
DI u32 asu(float x) { return __builtin_bit_cast(u32, x); }
DI float ex2(float x) { return __builtin_amdgcn_exp2f(x); }
template <int CTRL> DI float dppf(float x) { return asf(__builtin_amdgcn_mov_dpp(asu(x), CTRL, 0xf, 0xf, true)); }
template <int CTRL> DI u32 dppu(u32 x) { return __builtin_amdgcn_mov_dpp(x, CTRL, 0xf, 0xf, true); }
constexpr int XOR1 = 0xB1, XOR2 = 0x4E, XOR7 = 0x141, XOR15 = 0x140, ROR8 = 0x128, ROR4 = 0x124;
DI float x16max(float x) { auto s = __builtin_amdgcn_permlane16_swap(asu(x), asu(x), false, false); return fmaxf(asf(s[0]), asf(s[1])); }
DI float x32max(float x) { auto s = __builtin_amdgcn_permlane32_swap(asu(x), asu(x), false, false); return fmaxf(asf(s[0]), asf(s[1])); }
DI float x16sum(float x) { auto s = __builtin_amdgcn_permlane16_swap(asu(x), asu(x), false, false); return asf(s[0]) + asf(s[1]); }
DI float x32sum(float x) { auto s = __builtin_amdgcn_permlane32_swap(asu(x), asu(x), false, false); return asf(s[0]) + asf(s[1]); }
DI float sum_hi3(f32x4 a) {
  auto s0 = __builtin_amdgcn_permlane32_swap(asu(a[0]), asu(a[1]), false, false);
  auto s1 = __builtin_amdgcn_permlane32_swap(asu(a[2]), asu(a[3]), false, false);
  auto t = __builtin_amdgcn_permlane16_swap(asu(asf(s0[0]) + asf(s0[1])), asu(asf(s1[0]) + asf(s1[1])), false, false);
  const float c = asf(t[0]) + asf(t[1]);
  return c + dppf<ROR8>(c);
}
DI f32x4 sel4(bool c, f32x4 a, f32x4 b) { return f32x4{c ? a[0] : b[0], c ? a[1] : b[1], c ? a[2] : b[2], c ? a[3] : b[3]}; }

#define BAR() asm volatile("s_waitcnt lgkmcnt(0)\n\ts_barrier" ::: "memory")
#define VMWAIT(n) asm volatile("s_waitcnt vmcnt(%0)" ::"n"(n) : "memory")
#define SB() __builtin_amdgcn_sched_barrier(0)

DI v4u lld128(LAS char* L, u32 o) { return *(LAS v4u*)(L + o); }
DI void lst128(LAS char* L, u32 o, v4u v) { *(LAS v4u*)(L + o) = v; }
DI v2u ltr(LAS char* L, u32 o) { return __builtin_bit_cast(v2u, __builtin_amdgcn_ds_read_tr16_b64_v4i16((LAS s16x4*)(L + o))); }
DI v4u ltr2(LAS char* L, u32 o0, u32 o1) { v2u a = ltr(L, o0), b = ltr(L, o1); return v4u{a.x, a.y, b.x, b.y}; }
DI void dma16(const char* sbase, u32 voff, u32 lds) {
  u32 keep;
  asm volatile("s_mov_b32 %0, m0\n\ts_mov_b32 m0, %3\n\ts_nop 2\n\tglobal_load_lds_dwordx4 %1, %2 nt\n\ts_mov_b32 m0, %0"
               : "=&s"(keep) : "v"(voff), "s"(sbase), "s"(lds) : "memory");
}
DI void dma16c(const char* sbase, u32 voff, u32 lds) {
  u32 keep;
  asm volatile("s_mov_b32 %0, m0\n\ts_mov_b32 m0, %3\n\ts_nop 2\n\tglobal_load_lds_dwordx4 %1, %2\n\ts_mov_b32 m0, %0"
               : "=&s"(keep) : "v"(voff), "s"(sbase), "s"(lds) : "memory");
}
DI v4u ldnt(const char* base, u32 voff, int imm) { return __builtin_nontemporal_load((const GAS v4u*)((const GAS char*)base + voff + imm)); }
typedef __amdgpu_buffer_rsrc_t rsrc_t;
DI v4u ld_sc1(rsrc_t rs, u32 o) { return __builtin_amdgcn_raw_buffer_load_b128(rs, o, 0, 16); }
DI v2u ld_sc1_8(rsrc_t rs, u32 o) { return __builtin_amdgcn_raw_buffer_load_b64(rs, o, 0, 16); }
DI void st_sc1(rsrc_t rs, u32 o, v4u v) { __builtin_amdgcn_raw_buffer_store_b128(v, rs, o, 0, 16); }
DI void st_sc1_8(rsrc_t rs, u32 o, v2u v) { __builtin_amdgcn_raw_buffer_store_b64(v, rs, o, 0, 16); }
DI v4u tag4(f32x4 e, u32 tA) {
  v4u v;
#pragma unroll
  for (int k = 0; k < 4; ++k) v[k] = __builtin_amdgcn_perm(asu(e[k]) + 0x80u, tA, 0x07060500u + k);
  return v;
}
DI f32x4 untag4(v4u v) { return f32x4{asf(v.x & ~0xffu), asf(v.y & ~0xffu), asf(v.z & ~0xffu), asf(v.w & ~0xffu)}; }
template <int N, bool WORDS> DI void poll16(rsrc_t rs, u32 o, u32 stride, v4u (&v)[N], u32 tA, u32 tB, bool once = false) {
  for (u32 spins = once ? ~0u : 0u;; ++spins) {
    asm volatile("" ::: "memory");
    bool ok = true;
#pragma unroll
    for (int i = 0; i < N; ++i) v[i] = ld_sc1(rs, o + i * stride);
#pragma unroll
    for (int i = 0; i < N; ++i) {
      if (WORDS) ok = ok && (__builtin_amdgcn_perm(v[i].y, v[i].x, 0x0c0c0400u) | __builtin_amdgcn_perm(v[i].w, v[i].z, 0x04000c0cu)) == tA;
      else ok = ok && v[i].y == tA && v[i].w == tB;
    }
    if (__all(ok) || spins > SPIN_CAP) break;
    if (PSLEEP) __builtin_amdgcn_s_sleep(PSLEEP);
  }
}
template <int SLEEP> DI void poll_flags(rsrc_t rs, u32 o, u32 tA, u32 tB) {
  for (u32 spins = 0;; ++spins) {
    asm volatile("" ::: "memory");
    const v2u v = ld_sc1_8(rs, o);
    if (__all(v.x == tA && v.y == tB) || spins > SPIN_CAP) break;
    __builtin_amdgcn_s_sleep(SLEEP);
  }
  __builtin_amdgcn_fence(__ATOMIC_ACQUIRE, "wavefront");
}
DI u32 lds_inc(LAS char* L, u32 o) {
  asm volatile("s_waitcnt lgkmcnt(0)" ::: "memory");
  u32 old = 0;
  if ((threadIdx.x & 63) == 0) old = __hip_atomic_fetch_add((LAS u32*)(L + o), 1u, __ATOMIC_RELAXED, __HIP_MEMORY_SCOPE_WORKGROUP);
  return __builtin_amdgcn_readfirstlane(old);
}
DI void lds_wait(LAS char* L, u32 o, u32 want) {
#pragma unroll 1
  for (u32 spins = 0; spins < (1u << 24); ++spins) {
    if (__builtin_amdgcn_readfirstlane(__hip_atomic_load((LAS u32*)(L + o), __ATOMIC_RELAXED, __HIP_MEMORY_SCOPE_WORKGROUP)) >= want) break;
    __builtin_amdgcn_s_sleep(1);
  }
  asm volatile("" ::: "memory");
}

#define STAMP(i) do {} while (0)

DI f32x4 gemv288(LAS char* L, u32 base, u32 xo) {
  f32x4 d = {0.f, 0.f, 0.f, 0.f};
#pragma unroll
  for (int ks = 0; ks < 9; ++ks) d = mfma16(ltr2(L, base + ks * 4096, base + ks * 4096 + 512), lld128(L, xo + ks * 64), d);
  return d;
}
DI v2u fetch_q(rsrc_t rs, u32 o, u32 stride, u32 tA) {
  v4u v[2];
  poll16<2, true>(rs, o, stride, v, tA, 0u);
  const f32x4 q = untag4(v[0]) + untag4(v[1]);
  return v2u{pk2(q[0], q[1]), pk2(q[2], q[3])};
}

struct Acc { float m, l; f32x4 o; };
DI float a_qk(const v4u (&K)[8], const v4u (&QF)[8], bool t1) {
  f32x4 d = {0.f, 0.f, 0.f, 0.f};
#pragma unroll
  for (int j = 0; j < 8; ++j) d = mfma16(K[j], QF[j], d);
  const float keep = t1 ? d[3] : d[0], send = t1 ? d[1] : d[2];
  return keep + dppf<XOR1>(send);
}
DI u32 a_soft(float S, Acc& a) {
  const float t = rbf(rbf(S) * SCALE) * LOG2E;
  float cm = fmaxf(t, dppf<XOR1>(t));
  cm = fmaxf(cm, dppf<ROR4>(cm));
  cm = fmaxf(cm, dppf<ROR8>(cm));
  cm = x32max(x16max(cm));
  const float mn = fmaxf(a.m, cm);
  const float al = ex2(a.m - mn), p = ex2(t - mn);
  a.l = a.l * al + p;
  a.m = mn;
  a.o *= al;
  return pk2(p, dppf<XOR2>(p));
}
template <int J> DI void a_pv1(u32 PP, v4u V, const u32 (&sel)[4], Acc& a) {
  const u32 X = dppu<0x150 + 4 * (J >> 1) + (J & 1)>(PP);
  const v4u B = {__builtin_amdgcn_perm(X, X, sel[0]), __builtin_amdgcn_perm(X, X, sel[1]), __builtin_amdgcn_perm(X, X, sel[2]), __builtin_amdgcn_perm(X, X, sel[3])};
  a.o = mfma16(V, B, a.o);
}
DI void a_pv(u32 PP, const v4u (&V)[8], const u32 (&sel)[4], Acc& a) {
  a_pv1<0>(PP, V[0], sel, a); a_pv1<1>(PP, V[1], sel, a); a_pv1<2>(PP, V[2], sel, a); a_pv1<3>(PP, V[3], sel, a);
  a_pv1<4>(PP, V[4], sel, a); a_pv1<5>(PP, V[5], sel, a); a_pv1<6>(PP, V[6], sel, a); a_pv1<7>(PP, V[7], sel, a);
}
DI void a_loadq(LAS char* L, int lane, v4u (&QF)[8]) {
  const int n = lane & 15, g = lane >> 4;
  const u32 o = L_QS + ((n >> 1) & 1) * 256 + (2 * g + (n & 1)) * 16;
  const v4u F0 = lld128(L, o), F1 = lld128(L, o + 128), Z = {0u, 0u, 0u, 0u};
#pragma unroll
  for (int u = 0; u < 4; ++u) { const bool on = (n >> 2) == u; QF[2 * u] = on ? F0 : Z; QF[2 * u + 1] = on ? F1 : Z; }
}
DI void a_reset(Acc& a) { a.m = -INFINITY; a.l = 0.f; a.o = f32x4{0.f, 0.f, 0.f, 0.f}; }
DI u32 sw2(u32 row) { return (row & 2) | ((row >> 1) & 4); }
DI u32 fsw(u32 row) { return ((row & 3) << 2) | ((row >> 2) & 3); }
template <int N, class F> DI void reduce_oct(rsrc_t rs, u32 o, u32 tA, u16* out_, int lane, F&& gate) {
  GAS u16* out = (GAS u16*)out_ + ((lane >> 5) + ((lane >> 3) & 2)) * 1152 + (lane & 7);
  asm volatile("" : "+v"(out), "+v"(o));
#pragma unroll 1
  for (int pass = DRY ? 0 : 1; pass < 2; ++pass) {
    if (pass) gate();
    v4u v[N];
    poll16<N, true>(rs, pass ? o : OOB, 1024, v, tA, 0u, !pass);
    f32x4 a = untag4(v[0]);
#pragma unroll
    for (int i = 1; i < N; ++i) a += untag4(v[i]);
    const float c = sum_hi3(a);
    if (!(lane & 8) && pass) *out = (u16)pk2(c, c);
  }
}

template <bool FIRST, class F>
DI void ba_pass(LAS char* L, int w, int lane, const v4u (&qf)[16], f32x16 (&acc)[2], float& m, float& l, F&& hook) {
  asm volatile("" : "+v"(lane));
  const int kb = w & 3, ch = w >> 2, hh = lane >> 5, r31 = lane & 31;
  f32x16 S;
#pragma unroll
  for (int i = 0; i < 16; ++i) S[i] = 0.f;
  const u32 rowb = (32 * kb + r31) * 1024 + ch * 512, f = fsw(r31);
#pragma unroll
  for (int ks = 0; ks < 16; ++ks) S = mfma32(lld128(L, rowb + (ks >> 3) * 256 + (((2 * (ks & 7) + hh) ^ f) * 16)), qf[ks], S);
  float s[8];
  {
    v4u a, b;
#pragma unroll
    for (int j = 0; j < 4; ++j) { a[j] = asu(ch ? S[j] : S[8 + j]); b[j] = asu(ch ? S[4 + j] : S[12 + j]); }
    if (FIRST && LATEBAR) { asm volatile("" : "+v"(a), "+v"(b)); BAR(); }
    lst128(L, L_SX + w * 2048 + lane * 16, a);
    lst128(L, L_SX + w * 2048 + 1024 + lane * 16, b);
    asm volatile("" ::: "memory");
    hook(0);
    BAR();
    a = lld128(L, L_SX + (w ^ 4) * 2048 + lane * 16);
    b = lld128(L, L_SX + (w ^ 4) * 2048 + 1024 + lane * 16);
#pragma unroll
    for (int j = 0; j < 4; ++j) {
      s[j] = ((ch ? S[8 + j] : S[j]) + asf(a[j])) * (SCALE * LOG2E);
      s[4 + j] = ((ch ? S[12 + j] : S[4 + j]) + asf(b[j])) * (SCALE * LOG2E);
    }
  }
  float cm = fmaxf(fmaxf(fmaxf(s[0], s[1]), fmaxf(s[2], s[3])), fmaxf(fmaxf(s[4], s[5]), fmaxf(s[6], s[7])));
  cm = x32max(cm);
  if (lane < 32) *(LAS float*)(L + L_WM + (w * 32 + lane) * 4) = cm;
  BAR();
  float mn = FIRST ? -INFINITY : m;
#pragma unroll
  for (int i = 0; i < 8; ++i) mn = fmaxf(mn, *(LAS float*)(L + L_WM + (i * 32 + r31) * 4));
  float ps = 0.f;
#pragma unroll
  for (int j = 0; j < 8; ++j) { s[j] = ex2(s[j] - mn); ps += s[j]; }
  lst128(L, L_P + (2 * kb + ch) * 1024 + lane * 16, v4u{pk2(s[0], s[1]), pk2(s[2], s[3]), pk2(s[4], s[5]), pk2(s[6], s[7])});
  if (FIRST) {
    l = ps;
#pragma unroll
    for (int i = 0; i < 16; ++i) { acc[0][i] = 0.f; acc[1][i] = 0.f; }
  } else {
    const float al = ex2(m - mn);
    l = l * al + ps;
    acc[0] *= al;
    acc[1] *= al;
  }
  m = mn;
  asm volatile("" ::: "memory");
  hook(1);
  BAR();
  const int q = (lane & 15) >> 2, pp = lane & 3, blk = (lane >> 4) & 1;
  u32 ta[2][2];
#pragma unroll
  for (int T = 0; T < 2; ++T)
#pragma unroll
    for (int t = 0; t < 2; ++t) {
      const u32 c = 8 * w + 4 * T + 2 * blk + (pp >> 1);
      ta[T][t] = (8 * t + 4 * hh + q) * 1024 + ((c ^ ((q << 2) | (2 * t + hh))) * 16) + 8 * (pp & 1);
    }
#pragma unroll
  for (int ks = 0; ks < 8; ++ks) {
    const v4u P = lld128(L, L_P + ks * 1024 + lane * 16);
#pragma unroll
    for (int T = 0; T < 2; ++T) acc[T] = mfma32(ltr2(L, ta[T][0] + ks * 16384, ta[T][1] + ks * 16384), P, acc[T]);
  }
  asm volatile("" : "+v"(acc[0]), "+v"(acc[1]));
}

__global__ void __launch_bounds__(512, 2) decode_mega(Params args) {
  extern __shared__ __attribute__((aligned(16))) char smem[];
  LAS char* L = (LAS char*)smem;
  v16u ka;
  v8u kb;
  v2u kc;
  asm volatile("s_load_dwordx16 %0, %3, 0x0\n\ts_load_dwordx8 %1, %3, 0x40\n\ts_load_dwordx2 %2, %3, 0x60"
               : "=&s"(ka), "=&s"(kb), "=&s"(kc) : "s"(__builtin_amdgcn_kernarg_segment_ptr()) : "memory");
  SB();
  const int tid = threadIdx.x, lane = tid & 63;
  int w = __builtin_amdgcn_readfirstlane(tid >> 6), wg = blockIdx.x;
  u32 pf = 0;
  if (w == 7) {
    u64 pc;
    asm volatile("s_getpc_b64 %0" : "=s"(pc));
    if ((lane * 32 + (wg >> 3)) * 128 < CODE_BYTES) pf = *(const GAS u32*)((const GAS char*)(pc & ~127ull) + (lane * 32 + (wg >> 3)) * 128);
  }
  u32 vo, ld, step, lstep;
  const char* src0;
  {
    const int g = wg >> 3, r = (wg + g) & 7, hw = w & 3;
    const u32 koff = r < 4 ? (w < 4 ? 56 : (w & 1) ? 0 : 40) : r < 6 ? 48 : w >= 4 ? 64 : 72;
    asm volatile("s_load_dwordx2 %0, %1, %2" : "=s"(src0) : "s"(__builtin_amdgcn_kernarg_segment_ptr()), "s"(koff) : "memory");
    if (r < 4 && w >= 4) {
      u32 keep, xv = lane * 16 + (w & 2) * 448;
      asm volatile("s_setprio 3\n\ts_mov_b32 %0, m0\n\ts_mov_b32 m0, %2\n\ts_waitcnt lgkmcnt(0)\n\tglobal_load_lds_dwordx4 %1, %3\n\t"
                   "global_load_lds_dwordx4 %1, %3 offset:1792\n\tglobal_load_lds_dwordx4 %1, %3 offset:2688\n\ts_mov_b32 m0, %0"
                   : "=&s"(keep) : "v"(xv), "s"(L_XV + (w & 1) * 4608 + (w & 2) * 448), "s"(src0) : "memory");
    }
    const u32 hoff = ((g & ~7) | (wg & 7)) * 256;
    const bool cA = r < 4, cB = (r & 6) == 4, cK = r >= 6 && w >= 4;
    step = cB ? 16384 : cK ? 16 * 8192 : 32 * 8192;
    lstep = cB ? 16384 : 4096;
    ld = cB ? w * 1024 : hw * 1024 + (cA || cK ? 0 : L_UV);
    const u32 sA = (r >> 1) * (288 * 8192) + g * 256, sB = ((g * 2 + (r & 1)) * 256 + w) * 1024, sK = ((r & 1) * 256 + 4 * hw) * 8192 + hoff;
    const u32 so = cB ? sB : cK ? sK : (cA ? sA : hoff) + 8 * 8192 * w + (r & 1) * 128;
    const u32 sh = cB ? 6 : cK ? 4 : 3, row = (u32)lane >> sh;
    const u32 xs = cB ? fsw(w) : cK ? 4 * hw : 4 * (w & 1), xm = cK ? 3 : 2;
    vo = row * 8192 + ((((u32)lane - (row << sh)) ^ xs ^ (row & xm)) * 16) + so;
    if (TOPPRIO && w >= 4) __builtin_amdgcn_s_setprio(3);
    if (r >= 4 || w < 4) {
      u32 keep;
#define PIECE "global_load_lds_dwordx4 %1, %3 nt\n\ts_add_u32 m0, m0, %5\n\tv_add_u32 %1, %4, %1\n\t"
      asm volatile("s_mov_b32 %0, m0\n\ts_mov_b32 m0, %2\n\ts_waitcnt lgkmcnt(0)\n\t" PIECE PIECE PIECE PIECE PIECE PIECE PIECE PIECE "s_mov_b32 m0, %0"
                   : "=&s"(keep), "+v"(vo) : "s"(ld), "s"(src0), "s"(step), "s"(lstep) : "memory", "scc");
      ld += 8 * lstep;
    }
  }
  asm volatile("s_waitcnt lgkmcnt(0)" : "+s"(ka), "+s"(kb), "+s"(kc), "+s"(src0), "+s"(w), "+s"(wg)::"memory");
  if (w == 0) STAMP(20);
  Params p;
#define PTR(v, i) ((char*)(((u64)v[(i) + 1] << 32) | v[(i)]))
  p.Ax = PTR(ka, 0); p.AK = PTR(ka, 2); p.AV = PTR(ka, 4); p.Awq = PTR(ka, 6); p.Awo = PTR(ka, 8); p.Bx = PTR(ka, 10); p.BC = PTR(ka, 12); p.Bwq = PTR(ka, 14);
  p.Bwuk = PTR(kb, 0); p.Bwuv = PTR(kb, 2); p.Bwo = PTR(kb, 4); p.out = (u16*)PTR(kb, 6); p.ws = PTR(kc, 0);
  const int hw = w & 3, g = wg >> 3, s = wg & 7, r = (s + g) & 7, kvh = g >> 1;
  const bool isA = r < 4, isBA = r == 4 || r == 5, hlp = w >= 4;
  u64 epoch;
  asm volatile("s_load_dwordx2 %0, %1, 0x0" : "=s"(epoch) : "s"(p.ws) : "memory");
  if (hlp) __builtin_amdgcn_s_setprio(3);
  if (tid < 8) *(LAS u32*)(L + L_SYNC + tid * 4) = 0u;
  const int n16 = lane & 15, g4 = lane >> 4;
  const int ai = (g & 1) * 4 + r;
  const int bi = g * 2 + (r - 4);
  const int h = (g >> 3) * 8 + s, half = r - 6, bw = h * 2 + half;
  u32 vuv;
  {
    int n = isA ? (hlp ? 0 : 1) : isBA ? 0 : (hlp || !UVSPLIT) ? 8 : 0;
    vuv = vo;
#pragma unroll 1
    for (int bt = 0; bt < 2; ++bt) {
#pragma unroll 1
      for (; n > 0; --n) { VMWAIT(8); dma16(src0, vo, ld); vo += step; ld += lstep; }
      src0 = isA ? p.Awq : src0;
      ld = isA ? L_QAW + w * 1024 : (8 + w) * 1024;
      n = isA ? (hlp ? 0 : 9) : isBA ? 8 : 0;
      vo = (vo ^ (isA ? 0 : 32)) + (isA ? (u32)(((ai >> 2) - (r >> 1) - 1) * (288 * 8192) + kvh * 512 - g * 256 + (r & 2) * 128) : (u32)(-120 * 1024));
    }
    if (w == 0) STAMP(21);
    asm volatile("s_waitcnt lgkmcnt(0)" : "+s"(epoch)::"memory");
  }
  const rsrc_t rs = __builtin_amdgcn_make_buffer_rsrc((void*)p.ws, 0, WS_BYTES, 0x00020000);
  const u32 trrow = 8 * g4 + (n16 >> 2);
  const u32 tr128 = trrow * 128 + ((2 * hw + ((lane & 3) >> 1)) ^ sw2(trrow)) * 16 + 8 * (lane & 1);
  const u32 tA = (((u32)epoch & 0x7f7f7f7fu) + 0x01010101u) ^ ((u32)epoch & 0x80808080u), tB = (u32)(epoch >> 32) + 1u;

  u32 voffK, voffV, sel[4];
#define STREAM_CONSTS() do { u32 l_; asm volatile("v_mbcnt_lo_u32_b32 %0, -1, 0\n\tv_mbcnt_hi_u32_b32 %0, -1, %0" : "=v"(l_)); \
    const u32 n_ = l_ & 15, g_ = l_ >> 4; \
    voffK = (n_ >> 1) * 256 + (2 * g_ + (n_ & 1)) * 16; voffV = g_ * 512 + n_ * 16; \
    const u32 h2 = (n_ >> 1) & 1, by = (2 * h2) | ((2 * h2 + 1) << 8); \
    const u32 on = (n_ & 1) ? ((by << 16) | 0x0c0cu) : (0x0c0c0000u | by); \
    _Pragma("unroll") for (int i = 0; i < 4; ++i) sel[i] = (n_ >> 2) == i ? on : 0x0c0c0c0cu; } while (0)
  const int pair = isA ? r >> 1 : isBA ? r & 1 : (r & 1) ^ 1, prod = isA ? r & 1 : isBA ? 2 : 3;
  const int dp = (g & 1) ? -DPAR : DPAR, db = (g & 1) ? -DPB : DPB;
  const int c0 = prod == 0 ? 0 : prod == 1 ? C_A + dp : prod == 2 ? 2 * C_A : 2 * C_A + C_BA + (pair ? -db : db);
  const int c1 = prod == 0 ? C_A + dp : prod == 1 ? 2 * C_A : prod == 2 ? 2 * C_A + C_BA + (pair ? -db : db) : 128;
  const size_t kv0 = ((size_t)((g & 1) * 2 + pair) * 16 + kvh) * (4096 * 256) + 4096;
  const u32 q_off = WS_E2 + (g * 2 + pair) * 2048 + lane * 16;
#define LDK(id, j) ldnt(p.AK + kv0 + (size_t)(id) * 8192, voffK, ((j) >> 1) * 2048 + ((j) & 1) * 128 - 4096)
#define LDV(id, j) ldnt(p.AV + kv0 + (size_t)(id) * 8192, voffV, ((j) >> 1) * 2048 + ((j) & 1) * 256 - 4096)
  const bool t1 = lane & 1;
  const int nu = (ai & 3) == 3 ? 3 : 2;
#define LOAD_WO(SRC, NU, NI) do { \
    const u32 vx = (lane >> 3) * 1152 + (((lane & 7) ^ sw2((lane >> 3) | ((w & 1) << 3))) * 16); \
    u32 lx = L_WO + hw * 1024; \
    _Pragma("unroll 1") for (int u = 0; u < (NU); ++u) \
      _Pragma("unroll") for (int i = 0; i < (NI); ++i) { dma16((SRC) + (u32)(u * 128 + (32 * i + 8 * hw) * 1152), vx, lx); lx += 4096; } } while (0)

  v4u K[8], V[8], QF[8];
  Acc acc;
  bool chk = false;

  if (!isBA && !hlp) {
    STREAM_CONSTS();
    BAR();
    if (!isA) {
      if (UVSPLIT == 2) BAR();
      if (UVSPLIT) {
#pragma unroll 1
        for (int i = 8; i < 16; ++i) { dma16(p.Bwuv, vuv, L_UV + w * 1024 + i * 4096); vuv += 32 * 8192; }
      }
      if (UVSPLIT != 2) BAR();
    }
    v4u K0[8], V0[8];
#pragma unroll
    for (int j = 0; j < 8; ++j) { VMWAIT(8); K0[j] = LDK(c0 + w, j); SB(); }
    VMWAIT(8);
    if (isA) BAR();
#pragma unroll
    for (int j = 0; j < 8; ++j) { VMWAIT(PACE); V0[j] = LDV(c0 + w, j); SB(); }
#pragma unroll
    for (int j = 0; j < 8; ++j) { VMWAIT(PACE); K[j] = LDK(c0 + w + 4, j); SB(); }
#pragma unroll
    for (int j = 0; j < 8; ++j) { VMWAIT(PACE); V[j] = LDV(c0 + w + 4, j); SB(); }
    a_reset(acc);
    if (w == 0) STAMP(1);
    BAR();
    if (w == 0) STAMP(2);
    a_loadq(L, lane, QF);
    const u32 PP = a_soft(a_qk(K0, QF, t1), acc);
    a_pv(PP, V0, sel, acc);
    chk = DYNP && !isA;
  } else if (isA) {
    const u32 xo = L_XV + (lane & 3) * 1152 + g4 * 16;
    VMWAIT(0);
    if (w == 4) STAMP(23);
    BAR();
    if (w == 4) STAMP(3);
    f32x4 d = gemv288(L, L_QBW + tr128, xo + (r >> 1) * 576);
    st_sc1(rs, n16 < 4 ? WS_E1 + ((g * 2 + (r >> 1)) * 4 + n16) * 512 + (r & 1) * 256 + hw * 64 + g4 * 16 : OOB, tag4(d, tA));
    if (w == 4) STAMP(4);
    BAR();
    d = gemv288(L, L_QAW + tr128, xo + 4608 + (ai >> 2) * 576);
    st_sc1(rs, n16 < 4 ? WS_E2 + ((kvh * 4 + n16) * 2 + (ai >> 2)) * 1024 + (ai & 3) * 256 + hw * 64 + g4 * 16 : OOB, tag4(d, tA));
    if (w == 4) STAMP(5);
    if (w == 4) *(LAS v2u*)(L + L_QS + lane * 8) = fetch_q(rs, q_off, 1024, tA);
    if (w == 4) STAMP(6);
    BAR();
  } else if (isBA) {
    const int b = bi >> 4, sp = bi & 15;
    const char* cb = p.BC + (u32)((bi * 256 + w + 128) * 1024);
    const u32 vc0 = (lane ^ fsw(w)) * 16;
    v4u qs[4], c2[16];
#pragma unroll
    for (int i = 0; i < C2_EARLY; ++i) c2[i] = ldnt(cb + 8 * i * 1024, vc0 ^ ((i & 1) * 32), 0);
    SB();
    poll_flags<FQ_SLEEP>(rs, WS_FQ + (8 * w + (lane & 7)) * FSTR, tA, tB);
    if (w == 4) STAMP(7);
#pragma unroll
    for (int i = 0; i < 4; ++i) qs[i] = ld_sc1(rs, WS_QABS + (b * 32 + 4 * w + i) * 1024 + (lane ^ fsw(4 * w + i)) * 16);
    SB();
    asm volatile("s_barrier" ::: "memory");
#pragma unroll
    for (int i = C2_EARLY; i < (C2_EARLY + 16) / 2; ++i) c2[i] = ldnt(cb + 8 * i * 1024, vc0 ^ ((i & 1) * 32), 0);
    SB();
#pragma unroll
    for (int i = 0; i < 4; ++i) lst128(L, L_X + (4 * w + i) * 1024 + lane * 16, qs[i]);
    asm volatile("" ::: "memory");
    v4u K0[8], V0[8];
    if (EARLYK && !hlp) {
      STREAM_CONSTS();
#pragma unroll
      for (int j = 0; j < 8; ++j) K0[j] = LDK(c0 + w, j);
      SB();
    }
    BAR();
    v4u qf[16];
    {
      const u32 rowb = L_X + (lane & 31) * 1024 + (w >> 2) * 512, f = fsw(lane & 31);
#pragma unroll
      for (int ks = 0; ks < 16; ++ks) qf[ks] = lld128(L, rowb + (ks >> 3) * 256 + (((2 * (ks & 7) + (lane >> 5)) ^ f) * 16));
    }
    if (!LATEBAR) BAR();
#pragma unroll
    for (int i = (C2_EARLY + 16) / 2; i < 16; ++i) c2[i] = ldnt(cb + 8 * i * 1024, vc0 ^ ((i & 1) * 32), 0);
    if (w == 0) STAMP(8);
    f32x16 ac[2];
    float m, l;
    ba_pass<true>(L, w, lane, qf, ac, m, l, [](int) {});
    if (tid < 2) *(LAS u32*)(L + SY_A + tid * 4) = 0u;
    BAR();
    if (w == 0) STAMP(9);
#pragma unroll
    for (int i = 0; i < 16; ++i) lst128(L, (8 * i + w) * 1024 + lane * 16, c2[i]);
    v2u qpk = {0u, 0u};
    if (w == 4) qpk = fetch_q(rs, q_off, 1024, tA);
    BAR();
    if (w == 0) STAMP(10);
    if (!hlp) {
      STREAM_CONSTS();
      if (EARLYK) {
#pragma unroll
        for (int j = 0; j < 8; ++j) V0[j] = LDV(c0 + w, j);
      } else {
#pragma unroll
        for (int j = 0; j < 8; ++j) K0[j] = LDK(c0 + w, j);
      }
      SB();
    }
    ba_pass<false>(L, w, lane, qf, ac, m, l, [&](int at) {
      if (!hlp) {
        STREAM_CONSTS();
        if (at == 0 && !EARLYK) {
#pragma unroll
          for (int j = 0; j < 8; ++j) V0[j] = LDV(c0 + w, j);
        } else if (BA_PF >= 3 && at == (EARLYK == 1 ? 0 : 1)) {
#pragma unroll
          for (int j = 0; j < 8; ++j) K[j] = LDK(c0 + w + 4, j);
        }
        SB();
      }
    });
    if (w == 0) STAMP(29);
    l = x32sum(l);
    if (lane < 32) *(LAS float*)(L + L_WL + (w * 32 + lane) * 4) = l;
    BAR();
    {
      const u32 hd = lane & 31, hh = lane >> 5;
#pragma unroll
      for (int T = 0; T < 2; ++T)
#pragma unroll
        for (int rq = 0; rq < 4; ++rq)
          *(LAS v2u*)(L + hd * 1024 + (((8 * w + 4 * T + rq) ^ (hd & 15)) * 16) + 8 * hh) =
              v2u{pk2(ac[T][4 * rq], ac[T][4 * rq + 1]), pk2(ac[T][4 * rq + 2], ac[T][4 * rq + 3])};
    }
    if (w == 4) *(LAS v2u*)(L + L_QS + lane * 8) = qpk;
    BAR();
    if (hlp) {
#pragma unroll
      for (int i = 0; i < 8; ++i) {
        const u32 hd = 8 * hw + i;
        st_sc1(rs, WS_PART + (PLAY ? bi * 32 + hd : (hd * 4 + b) * 16 + sp) * 1024 + lane * 16, lld128(L, hd * 1024 + ((lane ^ (hd & 15)) * 16)));
      }
      if (w == 4 && lane < 32) {
        float ls = 0.f;
#pragma unroll
        for (int i = 0; i < 8; ++i) ls += *(LAS float*)(L + L_WL + (i * 32 + lane) * 4);
        st_sc1_8(rs, WS_ML + (MLAY ? bi * 32 + lane : (lane * 4 + b) * 16 + sp) * 8, v2u{asu(m), asu(ls)});
      }
      VMWAIT(0);
      if (lds_inc(L, SY_A) == 3 && lane == 0) st_sc1_8(rs, WS_FP + bi * FSTR, v2u{tA, tB});
      if (w == 4) STAMP(11);
    } else {
      STREAM_CONSTS();
      a_reset(acc);
      a_loadq(L, lane, QF);
      const u32 PP = a_soft(a_qk(K0, QF, t1), acc);
      a_pv(PP, V0, sel, acc);
      asm volatile("" : "+v"(acc.o)::"memory");
      if (BA_WAIT) lds_wait(L, SY_A, 4);
      if (BA_PF < 3) {
#pragma unroll
        for (int j = 0; j < 8; ++j) K[j] = LDK(c0 + w + 4, j);
      }
#pragma unroll
      for (int j = 0; j < 8; ++j) V[j] = LDV(c0 + w + 4, j);
      SB();
    }
  } else {
    if (w < 6) *(LAS v2u*)(L + L_QB + (2 * hw + (lane >> 5)) * 320 + (lane & 31) * 8) = fetch_q(rs, WS_E1 + h * 4096 + hw * 1024 + lane * 16, 2048, tA);
    VMWAIT(0);
    BAR();
    if (w == 4) STAMP(12);
    {
      f32x4 d[4];
#pragma unroll
      for (int T = 0; T < 4; ++T) d[T] = f32x4{0.f, 0.f, 0.f, 0.f};
#pragma unroll
      for (int ks = 0; ks < 4; ++ks) {
        const v4u qb = lld128(L, L_QB + (lane & 3) * 320 + ks * 64 + g4 * 16);
        const u32 a0 = (64 * hw + n16) * 256 + (((4 * ks + g4) ^ n16) * 16);
#pragma unroll
        for (int T = 0; T < 4; ++T) d[T] = mfma16(lld128(L, a0 + T * 4096), qb, d[T]);
      }
      const int T = n16 >> 2;
      const f32x4 e = sel4(T & 2, sel4(T & 1, d[3], d[2]), sel4(T & 1, d[1], d[0]));
      st_sc1_8(rs, WS_QABS + ((lane & 3) * 32 + h) * 1024 + (half * 256 + 64 * hw + 16 * T + 4 * g4) * 2, v2u{pk2(e[0], e[1]), pk2(e[2], e[3])});
    }
    VMWAIT(0);
    BAR();
    if (tid == 256) st_sc1_8(rs, WS_FQ + bw * FSTR, v2u{tA, tB});
    if (w == 4) STAMP(13);
    if (w == 4) *(LAS v2u*)(L + L_QS + lane * 8) = fetch_q(rs, q_off, 1024, tA);
    BAR();
    LOAD_WO(p.Bwo + (u32)(bw * 64 * 1152), 9, 2);
    v4u uf[16];
#pragma unroll
    for (int ks = 0; ks < 16; ++ks) uf[ks] = ltr2(L, L_UV + tr128 + ks * 4096, L_UV + tr128 + ks * 4096 + 512);
    if (w == 4) STAMP(24);
    poll_flags<FP_SLEEP>(rs, WS_FP + (16 * hw + n16) * FSTR, tA, tB);
    if (lane == 0) *(volatile LAS u32*)(L + SY_HOLD) = 1u;
    if (w == 4) STAMP(25);
    {
      const v2u ml = ld_sc1_8(rs, WS_ML + (MLAY ? (hw * 16 + n16) * 32 + h : (h * 4 + hw) * 16 + n16) * 8);
      v4u pt[16];
#pragma unroll
      for (int i = 0; i < 16; ++i) pt[i] = ld_sc1(rs, WS_PART + (PLAY ? (hw * 16 + i) * 32 + h : (h * 4 + hw) * 16 + i) * 1024 + lane * 16);
      float M = fmaxf(asf(ml.x), dppf<XOR1>(asf(ml.x)));
      M = fmaxf(M, dppf<XOR2>(M)); M = fmaxf(M, dppf<ROR4>(M)); M = fmaxf(M, dppf<ROR8>(M));
      const float wt = ex2(asf(ml.x) - M);
      float Ls = wt * asf(ml.y);
      Ls += dppf<XOR1>(Ls); Ls += dppf<XOR2>(Ls); Ls += dppf<XOR7>(Ls); Ls += dppf<XOR15>(Ls);
      const float wn = wt / Ls;
      float c[8];
#pragma unroll
      for (int k = 0; k < 8; ++k) c[k] = 0.f;
#pragma unroll
      for (int i = 0; i < 16; ++i) {
        const float wi = asf(__builtin_amdgcn_readlane(asu(wn), i));
#pragma unroll
        for (int k = 0; k < 4; ++k) { c[2 * k] += wi * asf(pt[i][k] << 16); c[2 * k + 1] += wi * asf(pt[i][k] & 0xffff0000u); }
      }
      lst128(L, L_CL + hw * 1088 + lane * 16, v4u{pk2(c[0], c[1]), pk2(c[2], c[3]), pk2(c[4], c[5]), pk2(c[6], c[7])});
    }
    lds_inc(L, SY_A);
    lds_wait(L, SY_A, 4);
    if (w == 4) STAMP(26);
    {
      f32x4 d = {0.f, 0.f, 0.f, 0.f};
#pragma unroll
      for (int ks = 0; ks < 16; ++ks) d = mfma16(lld128(L, L_CL + (lane & 3) * 1088 + ks * 64 + g4 * 16), uf[ks], d);
      const float dv = (g4 & 2) ? ((g4 & 1) ? d[3] : d[2]) : ((g4 & 1) ? d[1] : d[0]);
      *(LAS u16*)(L + L_OA + g4 * 320 + (16 * hw + n16) * 2) = (u16)pk2(dv, dv);
    }
    if (w == 4) STAMP(27);
  }

  if (!hlp) {
#define YIELD() do { if (chk && __builtin_amdgcn_readfirstlane(*(volatile LAS u32*)(L + SY_HOLD))) { lds_wait(L, SY_A, 4); chk = false; } } while (0)
    float S = a_qk(K, QF, t1);
    for (int id = c0 + w + 8; id < c1; id += 4) {
      YIELD();
      asm volatile("" : "+v"(S)::"memory");
#pragma unroll
      for (int j = 0; j < 8; ++j) K[j] = LDK(id, j);
      SB();
      const u32 PP = a_soft(S, acc);
      a_pv(PP, V, sel, acc);
      asm volatile("" : "+v"(acc.o)::"memory");
      YIELD();
#pragma unroll
      for (int j = 0; j < 8; ++j) V[j] = LDV(id, j);
      SB();
      if (id + 4 >= c1 && lane == 0) __hip_atomic_fetch_add((LAS u32*)(L + SY_LAST), 1u, __ATOMIC_RELAXED, __HIP_MEMORY_SCOPE_WORKGROUP);
      S = a_qk(K, QF, t1);
    }
    const u32 PP = a_soft(S, acc);
    float ls = acc.l + dppf<XOR1>(acc.l);
    ls += dppf<ROR4>(ls);
    ls += dppf<ROR8>(ls);
    ls = x32sum(x16sum(ls));
    u32 fa = L_FL + lane * 16, ma = L_FM + (lane & 2) * 4, sa = SY_B, one = 1;
    u32 eo = WS_E3 + (((g * 2 + pair) * 4 + prod) * 2 + ((lane >> 1) & 1)) * 512 + g4 * 128 + (((n16 >> 2) << 1) | (n16 & 1)) * 16;
    if ((lane & 61) == 0) *(LAS f32x2*)(L + ma + w * 16) = f32x2{acc.m, ls};
    asm volatile("" : "+v"(fa), "+v"(ma), "+v"(sa), "+v"(one), "+v"(eo)::"memory");
    a_pv(PP, V, sel, acc);
    *(LAS f32x4*)(L + fa + w * 1024) = acc.o;
    u32 old;
    u64 ex;
    asm volatile("s_mov_b64 %1, exec\n\ts_mov_b64 exec, 1\n\tds_add_rtn_u32 %0, %2, %3\n\ts_mov_b64 exec, %1" : "=&v"(old), "=&s"(ex) : "v"(sa), "v"(one) : "memory");
    f32x2 ml[4];
    f32x4 aw[4];
#pragma unroll
    for (int i = 0; i < 4; ++i) ml[i] = *(LAS f32x2*)(L + ma + i * 16);
#pragma unroll
    for (int i = 0; i < 4; ++i) aw[i] = *(LAS f32x4*)(L + fa + i * 1024);
    const float M = fmaxf(fmaxf(ml[0].x, ml[1].x), fmaxf(ml[2].x, ml[3].x));
    float wt[4], Ls = 0.f;
#pragma unroll
    for (int i = 0; i < 4; ++i) { wt[i] = ex2(ml[i].x - M); Ls += wt[i] * ml[i].y; }
    asm volatile("" : "+v"(old) : "v"(Ls));
    const bool last = __builtin_amdgcn_readfirstlane(old) == 3;
    if (last) STAMP(16);
    const f32x4 o = (wt[0] * aw[0] + wt[1] * aw[1] + wt[2] * aw[2] + wt[3] * aw[3]) * __builtin_amdgcn_rcpf(Ls);
    const u32 lse = asu(M + __builtin_amdgcn_logf(Ls));
    const u32 p01 = pk2(o[0], o[1]), p23 = pk2(o[2], o[3]);
    st_sc1(rs, last ? eo : OOB, v4u{__builtin_amdgcn_perm(p01, tA, 0x07060500u), __builtin_amdgcn_perm(p23, tA, 0x07060501u), __builtin_amdgcn_perm(lse, tA, 0x07060502u),
                                    __builtin_amdgcn_perm(p01, tA, 0x0c0c0403u) | __builtin_amdgcn_perm(lse, p23, 0x04000c0cu)});
    if (last) STAMP(15);
    if (isA) BAR();
  } else if (isA) {
    const int i16 = lane >> 2;
    u32 go = WS_E3 + (((kvh * 4 + hw) * 4 + (lane & 3)) * 2 + (ai >> 2)) * 512 + i16 * 16;
    u32 oa = L_OA + hw * 320 + (4 * (i16 & 8) + (i16 & 7) + 8 * (lane & 3)) * 2, ra = L_OA + (lane & 3) * 320 + g4 * 16;
    u32 eo = g4 < nu ? WS_E4 + ((16 * (ai & 3) + 8 * g4 + 2 * hw + (n16 >> 3)) * 32 + kvh * 2 + (ai >> 2)) * 128 + (n16 & 7) * 16 : OOB;
    asm volatile("" : "+v"(go), "+v"(oa), "+v"(ra), "+v"(eo));
#pragma unroll 1
    for (int pass = DRY ? 0 : 1; pass < 2; ++pass) {
      if (pass) {
        lds_wait(L, SY_LAST, LATE_A);
        LOAD_WO(p.Awo + (u32)((kvh * 256 + (ai >> 2) * 128) * 1152 + (ai & 3) * 256), nu, 4);
        VMWAIT(0);
        lds_inc(L, SY_WO);
        lds_wait(L, SY_WO, 4);
      }
      if (w == 4) STAMP(28);
      v4u wf[12];
#pragma unroll
      for (int i = 0; i < 12; ++i) wf[i] = ltr2(L, L_WO + tr128 + i * 4096, L_WO + tr128 + i * 4096 + 512);
      v4u v[2];
      poll16<2, true>(rs, pass ? go : OOB, 256, v, tA, tB, !pass);
      const float m = asf(__builtin_amdgcn_perm(v[0].z, v[0].w, 0x07060503u));
      float M = fmaxf(m, dppf<XOR1>(m));
      M = fmaxf(M, dppf<XOR2>(M));
      float wt = ex2(m - M), Ls = wt + dppf<XOR1>(wt);
      Ls += dppf<XOR2>(Ls);
      wt *= __builtin_amdgcn_rcpf(Ls);
      float y[4];
#pragma unroll
      for (int a = 0; a < 4; ++a) {
        const u32 x = a < 2 ? v[a].x : v[a - 2].y;
        const float lo = wt * asf(__builtin_amdgcn_perm(x, v[a & 1].w, a < 2 ? 0x05010c0cu : 0x05020c0cu)), hi = wt * asf(x & 0xffff0000u);
        y[a] = (t1 ? hi : lo) + dppf<XOR1>(t1 ? lo : hi);
      }
      const bool t2 = lane & 2;
      const float z0 = (t2 ? y[2] : y[0]) + dppf<XOR2>(t2 ? y[0] : y[2]), z1 = (t2 ? y[3] : y[1]) + dppf<XOR2>(t2 ? y[1] : y[3]);
      const u32 zz = pk2(z0, z1);
      *(LAS u16*)(L + oa) = (u16)zz;
      *(LAS u16*)(L + oa + 128) = (u16)(zz >> 16);
      if (pass) BAR();
      if (w == 4) STAMP(17);
      f32x4 e[3];
#pragma unroll
      for (int u = 0; u < 3; ++u) e[u] = f32x4{0.f, 0.f, 0.f, 0.f};
#pragma unroll
      for (int ks = 0; ks < 4; ++ks) {
        const v4u af = lld128(L, ra + ks * 64);
#pragma unroll
        for (int u = 0; u < 3; ++u) e[u] = mfma16(af, wf[4 * u + ks], e[u]);
      }
      st_sc1(rs, pass ? eo : OOB, tag4(sel4(g4 & 2, e[2], sel4(g4 & 1, e[1], e[0])), tA));
      if (w == 4) STAMP(18);
    }
  } else if (isBA) {
    const int oct = hw * 64 + bi;
    if (oct < 72) {
      reduce_oct<4>(rs, WS_E4 + oct * 4096 + lane * 16, tA, p.out + oct * 8, lane, [&] { lds_wait(L, SY_B, 4); });
      if (oct == 0 && lane == 0) __hip_atomic_store((GAS u64*)(p.ws + WS_EPOCH), ((u64)tB << 32) | tA, RLX_AGENT);
      STAMP(19);
    }
  } else {
    VMWAIT(0);
    lds_inc(L, SY_WO);
    lds_wait(L, SY_WO, 4);
    const v4u af0 = lld128(L, L_OA + (lane & 3) * 320 + g4 * 16), af1 = lld128(L, L_OA + (lane & 3) * 320 + 64 + g4 * 16);
    f32x4 e[9];
#pragma unroll
    for (int u = 0; u < 9; ++u) {
      const u32 t0 = L_WO + tr128 + u * 8192;
      e[u] = mfma16(af1, ltr2(L, t0 + 4096, t0 + 4608), mfma16(af0, ltr2(L, t0, t0 + 512), f32x4{0.f, 0.f, 0.f, 0.f}));
    }
    const u32 eo = WS_E6 + ((8 * g4 + 2 * hw + (n16 >> 3)) * 64 + bw) * 128 + (n16 & 7) * 16;
#pragma unroll
    for (int k = 0; k < 2; ++k)
      st_sc1(rs, eo + k * 32 * 8192, tag4(sel4(g4 & 2, sel4(g4 & 1, e[4 * k + 3], e[4 * k + 2]), sel4(g4 & 1, e[4 * k + 1], e[4 * k])), tA));
    st_sc1(rs, g4 == 0 ? eo + 64 * 8192 : OOB, tag4(e[8], tA));
    if (w == 4) STAMP(18);
    const int oct = hw * 64 + bw;
    if (oct < 72) {
      reduce_oct<8>(rs, WS_E6 + oct * 8192 + lane * 16, tA, p.out + 576 + oct * 8, lane, [] {});
      STAMP(14);
    }
  }
  asm volatile("" ::"v"(pf));
}

extern "C" void kernel_launch(void* const* d_in, const int* in_sizes, int n_in, void* d_out, int out_size, void* d_ws, size_t ws_size,
                              hipStream_t stream) {
  Params p;
  p.Ax = (const char*)d_in[0]; p.AK = (const char*)d_in[1]; p.AV = (const char*)d_in[2]; p.Awq = (const char*)d_in[3]; p.Awo = (const char*)d_in[4];
  p.Bx = (const char*)d_in[5]; p.BC = (const char*)d_in[6]; p.Bwq = (const char*)d_in[7]; p.Bwuk = (const char*)d_in[8]; p.Bwuv = (const char*)d_in[9];
  p.Bwo = (const char*)d_in[10];
  p.out = (u16*)d_out;
  p.ws = (char*)d_ws;
  (void)hipFuncSetAttribute((const void*)decode_mega, hipFuncAttributeMaxDynamicSharedMemorySize, LDS_BYTES);
  decode_mega<<<256, 512, LDS_BYTES, stream>>>(p);
}
```

```cpp
#include <hip/hip_runtime.h>
#include <stdint.h>

typedef unsigned int u32;
typedef unsigned long long u64;
typedef unsigned short u16;
typedef u32 v4u __attribute__((ext_vector_type(4)));
typedef u32 v2u __attribute__((ext_vector_type(2)));
typedef u32 v8u __attribute__((ext_vector_type(8)));
typedef u32 v16u __attribute__((ext_vector_type(16)));
typedef float f32x2 __attribute__((ext_vector_type(2)));
typedef float f32x4 __attribute__((ext_vector_type(4)));
typedef float f32x16 __attribute__((ext_vector_type(16)));
typedef __bf16 bf16x8 __attribute__((ext_vector_type(8)));
typedef __bf16 bf16x2 __attribute__((ext_vector_type(2)));
typedef short s16x4 __attribute__((ext_vector_type(4)));
#define DI __device__ __forceinline__
#define LAS __attribute__((address_space(3)))
#define GAS __attribute__((address_space(1)))
#define RLX_AGENT __ATOMIC_RELAXED, __HIP_MEMORY_SCOPE_AGENT

#define STAMPS 0
#define CODE_BYTES 0
#define C2_EARLY 16
#define DYNP 0
#define BA_PF 2
#define BA_WAIT 0
#define LATE_A 4
#define BRED_LATE 0
#define FSTR 128
#define FQ_SLEEP 2
#define FP_SLEEP 2
#define PLAY 1
#define MLAY 1
#define DRY 1
#define TOPPRIO 0
#define UVSPLIT 0
#define LATEBAR 1
#define EARLYK 0
#define DPB 0
#define C_A 40
#define C_BA 20
#define DPAR 0
#define PSLEEP 1
#define PACE 8

constexpr int WS_EPOCH = 0;
constexpr int WS_FQ = 32768;
constexpr int WS_FP = 49152;
constexpr int WS_ML = 16384;
constexpr int WS_QABS = 65536;
constexpr int WS_E1 = 262144;
constexpr int WS_E2 = 393216;
constexpr int WS_E3 = 524288;
constexpr int WS_E6 = 1 << 20;
constexpr int WS_E4 = WS_E6 + 72 * 8192;
constexpr int WS_PART = 2 << 20;
constexpr int WS_BYTES = 4 << 20;
constexpr u32 OOB = 0x7f000000u;
constexpr int LDS_BYTES = 163840;
constexpr int L_QBW = 0, L_QAW = 36864;
constexpr int L_XV = 110592;
constexpr int L_WO = 0;
constexpr int L_UV = 73728;
constexpr int L_QS = 139264;
constexpr int L_FL = L_QS + 1024;
constexpr int L_FM = L_FL + 4096;
constexpr int L_OA = L_FM + 128;
constexpr int L_X = 131072;
constexpr int L_SX = L_X, L_P = L_X + 16384, L_WM = L_X + 24576, L_WL = L_X + 25600;
constexpr int L_SYNC = L_X + 26624;
constexpr int L_QB = L_OA + 1280;
constexpr int L_CL = L_QB + 1280;
static_assert(L_CL + 4352 <= L_WM, "LDS map");
enum { SY_A = L_SYNC, SY_B = L_SYNC + 4, SY_LAST = L_SYNC + 8, SY_QB = L_SYNC + 12, SY_QABS = L_SYNC + 16, SY_HOLD = L_SYNC + 20, SY_WO = L_SYNC + 24 };

constexpr float SCALE = 0.08838834764831845f;
constexpr float LOG2E = 1.4426950408889634f;
constexpr u32 SPIN_CAP = 1u << 18;

struct Params {
  const char *Ax, *AK, *AV, *Awq, *Awo, *Bx, *BC, *Bwq, *Bwuk, *Bwuv, *Bwo;
  u16* out;
  char* ws;
};

DI f32x4 mfma16(v4u a, v4u b, f32x4 c) {
  return __builtin_amdgcn_mfma_f32_16x16x32_bf16(__builtin_bit_cast(bf16x8, a), __builtin_bit_cast(bf16x8, b), c, 0, 0, 0);
}
DI f32x16 mfma32(v4u a, v4u b, f32x16 c) {
  return __builtin_amdgcn_mfma_f32_32x32x16_bf16(__builtin_bit_cast(bf16x8, a), __builtin_bit_cast(bf16x8, b), c, 0, 0, 0);
}
DI u32 pk2(float a, float b) { f32x2 v = {a, b}; return __builtin_bit_cast(u32, __builtin_convertvector(v, bf16x2)); }
DI float rbf(float a) { return (float)(__bf16)a; }
DI float asf(u32 x) { return __builtin_bit_cast(float, x); }
DI u32 asu(float x) { return __builtin_bit_cast(u32, x); }
DI float ex2(float x) { return __builtin_amdgcn_exp2f(x); }
template <int CTRL> DI float dppf(float x) { return asf(__builtin_amdgcn_mov_dpp(asu(x), CTRL, 0xf, 0xf, true)); }
template <int CTRL> DI u32 dppu(u32 x) { return __builtin_amdgcn_mov_dpp(x, CTRL, 0xf, 0xf, true); }
constexpr int XOR1 = 0xB1, XOR2 = 0x4E, XOR7 = 0x141, XOR15 = 0x140, ROR8 = 0x128, ROR4 = 0x124;
DI float x16max(float x) { auto s = __builtin_amdgcn_permlane16_swap(asu(x), asu(x), false, false); return fmaxf(asf(s[0]), asf(s[1])); }
DI float x32max(float x) { auto s = __builtin_amdgcn_permlane32_swap(asu(x), asu(x), false, false); return fmaxf(asf(s[0]), asf(s[1])); }
DI float x16sum(float x) { auto s = __builtin_amdgcn_permlane16_swap(asu(x), asu(x), false, false); return asf(s[0]) + asf(s[1]); }
DI float x32sum(float x) { auto s = __builtin_amdgcn_permlane32_swap(asu(x), asu(x), false, false); return asf(s[0]) + asf(s[1]); }
DI float sum_hi3(f32x4 a) {
  auto s0 = __builtin_amdgcn_permlane32_swap(asu(a[0]), asu(a[1]), false, false);
  auto s1 = __builtin_amdgcn_permlane32_swap(asu(a[2]), asu(a[3]), false, false);
  auto t = __builtin_amdgcn_permlane16_swap(asu(asf(s0[0]) + asf(s0[1])), asu(asf(s1[0]) + asf(s1[1])), false, false);
  const float c = asf(t[0]) + asf(t[1]);
  return c + dppf<ROR8>(c);
}
DI f32x4 sel4(bool c, f32x4 a, f32x4 b) { return f32x4{c ? a[0] : b[0], c ? a[1] : b[1], c ? a[2] : b[2], c ? a[3] : b[3]}; }

#define BAR() asm volatile("s_waitcnt lgkmcnt(0)\n\ts_barrier" ::: "memory")
#define VMWAIT(n) asm volatile("s_waitcnt vmcnt(%0)" ::"n"(n) : "memory")
#define SB() __builtin_amdgcn_sched_barrier(0)

DI v4u lld128(LAS char* L, u32 o) { return *(LAS v4u*)(L + o); }
DI void lst128(LAS char* L, u32 o, v4u v) { *(LAS v4u*)(L + o) = v; }
DI v2u ltr(LAS char* L, u32 o) { return __builtin_bit_cast(v2u, __builtin_amdgcn_ds_read_tr16_b64_v4i16((LAS s16x4*)(L + o))); }
DI v4u ltr2(LAS char* L, u32 o0, u32 o1) { v2u a = ltr(L, o0), b = ltr(L, o1); return v4u{a.x, a.y, b.x, b.y}; }
DI void dma16(const char* sbase, u32 voff, u32 lds) {
  u32 keep;
  asm volatile("s_mov_b32 %0, m0\n\ts_mov_b32 m0, %3\n\ts_nop 2\n\tglobal_load_lds_dwordx4 %1, %2 nt\n\ts_mov_b32 m0, %0"
               : "=&s"(keep) : "v"(voff), "s"(sbase), "s"(lds) : "memory");
}
DI void dma16c(const char* sbase, u32 voff, u32 lds) {
  u32 keep;
  asm volatile("s_mov_b32 %0, m0\n\ts_mov_b32 m0, %3\n\ts_nop 2\n\tglobal_load_lds_dwordx4 %1, %2\n\ts_mov_b32 m0, %0"
               : "=&s"(keep) : "v"(voff), "s"(sbase), "s"(lds) : "memory");
}
DI v4u ldnt(const char* base, u32 voff, int imm) { return __builtin_nontemporal_load((const GAS v4u*)((const GAS char*)base + voff + imm)); }
typedef __amdgpu_buffer_rsrc_t rsrc_t;
DI v4u ld_sc1(rsrc_t rs, u32 o) { return __builtin_amdgcn_raw_buffer_load_b128(rs, o, 0, 16); }
DI v2u ld_sc1_8(rsrc_t rs, u32 o) { return __builtin_amdgcn_raw_buffer_load_b64(rs, o, 0, 16); }
DI void st_sc1(rsrc_t rs, u32 o, v4u v) { __builtin_amdgcn_raw_buffer_store_b128(v, rs, o, 0, 16); }
DI void st_sc1_8(rsrc_t rs, u32 o, v2u v) { __builtin_amdgcn_raw_buffer_store_b64(v, rs, o, 0, 16); }
DI v4u tag4(f32x4 e, u32 tA) {
  v4u v;
#pragma unroll
  for (int k = 0; k < 4; ++k) v[k] = __builtin_amdgcn_perm(asu(e[k]) + 0x80u, tA, 0x07060500u + k);
  return v;
}
DI f32x4 untag4(v4u v) { return f32x4{asf(v.x & ~0xffu), asf(v.y & ~0xffu), asf(v.z & ~0xffu), asf(v.w & ~0xffu)}; }
template <int N, bool WORDS> DI void poll16(rsrc_t rs, u32 o, u32 stride, v4u (&v)[N], u32 tA, u32 tB, bool once = false) {
  for (u32 spins = once ? ~0u : 0u;; ++spins) {
    asm volatile("" ::: "memory");
    bool ok = true;
#pragma unroll
    for (int i = 0; i < N; ++i) v[i] = ld_sc1(rs, o + i * stride);
#pragma unroll
    for (int i = 0; i < N; ++i) {
      if (WORDS) ok = ok && (__builtin_amdgcn_perm(v[i].y, v[i].x, 0x0c0c0400u) | __builtin_amdgcn_perm(v[i].w, v[i].z, 0x04000c0cu)) == tA;
      else ok = ok && v[i].y == tA && v[i].w == tB;
    }
    if (__all(ok) || spins > SPIN_CAP) break;
    if (PSLEEP) __builtin_amdgcn_s_sleep(PSLEEP);
  }
}
template <int SLEEP> DI void poll_flags(rsrc_t rs, u32 o, u32 tA, u32 tB) {
  for (u32 spins = 0;; ++spins) {
    asm volatile("" ::: "memory");
    const v2u v = ld_sc1_8(rs, o);
    if (__all(v.x == tA && v.y == tB) || spins > SPIN_CAP) break;
    __builtin_amdgcn_s_sleep(SLEEP);
  }
  __builtin_amdgcn_fence(__ATOMIC_ACQUIRE, "wavefront");
}
DI u32 lds_inc(LAS char* L, u32 o) {
  asm volatile("s_waitcnt lgkmcnt(0)" ::: "memory");
  u32 old = 0;
  if ((threadIdx.x & 63) == 0) old = __hip_atomic_fetch_add((LAS u32*)(L + o), 1u, __ATOMIC_RELAXED, __HIP_MEMORY_SCOPE_WORKGROUP);
  return __builtin_amdgcn_readfirstlane(old);
}
DI void lds_wait(LAS char* L, u32 o, u32 want) {
#pragma unroll 1
  for (u32 spins = 0; spins < (1u << 24); ++spins) {
    if (__builtin_amdgcn_readfirstlane(__hip_atomic_load((LAS u32*)(L + o), __ATOMIC_RELAXED, __HIP_MEMORY_SCOPE_WORKGROUP)) >= want) break;
    __builtin_amdgcn_s_sleep(1);
  }
  asm volatile("" ::: "memory");
}

#define STAMP(i) do {} while (0)

DI f32x4 gemv288(LAS char* L, u32 base, u32 xo) {
  f32x4 d = {0.f, 0.f, 0.f, 0.f};
#pragma unroll
  for (int ks = 0; ks < 9; ++ks) d = mfma16(ltr2(L, base + ks * 4096, base + ks * 4096 + 512), lld128(L, xo + ks * 64), d);
  return d;
}
DI v2u fetch_q(rsrc_t rs, u32 o, u32 stride, u32 tA) {
  v4u v[2];
  poll16<2, true>(rs, o, stride, v, tA, 0u);
  const f32x4 q = untag4(v[0]) + untag4(v[1]);
  return v2u{pk2(q[0], q[1]), pk2(q[2], q[3])};
}

struct Acc { float m, l; f32x4 o; };
DI float a_qk(const v4u (&K)[8], const v4u (&QF)[8], bool t1) {
  f32x4 d = {0.f, 0.f, 0.f, 0.f};
#pragma unroll
  for (int j = 0; j < 8; ++j) d = mfma16(K[j], QF[j], d);
  const float keep = t1 ? d[3] : d[0], send = t1 ? d[1] : d[2];
  return keep + dppf<XOR1>(send);
}
DI u32 a_soft(float S, Acc& a) {
  const float t = rbf(rbf(S) * SCALE) * LOG2E;
  float cm = fmaxf(t, dppf<XOR1>(t));
  cm = fmaxf(cm, dppf<ROR4>(cm));
  cm = fmaxf(cm, dppf<ROR8>(cm));
  cm = x32max(x16max(cm));
  const float mn = fmaxf(a.m, cm);
  const float al = ex2(a.m - mn), p = ex2(t - mn);
  a.l = a.l * al + p;
  a.m = mn;
  a.o *= al;
  return pk2(p, dppf<XOR2>(p));
}
template <int J> DI void a_pv1(u32 PP, v4u V, const u32 (&sel)[4], Acc& a) {
  const u32 X = dppu<0x150 + 4 * (J >> 1) + (J & 1)>(PP);
  const v4u B = {__builtin_amdgcn_perm(X, X, sel[0]), __builtin_amdgcn_perm(X, X, sel[1]), __builtin_amdgcn_perm(X, X, sel[2]), __builtin_amdgcn_perm(X, X, sel[3])};
  a.o = mfma16(V, B, a.o);
}
DI void a_pv(u32 PP, const v4u (&V)[8], const u32 (&sel)[4], Acc& a) {
  a_pv1<0>(PP, V[0], sel, a); a_pv1<1>(PP, V[1], sel, a); a_pv1<2>(PP, V[2], sel, a); a_pv1<3>(PP, V[3], sel, a);
  a_pv1<4>(PP, V[4], sel, a); a_pv1<5>(PP, V[5], sel, a); a_pv1<6>(PP, V[6], sel, a); a_pv1<7>(PP, V[7], sel, a);
}
DI void a_loadq(LAS char* L, int lane, v4u (&QF)[8]) {
  const int n = lane & 15, g = lane >> 4;
  const u32 o = L_QS + ((n >> 1) & 1) * 256 + (2 * g + (n & 1)) * 16;
  const v4u F0 = lld128(L, o), F1 = lld128(L, o + 128), Z = {0u, 0u, 0u, 0u};
#pragma unroll
  for (int u = 0; u < 4; ++u) { const bool on = (n >> 2) == u; QF[2 * u] = on ? F0 : Z; QF[2 * u + 1] = on ? F1 : Z; }
}
DI void a_reset(Acc& a) { a.m = -INFINITY; a.l = 0.f; a.o = f32x4{0.f, 0.f, 0.f, 0.f}; }
DI u32 sw2(u32 row) { return (row & 2) | ((row >> 1) & 4); }
DI u32 fsw(u32 row) { return ((row & 3) << 2) | ((row >> 2) & 3); }
template <int N, class F> DI void reduce_oct(rsrc_t rs, u32 o, u32 tA, u16* out_, int lane, F&& gate) {
  GAS u16* out = (GAS u16*)out_ + ((lane >> 5) + ((lane >> 3) & 2)) * 1152 + (lane & 7);
  asm volatile("" : "+v"(out), "+v"(o));
#pragma unroll 1
  for (int pass = DRY ? 0 : 1; pass < 2; ++pass) {
    if (pass) gate();
    v4u v[N];
    poll16<N, true>(rs, pass ? o : OOB, 1024, v, tA, 0u, !pass);
    f32x4 a = untag4(v[0]);
#pragma unroll
    for (int i = 1; i < N; ++i) a += untag4(v[i]);
    const float c = sum_hi3(a);
    if (!(lane & 8) && pass) *out = (u16)pk2(c, c);
  }
}

template <bool FIRST, class F>
DI void ba_pass(LAS char* L, int w, int lane, const v4u (&qf)[16], f32x16 (&acc)[2], float& m, float& l, F&& hook) {
  asm volatile("" : "+v"(lane));
  const int kb = w & 3, ch = w >> 2, hh = lane >> 5, r31 = lane & 31;
  f32x16 S;
#pragma unroll
  for (int i = 0; i < 16; ++i) S[i] = 0.f;
  const u32 rowb = (32 * kb + r31) * 1024 + ch * 512, f = fsw(r31);
#pragma unroll
  for (int ks = 0; ks < 16; ++ks) S = mfma32(lld128(L, rowb + (ks >> 3) * 256 + (((2 * (ks & 7) + hh) ^ f) * 16)), qf[ks], S);
  float s[8];
  {
    v4u a, b;
#pragma unroll
    for (int j = 0; j < 4; ++j) { a[j] = asu(ch ? S[j] : S[8 + j]); b[j] = asu(ch ? S[4 + j] : S[12 + j]); }
    if (FIRST && LATEBAR) { asm volatile("" : "+v"(a), "+v"(b)); BAR(); }
    lst128(L, L_SX + w * 2048 + lane * 16, a);
    lst128(L, L_SX + w * 2048 + 1024 + lane * 16, b);
    asm volatile("" ::: "memory");
    hook(0);
    BAR();
    a = lld128(L, L_SX + (w ^ 4) * 2048 + lane * 16);
    b = lld128(L, L_SX + (w ^ 4) * 2048 + 1024 + lane * 16);
#pragma unroll
    for (int j = 0; j < 4; ++j) {
      s[j] = ((ch ? S[8 + j] : S[j]) + asf(a[j])) * (SCALE * LOG2E);
      s[4 + j] = ((ch ? S[12 + j] : S[4 + j]) + asf(b[j])) * (SCALE * LOG2E);
    }
  }
  float cm = fmaxf(fmaxf(fmaxf(s[0], s[1]), fmaxf(s[2], s[3])), fmaxf(fmaxf(s[4], s[5]), fmaxf(s[6], s[7])));
  cm = x32max(cm);
  if (lane < 32) *(LAS float*)(L + L_WM + (w * 32 + lane) * 4) = cm;
  BAR();
  float mn = FIRST ? -INFINITY : m;
#pragma unroll
  for (int i = 0; i < 8; ++i) mn = fmaxf(mn, *(LAS float*)(L + L_WM + (i * 32 + r31) * 4));
  float ps = 0.f;
#pragma unroll
  for (int j = 0; j < 8; ++j) { s[j] = ex2(s[j] - mn); ps += s[j]; }
  lst128(L, L_P + (2 * kb + ch) * 1024 + lane * 16, v4u{pk2(s[0], s[1]), pk2(s[2], s[3]), pk2(s[4], s[5]), pk2(s[6], s[7])});
  if (FIRST) {
    l = ps;
#pragma unroll
    for (int i = 0; i < 16; ++i) { acc[0][i] = 0.f; acc[1][i] = 0.f; }
  } else {
    const float al = ex2(m - mn);
    l = l * al + ps;
    acc[0] *= al;
    acc[1] *= al;
  }
  m = mn;
  asm volatile("" ::: "memory");
  hook(1);
  BAR();
  const int q = (lane & 15) >> 2, pp = lane & 3, blk = (lane >> 4) & 1;
  u32 ta[2][2];
#pragma unroll
  for (int T = 0; T < 2; ++T)
#pragma unroll
    for (int t = 0; t < 2; ++t) {
      const u32 c = 8 * w + 4 * T + 2 * blk + (pp >> 1);
      ta[T][t] = (8 * t + 4 * hh + q) * 1024 + ((c ^ ((q << 2) | (2 * t + hh))) * 16) + 8 * (pp & 1);
    }
#pragma unroll
  for (int ks = 0; ks < 8; ++ks) {
    const v4u P = lld128(L, L_P + ks * 1024 + lane * 16);
#pragma unroll
    for (int T = 0; T < 2; ++T) acc[T] = mfma32(ltr2(L, ta[T][0] + ks * 16384, ta[T][1] + ks * 16384), P, acc[T]);
  }
  asm volatile("" : "+v"(acc[0]), "+v"(acc[1]));
}

__global__ void __launch_bounds__(512, 2) decode_mega(Params args) {
  extern __shared__ __attribute__((aligned(16))) char smem[];
  LAS char* L = (LAS char*)smem;
  v16u ka;
  v8u kb;
  v2u kc;
  asm volatile("s_load_dwordx16 %0, %3, 0x0\n\ts_load_dwordx8 %1, %3, 0x40\n\ts_load_dwordx2 %2, %3, 0x60"
               : "=&s"(ka), "=&s"(kb), "=&s"(kc) : "s"(__builtin_amdgcn_kernarg_segment_ptr()) : "memory");
  SB();
  const int tid = threadIdx.x, lane = tid & 63;
  int w = __builtin_amdgcn_readfirstlane(tid >> 6), wg = blockIdx.x;
  u32 pf = 0;
  u32 vo, ld, step, lstep;
  const char* src0;
  {
    const int g = wg >> 3, r = (wg + g) & 7, hw = w & 3;
    const u32 koff = r < 4 ? (w < 4 ? 56 : (w & 1) ? 0 : 40) : r < 6 ? 48 : w >= 4 ? 64 : 72;
    asm volatile("s_load_dwordx2 %0, %1, %2" : "=s"(src0) : "s"(__builtin_amdgcn_kernarg_segment_ptr()), "s"(koff) : "memory");
    if (r < 4 && w >= 4) {
      u32 keep, xv = lane * 16 + (w & 2) * 448;
      asm volatile("s_setprio 3\n\ts_mov_b32 %0, m0\n\ts_mov_b32 m0, %2\n\ts_waitcnt lgkmcnt(0)\n\tglobal_load_lds_dwordx4 %1, %3\n\t"
                   "global_load_lds_dwordx4 %1, %3 offset:1792\n\tglobal_load_lds_dwordx4 %1, %3 offset:2688\n\ts_mov_b32 m0, %0"
                   : "=&s"(keep) : "v"(xv), "s"(L_XV + (w & 1) * 4608 + (w & 2) * 448), "s"(src0) : "memory");
    }
    const u32 hoff = ((g & ~7) | (wg & 7)) * 256;
    const bool cA = r < 4, cB = (r & 6) == 4, cK = r >= 6 && w >= 4;
    step = cB ? 16384 : cK ? 16 * 8192 : 32 * 8192;
    lstep = cB ? 16384 : 4096;
    ld = cB ? w * 1024 : hw * 1024 + (cA || cK ? 0 : L_UV);
    const u32 sA = (r >> 1) * (288 * 8192) + g * 256, sB = ((g * 2 + (r & 1)) * 256 + w) * 1024, sK = ((r & 1) * 256 + 4 * hw) * 8192 + hoff;
    const u32 so = cB ? sB : cK ? sK : (cA ? sA : hoff) + 8 * 8192 * w + (r & 1) * 128;
    const u32 sh = cB ? 6 : cK ? 4 : 3, row = (u32)lane >> sh;
    const u32 xs = cB ? fsw(w) : cK ? 4 * hw : 4 * (w & 1), xm = cK ? 3 : 2;
    vo = row * 8192 + ((((u32)lane - (row << sh)) ^ xs ^ (row & xm)) * 16) + so;
    if (TOPPRIO && w >= 4) __builtin_amdgcn_s_setprio(3);
    if (r >= 4 || w < 4) {
      u32 keep;
#define PIECE "global_load_lds_dwordx4 %1, %3 nt\n\ts_add_u32 m0, m0, %5\n\tv_add_u32 %1, %4, %1\n\t"
      asm volatile("s_mov_b32 %0, m0\n\ts_mov_b32 m0, %2\n\ts_waitcnt lgkmcnt(0)\n\t" PIECE PIECE PIECE PIECE PIECE PIECE PIECE PIECE "s_mov_b32 m0, %0"
                   : "=&s"(keep), "+v"(vo) : "s"(ld), "s"(src0), "s"(step), "s"(lstep) : "memory", "scc");
      ld += 8 * lstep;
    }
  }
  asm volatile("s_waitcnt lgkmcnt(0)" : "+s"(ka), "+s"(kb), "+s"(kc), "+s"(src0), "+s"(w), "+s"(wg)::"memory");
  if (w == 0) STAMP(20);
  Params p;
#define PTR(v, i) ((char*)(((u64)v[(i) + 1] << 32) | v[(i)]))
  p.Ax = PTR(ka, 0); p.AK = PTR(ka, 2); p.AV = PTR(ka, 4); p.Awq = PTR(ka, 6); p.Awo = PTR(ka, 8); p.Bx = PTR(ka, 10); p.BC = PTR(ka, 12); p.Bwq = PTR(ka, 14);
  p.Bwuk = PTR(kb, 0); p.Bwuv = PTR(kb, 2); p.Bwo = PTR(kb, 4); p.out = (u16*)PTR(kb, 6); p.ws = PTR(kc, 0);
  const int hw = w & 3, g = wg >> 3, s = wg & 7, r = (s + g) & 7, kvh = g >> 1;
  const bool isA = r < 4, isBA = r == 4 || r == 5, hlp = w >= 4;
  u64 epoch;
  asm volatile("s_load_dwordx2 %0, %1, 0x0" : "=s"(epoch) : "s"(p.ws) : "memory");
  if (hlp) __builtin_amdgcn_s_setprio(3);
  if (tid < 8) *(LAS u32*)(L + L_SYNC + tid * 4) = 0u;
  const int n16 = lane & 15, g4 = lane >> 4;
  const int ai = (g & 1) * 4 + r;
  const int bi = g * 2 + (r - 4);
  const int h = (g >> 3) * 8 + s, half = r - 6, bw = h * 2 + half;
  u32 vuv;
  {
    int n = isA ? (hlp ? 0 : 1) : isBA ? 0 : (hlp || !UVSPLIT) ? 8 : 0;
    vuv = vo;
#pragma unroll 1
    for (int bt = 0; bt < 2; ++bt) {
#pragma unroll 1
      for (; n > 0; --n) { VMWAIT(8); dma16(src0, vo, ld); vo += step; ld += lstep; }
      src0 = isA ? p.Awq : src0;
      ld = isA ? L_QAW + w * 1024 : (8 + w) * 1024;
      n = isA ? (hlp ? 0 : 9) : isBA ? 8 : 0;
      vo = (vo ^ (isA ? 0 : 32)) + (isA ? (u32)(((ai >> 2) - (r >> 1) - 1) * (288 * 8192) + kvh * 512 - g * 256 + (r & 2) * 128) : (u32)(-120 * 1024));
    }
    if (w == 0) STAMP(21);
    asm volatile("s_waitcnt lgkmcnt(0)" : "+s"(epoch)::"memory");
  }
  const rsrc_t rs = __builtin_amdgcn_make_buffer_rsrc((void*)p.ws, 0, WS_BYTES, 0x00020000);
  const u32 trrow = 8 * g4 + (n16 >> 2);
  const u32 tr128 = trrow * 128 + ((2 * hw + ((lane & 3) >> 1)) ^ sw2(trrow)) * 16 + 8 * (lane & 1);
  const u32 tA = (((u32)epoch & 0x7f7f7f7fu) + 0x01010101u) ^ ((u32)epoch & 0x80808080u), tB = (u32)(epoch >> 32) + 1u;

  u32 voffK, voffV, sel[4];
#define STREAM_CONSTS() do { u32 l_; asm volatile("v_mbcnt_lo_u32_b32 %0, -1, 0\n\tv_mbcnt_hi_u32_b32 %0, -1, %0" : "=v"(l_)); \
    const u32 n_ = l_ & 15, g_ = l_ >> 4; \
    voffK = (n_ >> 1) * 256 + (2 * g_ + (n_ & 1)) * 16; voffV = g_ * 512 + n_ * 16; \
    const u32 h2 = (n_ >> 1) & 1, by = (2 * h2) | ((2 * h2 + 1) << 8); \
    const u32 on = (n_ & 1) ? ((by << 16) | 0x0c0cu) : (0x0c0c0000u | by); \
    _Pragma("unroll") for (int i = 0; i < 4; ++i) sel[i] = (n_ >> 2) == i ? on : 0x0c0c0c0cu; } while (0)
  const int pair = isA ? r >> 1 : isBA ? r & 1 : (r & 1) ^ 1, prod = isA ? r & 1 : isBA ? 2 : 3;
  const int dp = (g & 1) ? -DPAR : DPAR, db = (g & 1) ? -DPB : DPB;
  const int c0 = prod == 0 ? 0 : prod == 1 ? C_A + dp : prod == 2 ? 2 * C_A : 2 * C_A + C_BA + (pair ? -db : db);
  const int c1 = prod == 0 ? C_A + dp : prod == 1 ? 2 * C_A : prod == 2 ? 2 * C_A + C_BA + (pair ? -db : db) : 128;
  const size_t kv0 = ((size_t)((g & 1) * 2 + pair) * 16 + kvh) * (4096 * 256) + 4096;
  const u32 q_off = WS_E2 + (g * 2 + pair) * 2048 + lane * 16;
#define LDK(id, j) ldnt(p.AK + kv0 + (size_t)(id) * 8192, voffK, ((j) >> 1) * 2048 + ((j) & 1) * 128 - 4096)
#define LDV(id, j) ldnt(p.AV + kv0 + (size_t)(id) * 8192, voffV, ((j) >> 1) * 2048 + ((j) & 1) * 256 - 4096)
  const bool t1 = lane & 1;
  const int nu = (ai & 3) == 3 ? 3 : 2;
#define LOAD_WO(SRC, NU, NI) do { \
    const u32 vx = (lane >> 3) * 1152 + (((lane & 7) ^ sw2((lane >> 3) | ((w & 1) << 3))) * 16); \
    u32 lx = L_WO + hw * 1024; \
    _Pragma("unroll 1") for (int u = 0; u < (NU); ++u) \
      _Pragma("unroll") for (int i = 0; i < (NI); ++i) { dma16((SRC) + (u32)(u * 128 + (32 * i + 8 * hw) * 1152), vx, lx); lx += 4096; } } while (0)

  v4u K[8], V[8], QF[8];
  Acc acc;
  bool chk = false;

  if (!isBA && !hlp) {
    STREAM_CONSTS();
    BAR();
    if (!isA) {
      if (UVSPLIT == 2) BAR();
      if (UVSPLIT) {
#pragma unroll 1
        for (int i = 8; i < 16; ++i) { dma16(p.Bwuv, vuv, L_UV + w * 1024 + i * 4096); vuv += 32 * 8192; }
      }
      if (UVSPLIT != 2) BAR();
    }
    v4u K0[8], V0[8];
#pragma unroll
    for (int j = 0; j < 8; ++j) { VMWAIT(8); K0[j] = LDK(c0 + w, j); SB(); }
    VMWAIT(8);
    if (isA) BAR();
#pragma unroll
    for (int j = 0; j < 8; ++j) { VMWAIT(PACE); V0[j] = LDV(c0 + w, j); SB(); }
#pragma unroll
    for (int j = 0; j < 8; ++j) { VMWAIT(PACE); K[j] = LDK(c0 + w + 4, j); SB(); }
#pragma unroll
    for (int j = 0; j < 8; ++j) { VMWAIT(PACE); V[j] = LDV(c0 + w + 4, j); SB(); }
    a_reset(acc);
    if (w == 0) STAMP(1);
    BAR();
    if (w == 0) STAMP(2);
    a_loadq(L, lane, QF);
    const u32 PP = a_soft(a_qk(K0, QF, t1), acc);
    a_pv(PP, V0, sel, acc);
    chk = DYNP && !isA;
  } else if (isA) {
    const u32 xo = L_XV + (lane & 3) * 1152 + g4 * 16;
    VMWAIT(0);
    if (w == 4) STAMP(23);
    BAR();
    if (w == 4) STAMP(3);
    f32x4 d = gemv288(L, L_QBW + tr128, xo + (r >> 1) * 576);
    st_sc1(rs, n16 < 4 ? WS_E1 + ((g * 2 + (r >> 1)) * 4 + n16) * 512 + (r & 1) * 256 + hw * 64 + g4 * 16 : OOB, tag4(d, tA));
    if (w == 4) STAMP(4);
    BAR();
    d = gemv288(L, L_QAW + tr128, xo + 4608 + (ai >> 2) * 576);
    st_sc1(rs, n16 < 4 ? WS_E2 + ((kvh * 4 + n16) * 2 + (ai >> 2)) * 1024 + (ai & 3) * 256 + hw * 64 + g4 * 16 : OOB, tag4(d, tA));
    if (w == 4) STAMP(5);
    if (w == 4) *(LAS v2u*)(L + L_QS + lane * 8) = fetch_q(rs, q_off, 1024, tA);
    if (w == 4) STAMP(6);
    BAR();
  } else if (isBA) {
    const int b = bi >> 4, sp = bi & 15;
    const char* cb = p.BC + (u32)((bi * 256 + w + 128) * 1024);
    const u32 vc0 = (lane ^ fsw(w)) * 16;
    v4u qs[4], c2[16];
#pragma unroll
    for (int i = 0; i < C2_EARLY; ++i) c2[i] = ldnt(cb + 8 * i * 1024, vc0 ^ ((i & 1) * 32), 0);
    SB();
    poll_flags<FQ_SLEEP>(rs, WS_FQ + (8 * w + (lane & 7)) * FSTR, tA, tB);
    if (w == 4) STAMP(7);
#pragma unroll
    for (int i = 0; i < 4; ++i) qs[i] = ld_sc1(rs, WS_QABS + (b * 32 + 4 * w + i) * 1024 + (lane ^ fsw(4 * w + i)) * 16);
    SB();
    asm volatile("s_barrier" ::: "memory");
#pragma unroll
    for (int i = C2_EARLY; i < (C2_EARLY + 16) / 2; ++i) c2[i] = ldnt(cb + 8 * i * 1024, vc0 ^ ((i & 1) * 32), 0);
    SB();
#pragma unroll
    for (int i = 0; i < 4; ++i) lst128(L, L_X + (4 * w + i) * 1024 + lane * 16, qs[i]);
    asm volatile("" ::: "memory");
    v4u K0[8], V0[8];
    if (EARLYK && !hlp) {
      STREAM_CONSTS();
#pragma unroll
      for (int j = 0; j < 8; ++j) K0[j] = LDK(c0 + w, j);
      SB();
    }
    BAR();
    v4u qf[16];
    {
      const u32 rowb = L_X + (lane & 31) * 1024 + (w >> 2) * 512, f = fsw(lane & 31);
#pragma unroll
      for (int ks = 0; ks < 16; ++ks) qf[ks] = lld128(L, rowb + (ks >> 3) * 256 + (((2 * (ks & 7) + (lane >> 5)) ^ f) * 16));
    }
    if (!LATEBAR) BAR();
#pragma unroll
    for (int i = (C2_EARLY + 16) / 2; i < 16; ++i) c2[i] = ldnt(cb + 8 * i * 1024, vc0 ^ ((i & 1) * 32), 0);
    if (w == 0) STAMP(8);
    f32x16 ac[2];
    float m, l;
    ba_pass<true>(L, w, lane, qf, ac, m, l, [](int) {});
    if (tid < 2) *(LAS u32*)(L + SY_A + tid * 4) = 0u;
    BAR();
    if (w == 0) STAMP(9);
#pragma unroll
    for (int i = 0; i < 16; ++i) lst128(L, (8 * i + w) * 1024 + lane * 16, c2[i]);
    v2u qpk = {0u, 0u};
    if (w == 4) qpk = fetch_q(rs, q_off, 1024, tA);
    BAR();
    if (w == 0) STAMP(10);
    if (!hlp) {
      STREAM_CONSTS();
      if (EARLYK) {
#pragma unroll
        for (int j = 0; j < 8; ++j) V0[j] = LDV(c0 + w, j);
      } else {
#pragma unroll
        for (int j = 0; j < 8; ++j) K0[j] = LDK(c0 + w, j);
      }
      SB();
    }
    ba_pass<false>(L, w, lane, qf, ac, m, l, [&](int at) {
      if (!hlp) {
        STREAM_CONSTS();
        if (at == 0 && !EARLYK) {
#pragma unroll
          for (int j = 0; j < 8; ++j) V0[j] = LDV(c0 + w, j);
        } else if (BA_PF >= 3 && at == (EARLYK == 1 ? 0 : 1)) {
#pragma unroll
          for (int j = 0; j < 8; ++j) K[j] = LDK(c0 + w + 4, j);
        }
        SB();
      }
    });
    if (w == 0) STAMP(29);
    l = x32sum(l);
    if (lane < 32) *(LAS float*)(L + L_WL + (w * 32 + lane) * 4) = l;
    BAR();
    {
      const u32 hd = lane & 31, hh = lane >> 5;
#pragma unroll
      for (int T = 0; T < 2; ++T)
#pragma unroll
        for (int rq = 0; rq < 4; ++rq)
          *(LAS v2u*)(L + hd * 1024 + (((8 * w + 4 * T + rq) ^ (hd & 15)) * 16) + 8 * hh) =
              v2u{pk2(ac[T][4 * rq], ac[T][4 * rq + 1]), pk2(ac[T][4 * rq + 2], ac[T][4 * rq + 3])};
    }
    if (w == 4) *(LAS v2u*)(L + L_QS + lane * 8) = qpk;
    BAR();
    if (hlp) {
#pragma unroll
      for (int i = 0; i < 8; ++i) {
        const u32 hd = 8 * hw + i;
        st_sc1(rs, WS_PART + (PLAY ? bi * 32 + hd : (hd * 4 + b) * 16 + sp) * 1024 + lane * 16, lld128(L, hd * 1024 + ((lane ^ (hd & 15)) * 16)));
      }
      if (w == 4 && lane < 32) {
        float ls = 0.f;
#pragma unroll
        for (int i = 0; i < 8; ++i) ls += *(LAS float*)(L + L_WL + (i * 32 + lane) * 4);
        st_sc1_8(rs, WS_ML + (MLAY ? bi * 32 + lane : (lane * 4 + b) * 16 + sp) * 8, v2u{asu(m), asu(ls)});
      }
      VMWAIT(0);
      if (lds_inc(L, SY_A) == 3 && lane == 0) st_sc1_8(rs, WS_FP + bi * FSTR, v2u{tA, tB});
      if (w == 4) STAMP(11);
    } else {
      STREAM_CONSTS();
      a_reset(acc);
      a_loadq(L, lane, QF);
      const u32 PP = a_soft(a_qk(K0, QF, t1), acc);
      a_pv(PP, V0, sel, acc);
      asm volatile("" : "+v"(acc.o)::"memory");
      if (BA_WAIT) lds_wait(L, SY_A, 4);
      if (BA_PF < 3) {
#pragma unroll
        for (int j = 0; j < 8; ++j) K[j] = LDK(c0 + w + 4, j);
      }
#pragma unroll
      for (int j = 0; j < 8; ++j) V[j] = LDV(c0 + w + 4, j);
      SB();
    }
  } else {
    if (w < 6) *(LAS v2u*)(L + L_QB + (2 * hw + (lane >> 5)) * 320 + (lane & 31) * 8) = fetch_q(rs, WS_E1 + h * 4096 + hw * 1024 + lane * 16, 2048, tA);
    VMWAIT(0);
    BAR();
    if (w == 4) STAMP(12);
    {
      f32x4 d[4];
#pragma unroll
      for (int T = 0; T < 4; ++T) d[T] = f32x4{0.f, 0.f, 0.f, 0.f};
#pragma unroll
      for (int ks = 0; ks < 4; ++ks) {
        const v4u qb = lld128(L, L_QB + (lane & 3) * 320 + ks * 64 + g4 * 16);
        const u32 a0 = (64 * hw + n16) * 256 + (((4 * ks + g4) ^ n16) * 16);
#pragma unroll
        for (int T = 0; T < 4; ++T) d[T] = mfma16(lld128(L, a0 + T * 4096), qb, d[T]);
      }
      const int T = n16 >> 2;
      const f32x4 e = sel4(T & 2, sel4(T & 1, d[3], d[2]), sel4(T & 1, d[1], d[0]));
      st_sc1_8(rs, WS_QABS + ((lane & 3) * 32 + h) * 1024 + (half * 256 + 64 * hw + 16 * T + 4 * g4) * 2, v2u{pk2(e[0], e[1]), pk2(e[2], e[3])});
    }
    VMWAIT(0);
    BAR();
    if (tid == 256) st_sc1_8(rs, WS_FQ + bw * FSTR, v2u{tA, tB});
    if (w == 4) STAMP(13);
    if (w == 4) *(LAS v2u*)(L + L_QS + lane * 8) = fetch_q(rs, q_off, 1024, tA);
    BAR();
    LOAD_WO(p.Bwo + (u32)(bw * 64 * 1152), 9, 2);
    v4u uf[16];
#pragma unroll
    for (int ks = 0; ks < 16; ++ks) uf[ks] = ltr2(L, L_UV + tr128 + ks * 4096, L_UV + tr128 + ks * 4096 + 512);
    if (w == 4) STAMP(24);
    poll_flags<FP_SLEEP>(rs, WS_FP + (16 * hw + n16) * FSTR, tA, tB);
    if (lane == 0) *(volatile LAS u32*)(L + SY_HOLD) = 1u;
    if (w == 4) STAMP(25);
    {
      const v2u ml = ld_sc1_8(rs, WS_ML + (MLAY ? (hw * 16 + n16) * 32 + h : (h * 4 + hw) * 16 + n16) * 8);
      v4u pt[16];
#pragma unroll
      for (int i = 0; i < 16; ++i) pt[i] = ld_sc1(rs, WS_PART + (PLAY ? (hw * 16 + i) * 32 + h : (h * 4 + hw) * 16 + i) * 1024 + lane * 16);
      float M = fmaxf(asf(ml.x), dppf<XOR1>(asf(ml.x)));
      M = fmaxf(M, dppf<XOR2>(M)); M = fmaxf(M, dppf<ROR4>(M)); M = fmaxf(M, dppf<ROR8>(M));
      const float wt = ex2(asf(ml.x) - M);
      float Ls = wt * asf(ml.y);
      Ls += dppf<XOR1>(Ls); Ls += dppf<XOR2>(Ls); Ls += dppf<XOR7>(Ls); Ls += dppf<XOR15>(Ls);
      const float wn = wt / Ls;
      float c[8];
#pragma unroll
      for (int k = 0; k < 8; ++k) c[k] = 0.f;
#pragma unroll
      for (int i = 0; i < 16; ++i) {
        const float wi = asf(__builtin_amdgcn_readlane(asu(wn), i));
#pragma unroll
        for (int k = 0; k < 4; ++k) { c[2 * k] += wi * asf(pt[i][k] << 16); c[2 * k + 1] += wi * asf(pt[i][k] & 0xffff0000u); }
      }
      lst128(L, L_CL + hw * 1088 + lane * 16, v4u{pk2(c[0], c[1]), pk2(c[2], c[3]), pk2(c[4], c[5]), pk2(c[6], c[7])});
    }
    lds_inc(L, SY_A);
    lds_wait(L, SY_A, 4);
    if (w == 4) STAMP(26);
    {
      f32x4 d = {0.f, 0.f, 0.f, 0.f};
#pragma unroll
      for (int ks = 0; ks < 16; ++ks) d = mfma16(lld128(L, L_CL + (lane & 3) * 1088 + ks * 64 + g4 * 16), uf[ks], d);
      const float dv = (g4 & 2) ? ((g4 & 1) ? d[3] : d[2]) : ((g4 & 1) ? d[1] : d[0]);
      *(LAS u16*)(L + L_OA + g4 * 320 + (16 * hw + n16) * 2) = (u16)pk2(dv, dv);
    }
    if (w == 4) STAMP(27);
  }

  if (!hlp) {
#define YIELD() do { if (chk && __builtin_amdgcn_readfirstlane(*(volatile LAS u32*)(L + SY_HOLD))) { lds_wait(L, SY_A, 4); chk = false; } } while (0)
    float S = a_qk(K, QF, t1);
    for (int id = c0 + w + 8; id < c1; id += 4) {
      YIELD();
      asm volatile("" : "+v"(S)::"memory");
#pragma unroll
      for (int j = 0; j < 8; ++j) K[j] = LDK(id, j);
      SB();
      const u32 PP = a_soft(S, acc);
      a_pv(PP, V, sel, acc);
      asm volatile("" : "+v"(acc.o)::"memory");
      YIELD();
#pragma unroll
      for (int j = 0; j < 8; ++j) V[j] = LDV(id, j);
      SB();
      if (id + 4 >= c1 && lane == 0) __hip_atomic_fetch_add((LAS u32*)(L + SY_LAST), 1u, __ATOMIC_RELAXED, __HIP_MEMORY_SCOPE_WORKGROUP);
      S = a_qk(K, QF, t1);
    }
    const u32 PP = a_soft(S, acc);
    float ls = acc.l + dppf<XOR1>(acc.l);
    ls += dppf<ROR4>(ls);
    ls += dppf<ROR8>(ls);
    ls = x32sum(x16sum(ls));
    u32 fa = L_FL + lane * 16, ma = L_FM + (lane & 2) * 4, sa = SY_B, one = 1;
    u32 eo = WS_E3 + (((g * 2 + pair) * 4 + prod) * 2 + ((lane >> 1) & 1)) * 512 + g4 * 128 + (((n16 >> 2) << 1) | (n16 & 1)) * 16;
    if ((lane & 61) == 0) *(LAS f32x2*)(L + ma + w * 16) = f32x2{acc.m, ls};
    asm volatile("" : "+v"(fa), "+v"(ma), "+v"(sa), "+v"(one), "+v"(eo)::"memory");
    a_pv(PP, V, sel, acc);
    *(LAS f32x4*)(L + fa + w * 1024) = acc.o;
    u32 old;
    u64 ex;
    asm volatile("s_mov_b64 %1, exec\n\ts_mov_b64 exec, 1\n\tds_add_rtn_u32 %0, %2, %3\n\ts_mov_b64 exec, %1" : "=&v"(old), "=&s"(ex) : "v"(sa), "v"(one) : "memory");
    f32x2 ml[4];
    f32x4 aw[4];
#pragma unroll
    for (int i = 0; i < 4; ++i) ml[i] = *(LAS f32x2*)(L + ma + i * 16);
#pragma unroll
    for (int i = 0; i < 4; ++i) aw[i] = *(LAS f32x4*)(L + fa + i * 1024);
    const float M = fmaxf(fmaxf(ml[0].x, ml[1].x), fmaxf(ml[2].x, ml[3].x));
    float wt[4], Ls = 0.f;
#pragma unroll
    for (int i = 0; i < 4; ++i) { wt[i] = ex2(ml[i].x - M); Ls += wt[i] * ml[i].y; }
    asm volatile("" : "+v"(old) : "v"(Ls));
    const bool last = __builtin_amdgcn_readfirstlane(old) == 3;
    if (last) STAMP(16);
    const f32x4 o = (wt[0] * aw[0] + wt[1] * aw[1] + wt[2] * aw[2] + wt[3] * aw[3]) * __builtin_amdgcn_rcpf(Ls);
    const u32 lse = asu(M + __builtin_amdgcn_logf(Ls));
    const u32 p01 = pk2(o[0], o[1]), p23 = pk2(o[2], o[3]);
    st_sc1(rs, last ? eo : OOB, v4u{__builtin_amdgcn_perm(p01, tA, 0x07060500u), __builtin_amdgcn_perm(p23, tA, 0x07060501u), __builtin_amdgcn_perm(lse, tA, 0x07060502u),
                                    __builtin_amdgcn_perm(p01, tA, 0x0c0c0403u) | __builtin_amdgcn_perm(lse, p23, 0x04000c0cu)});
    if (last) STAMP(15);
    if (isA) BAR();
  } else if (isA) {
    const int i16 = lane >> 2;
    u32 go = WS_E3 + (((kvh * 4 + hw) * 4 + (lane & 3)) * 2 + (ai >> 2)) * 512 + i16 * 16;
    u32 oa = L_OA + hw * 320 + (4 * (i16 & 8) + (i16 & 7) + 8 * (lane & 3)) * 2, ra = L_OA + (lane & 3) * 320 + g4 * 16;
    u32 eo = g4 < nu ? WS_E4 + ((16 * (ai & 3) + 8 * g4 + 2 * hw + (n16 >> 3)) * 32 + kvh * 2 + (ai >> 2)) * 128 + (n16 & 7) * 16 : OOB;
    asm volatile("" : "+v"(go), "+v"(oa), "+v"(ra), "+v"(eo));
#pragma unroll 1
    for (int pass = DRY ? 0 : 1; pass < 2; ++pass) {
      if (pass) {
        lds_wait(L, SY_LAST, LATE_A);
        LOAD_WO(p.Awo + (u32)((kvh * 256 + (ai >> 2) * 128) * 1152 + (ai & 3) * 256), nu, 4);
        VMWAIT(0);
        lds_inc(L, SY_WO);
        lds_wait(L, SY_WO, 4);
      }
      if (w == 4) STAMP(28);
      v4u wf[12];
#pragma unroll
      for (int i = 0; i < 12; ++i) wf[i] = ltr2(L, L_WO + tr128 + i * 4096, L_WO + tr128 + i * 4096 + 512);
      v4u v[2];
      poll16<2, true>(rs, pass ? go : OOB, 256, v, tA, tB, !pass);
      const float m = asf(__builtin_amdgcn_perm(v[0].z, v[0].w, 0x07060503u));
      float M = fmaxf(m, dppf<XOR1>(m));
      M = fmaxf(M, dppf<XOR2>(M));
      float wt = ex2(m - M), Ls = wt + dppf<XOR1>(wt);
      Ls += dppf<XOR2>(Ls);
      wt *= __builtin_amdgcn_rcpf(Ls);
      float y[4];
#pragma unroll
      for (int a = 0; a < 4; ++a) {
        const u32 x = a < 2 ? v[a].x : v[a - 2].y;
        const float lo = wt * asf(__builtin_amdgcn_perm(x, v[a & 1].w, a < 2 ? 0x05010c0cu : 0x05020c0cu)), hi = wt * asf(x & 0xffff0000u);
        y[a] = (t1 ? hi : lo) + dppf<XOR1>(t1 ? lo : hi);
      }
      const bool t2 = lane & 2;
      const float z0 = (t2 ? y[2] : y[0]) + dppf<XOR2>(t2 ? y[0] : y[2]), z1 = (t2 ? y[3] : y[1]) + dppf<XOR2>(t2 ? y[1] : y[3]);
      const u32 zz = pk2(z0, z1);
      *(LAS u16*)(L + oa) = (u16)zz;
      *(LAS u16*)(L + oa + 128) = (u16)(zz >> 16);
      if (pass) BAR();
      if (w == 4) STAMP(17);
      f32x4 e[3];
#pragma unroll
      for (int u = 0; u < 3; ++u) e[u] = f32x4{0.f, 0.f, 0.f, 0.f};
#pragma unroll
      for (int ks = 0; ks < 4; ++ks) {
        const v4u af = lld128(L, ra + ks * 64);
#pragma unroll
        for (int u = 0; u < 3; ++u) e[u] = mfma16(af, wf[4 * u + ks], e[u]);
      }
      st_sc1(rs, pass ? eo : OOB, tag4(sel4(g4 & 2, e[2], sel4(g4 & 1, e[1], e[0])), tA));
      if (w == 4) STAMP(18);
    }
  } else if (isBA) {
    const int oct = hw * 64 + bi;
    if (oct < 72) {
      reduce_oct<4>(rs, WS_E4 + oct * 4096 + lane * 16, tA, p.out + oct * 8, lane, [&] { lds_wait(L, SY_B, 4); });
      if (oct == 0 && lane == 0) __hip_atomic_store((GAS u64*)(p.ws + WS_EPOCH), ((u64)tB << 32) | tA, RLX_AGENT);
      STAMP(19);
    }
  } else {
    VMWAIT(0);
    lds_inc(L, SY_WO);
    lds_wait(L, SY_WO, 4);
    const v4u af0 = lld128(L, L_OA + (lane & 3) * 320 + g4 * 16), af1 = lld128(L, L_OA + (lane & 3) * 320 + 64 + g4 * 16);
    f32x4 e[9];
#pragma unroll
    for (int u = 0; u < 9; ++u) {
      const u32 t0 = L_WO + tr128 + u * 8192;
      e[u] = mfma16(af1, ltr2(L, t0 + 4096, t0 + 4608), mfma16(af0, ltr2(L, t0, t0 + 512), f32x4{0.f, 0.f, 0.f, 0.f}));
    }
    const u32 eo = WS_E6 + ((8 * g4 + 2 * hw + (n16 >> 3)) * 64 + bw) * 128 + (n16 & 7) * 16;
#pragma unroll
    for (int k = 0; k < 2; ++k)
      st_sc1(rs, eo + k * 32 * 8192, tag4(sel4(g4 & 2, sel4(g4 & 1, e[4 * k + 3], e[4 * k + 2]), sel4(g4 & 1, e[4 * k + 1], e[4 * k])), tA));
    st_sc1(rs, g4 == 0 ? eo + 64 * 8192 : OOB, tag4(e[8], tA));
    if (w == 4) STAMP(18);
    const int oct = hw * 64 + bw;
    if (oct < 72) {
      reduce_oct<8>(rs, WS_E6 + oct * 8192 + lane * 16, tA, p.out + 576 + oct * 8, lane, [] {});
      STAMP(14);
    }
  }
  asm volatile("" ::"v"(pf));
}

extern "C" void kernel_launch(void* const* d_in, const int* in_sizes, int n_in, void* d_out, int out_size, void* d_ws, size_t ws_size,
                              hipStream_t stream) {
  Params p;
  p.Ax = (const char*)d_in[0]; p.AK = (const char*)d_in[1]; p.AV = (const char*)d_in[2]; p.Awq = (const char*)d_in[3]; p.Awo = (const char*)d_in[4];
  p.Bx = (const char*)d_in[5]; p.BC = (const char*)d_in[6]; p.Bwq = (const char*)d_in[7]; p.Bwuk = (const char*)d_in[8]; p.Bwuv = (const char*)d_in[9];
  p.Bwo = (const char*)d_in[10];
  p.out = (u16*)d_out;
  p.ws = (char*)d_ws;
  (void)hipFuncSetAttribute((const void*)decode_mega, hipFuncAttributeMaxDynamicSharedMemorySize, LDS_BYTES);
  decode_mega<<<256, 512, LDS_BYTES, stream>>>(p);
}
```

```cpp
#include <hip/hip_runtime.h>
#include <stdint.h>

typedef unsigned int u32;
typedef unsigned long long u64;
typedef unsigned short u16;
typedef u32 v4u __attribute__((ext_vector_type(4)));
typedef u32 v2u __attribute__((ext_vector_type(2)));
typedef u32 v8u __attribute__((ext_vector_type(8)));
typedef u32 v16u __attribute__((ext_vector_type(16)));
typedef float f32x2 __attribute__((ext_vector_type(2)));
typedef float f32x4 __attribute__((ext_vector_type(4)));
typedef float f32x16 __attribute__((ext_vector_type(16)));
typedef __bf16 bf16x8 __attribute__((ext_vector_type(8)));
typedef __bf16 bf16x2 __attribute__((ext_vector_type(2)));
typedef short s16x4 __attribute__((ext_vector_type(4)));
#define DI __device__ __forceinline__
#define LAS __attribute__((address_space(3)))
#define GAS __attribute__((address_space(1)))
#define RLX_AGENT __ATOMIC_RELAXED, __HIP_MEMORY_SCOPE_AGENT

#define STAMPS 0
#define CODE_BYTES 0
#define C2_EARLY 16
#define DYNP 0
#define BA_PF 2
#define BA_WAIT 1
#define LATE_A 4
#define BRED_LATE 0
#define FSTR 128
#define FQ_SLEEP 1
#define FP_SLEEP 1
#define PLAY 1
#define MLAY 1
#define DRY 1
#define TOPPRIO 0
#define UVSPLIT 0
#define LATEBAR 1
#define EARLYK 0
#define DPB 0
#define PBURST 0
#define DBURST 0
#define PARK 0
#define PAUSE_AT 0
#define BA_DELAY 0
#define C2B(k) (C2_EARLY + ((16 - C2_EARLY) * (k) + 3) / 4)
#define LATEB 1
#define WO_T 0
#define WB_T 0
#define WOB_AFTER 1
#define GATE 0
#define PS_T 0
#define GWAIT 0
#define C_A 44
#define C_BA 16
#define DPAR 0
#define PSLEEP 1
#define PACE 8

constexpr int WS_EPOCH = 0;
constexpr int WS_FQ = 32768;
constexpr int WS_FP = 49152;
constexpr int WS_ML = 16384;
constexpr int WS_QABS = 65536;
constexpr int WS_E1 = 262144;
constexpr int WS_E2 = 393216;
constexpr int WS_E3 = 524288;
constexpr int WS_E6 = 1 << 20;
constexpr int WS_E4 = WS_E6 + 72 * 8192;
constexpr int WS_PART = 2 << 20;
constexpr int WS_BYTES = 4 << 20;
constexpr u32 OOB = 0x7f000000u;
constexpr int LDS_BYTES = 163840;
constexpr int L_QBW = 0, L_QAW = 36864;
constexpr int L_PK = 0, L_PV = 73728;
constexpr int L_XV = 110592;
constexpr int L_WO = 0;
constexpr int L_UV = 73728;
constexpr int L_QS = 139264;
constexpr int L_FL = L_QS + 1024;
constexpr int L_FM = L_FL + 4096;
constexpr int L_OA = L_FM + 128;
constexpr int L_X = 131072;
constexpr int L_SX = L_X, L_P = L_X + 16384, L_WM = L_X + 24576, L_WL = L_X + 25600;
constexpr int L_SYNC = L_X + 26624;
constexpr int L_QB = L_OA + 1280;
constexpr int L_OB = L_QB;
constexpr int L_CL = L_QB + 1280;
static_assert(L_CL + 4352 <= L_WM, "LDS map");
enum { SY_A = L_SYNC, SY_B = L_SYNC + 4, SY_LAST = L_SYNC + 8, SY_C1 = L_SYNC + 12, SY_C2 = L_SYNC + 16, SY_UF = L_SYNC + 28, SY_HOLD = L_SYNC + 20, SY_WO = L_SYNC + 24 };

constexpr float SCALE = 0.08838834764831845f;
constexpr float LOG2E = 1.4426950408889634f;
constexpr u32 SPIN_CAP = 1u << 18;

struct Params {
  const char *Ax, *AK, *AV, *Awq, *Awo, *Bx, *BC, *Bwq, *Bwuk, *Bwuv, *Bwo;
  u16* out;
  char* ws;
};

DI f32x4 mfma16(v4u a, v4u b, f32x4 c) {
  return __builtin_amdgcn_mfma_f32_16x16x32_bf16(__builtin_bit_cast(bf16x8, a), __builtin_bit_cast(bf16x8, b), c, 0, 0, 0);
}
DI f32x16 mfma32(v4u a, v4u b, f32x16 c) {
  return __builtin_amdgcn_mfma_f32_32x32x16_bf16(__builtin_bit_cast(bf16x8, a), __builtin_bit_cast(bf16x8, b), c, 0, 0, 0);
}
DI u32 pk2(float a, float b) { f32x2 v = {a, b}; return __builtin_bit_cast(u32, __builtin_convertvector(v, bf16x2)); }
DI float rbf(float a) { return (float)(__bf16)a; }
DI float asf(u32 x) { return __builtin_bit_cast(float, x); }
DI u32 asu(float x) { return __builtin_bit_cast(u32, x); }
DI float ex2(float x) { return __builtin_amdgcn_exp2f(x); }
template <int CTRL> DI float dppf(float x) { return asf(__builtin_amdgcn_mov_dpp(asu(x), CTRL, 0xf, 0xf, true)); }
template <int CTRL> DI u32 dppu(u32 x) { return __builtin_amdgcn_mov_dpp(x, CTRL, 0xf, 0xf, true); }
constexpr int XOR1 = 0xB1, XOR2 = 0x4E, XOR7 = 0x141, XOR15 = 0x140, ROR8 = 0x128, ROR4 = 0x124;
DI float x16max(float x) { auto s = __builtin_amdgcn_permlane16_swap(asu(x), asu(x), false, false); return fmaxf(asf(s[0]), asf(s[1])); }
DI float x32max(float x) { auto s = __builtin_amdgcn_permlane32_swap(asu(x), asu(x), false, false); return fmaxf(asf(s[0]), asf(s[1])); }
DI float x16sum(float x) { auto s = __builtin_amdgcn_permlane16_swap(asu(x), asu(x), false, false); return asf(s[0]) + asf(s[1]); }
DI float x32sum(float x) { auto s = __builtin_amdgcn_permlane32_swap(asu(x), asu(x), false, false); return asf(s[0]) + asf(s[1]); }
DI float sum_hi3(f32x4 a) {
  auto s0 = __builtin_amdgcn_permlane32_swap(asu(a[0]), asu(a[1]), false, false);
  auto s1 = __builtin_amdgcn_permlane32_swap(asu(a[2]), asu(a[3]), false, false);
  auto t = __builtin_amdgcn_permlane16_swap(asu(asf(s0[0]) + asf(s0[1])), asu(asf(s1[0]) + asf(s1[1])), false, false);
  const float c = asf(t[0]) + asf(t[1]);
  return c + dppf<ROR8>(c);
}
DI f32x4 sel4(bool c, f32x4 a, f32x4 b) { return f32x4{c ? a[0] : b[0], c ? a[1] : b[1], c ? a[2] : b[2], c ? a[3] : b[3]}; }

#define BAR() asm volatile("s_waitcnt lgkmcnt(0)\n\ts_barrier" ::: "memory")
#define VMWAIT(n) asm volatile("s_waitcnt vmcnt(%0)" ::"n"(n) : "memory")
#define SB() __builtin_amdgcn_sched_barrier(0)

DI v4u lld128(LAS char* L, u32 o) { return *(LAS v4u*)(L + o); }
DI void lst128(LAS char* L, u32 o, v4u v) { *(LAS v4u*)(L + o) = v; }
DI v2u ltr(LAS char* L, u32 o) { return __builtin_bit_cast(v2u, __builtin_amdgcn_ds_read_tr16_b64_v4i16((LAS s16x4*)(L + o))); }
DI v4u ltr2(LAS char* L, u32 o0, u32 o1) { v2u a = ltr(L, o0), b = ltr(L, o1); return v4u{a.x, a.y, b.x, b.y}; }
DI void dma16(const char* sbase, u32 voff, u32 lds) {
  u32 keep;
  asm volatile("s_mov_b32 %0, m0\n\ts_mov_b32 m0, %3\n\ts_nop 2\n\tglobal_load_lds_dwordx4 %1, %2 nt\n\ts_mov_b32 m0, %0"
               : "=&s"(keep) : "v"(voff), "s"(sbase), "s"(lds) : "memory");
}
DI void dma16c(const char* sbase, u32 voff, u32 lds) {
  u32 keep;
  asm volatile("s_mov_b32 %0, m0\n\ts_mov_b32 m0, %3\n\ts_nop 2\n\tglobal_load_lds_dwordx4 %1, %2\n\ts_mov_b32 m0, %0"
               : "=&s"(keep) : "v"(voff), "s"(sbase), "s"(lds) : "memory");
}
DI v4u ldnt(const char* base, u32 voff, int imm) { return __builtin_nontemporal_load((const GAS v4u*)((const GAS char*)base + voff + imm)); }
typedef __amdgpu_buffer_rsrc_t rsrc_t;
DI v4u ld_sc1(rsrc_t rs, u32 o) { return __builtin_amdgcn_raw_buffer_load_b128(rs, o, 0, 16); }
DI v2u ld_sc1_8(rsrc_t rs, u32 o) { return __builtin_amdgcn_raw_buffer_load_b64(rs, o, 0, 16); }
DI void st_sc1(rsrc_t rs, u32 o, v4u v) { __builtin_amdgcn_raw_buffer_store_b128(v, rs, o, 0, 16); }
DI void st_sc1_8(rsrc_t rs, u32 o, v2u v) { __builtin_amdgcn_raw_buffer_store_b64(v, rs, o, 0, 16); }
DI v4u tag4(f32x4 e, u32 tA) {
  v4u v;
#pragma unroll
  for (int k = 0; k < 4; ++k) v[k] = __builtin_amdgcn_perm(asu(e[k]) + 0x80u, tA, 0x07060500u + k);
  return v;
}
DI f32x4 untag4(v4u v) { return f32x4{asf(v.x & ~0xffu), asf(v.y & ~0xffu), asf(v.z & ~0xffu), asf(v.w & ~0xffu)}; }
template <int N, bool WORDS> DI void poll16(rsrc_t rs, u32 o, u32 stride, v4u (&v)[N], u32 tA, u32 tB, bool once = false) {
  for (u32 spins = once ? ~0u : 0u;; ++spins) {
    asm volatile("" ::: "memory");
    bool ok = true;
#pragma unroll
    for (int i = 0; i < N; ++i) v[i] = ld_sc1(rs, o + i * stride);
#pragma unroll
    for (int i = 0; i < N; ++i) {
      if (WORDS) ok = ok && (__builtin_amdgcn_perm(v[i].y, v[i].x, 0x0c0c0400u) | __builtin_amdgcn_perm(v[i].w, v[i].z, 0x04000c0cu)) == tA;
      else ok = ok && v[i].y == tA && v[i].w == tB;
    }
    if (__all(ok) || spins > SPIN_CAP) break;
    if (PSLEEP) __builtin_amdgcn_s_sleep(PSLEEP);
  }
}
template <int SLEEP> DI void poll_flags(rsrc_t rs, u32 o, u32 tA, u32 tB) {
  for (u32 spins = 0;; ++spins) {
    asm volatile("" ::: "memory");
    const v2u v = ld_sc1_8(rs, o);
    if (__all(v.x == tA && v.y == tB) || spins > SPIN_CAP) break;
    __builtin_amdgcn_s_sleep(SLEEP);
  }
  __builtin_amdgcn_fence(__ATOMIC_ACQUIRE, "wavefront");
}
DI u32 lds_inc(LAS char* L, u32 o) {
  asm volatile("s_waitcnt lgkmcnt(0)" ::: "memory");
  u32 old = 0;
  if ((threadIdx.x & 63) == 0) old = __hip_atomic_fetch_add((LAS u32*)(L + o), 1u, __ATOMIC_RELAXED, __HIP_MEMORY_SCOPE_WORKGROUP);
  return __builtin_amdgcn_readfirstlane(old);
}
DI void lds_wait(LAS char* L, u32 o, u32 want) {
#pragma unroll 1
  for (u32 spins = 0; spins < (1u << 24); ++spins) {
    if (__builtin_amdgcn_readfirstlane(__hip_atomic_load((LAS u32*)(L + o), __ATOMIC_RELAXED, __HIP_MEMORY_SCOPE_WORKGROUP)) >= want) break;
    __builtin_amdgcn_s_sleep(1);
  }
  asm volatile("" ::: "memory");
}

#define STAMP(i) do {} while (0)

DI f32x4 gemv288(LAS char* L, u32 base, u32 xo) {
  f32x4 d = {0.f, 0.f, 0.f, 0.f};
#pragma unroll
  for (int ks = 0; ks < 9; ++ks) d = mfma16(ltr2(L, base + ks * 4096, base + ks * 4096 + 512), lld128(L, xo + ks * 64), d);
  return d;
}
DI v2u fetch_q(rsrc_t rs, u32 o, u32 stride, u32 tA) {
  v4u v[2];
  poll16<2, true>(rs, o, stride, v, tA, 0u);
  const f32x4 q = untag4(v[0]) + untag4(v[1]);
  return v2u{pk2(q[0], q[1]), pk2(q[2], q[3])};
}

struct Acc { float m, l; f32x4 o; };
DI float a_qk(const v4u (&K)[8], const v4u (&QF)[8], bool t1) {
  f32x4 d = {0.f, 0.f, 0.f, 0.f};
#pragma unroll
  for (int j = 0; j < 8; ++j) d = mfma16(K[j], QF[j], d);
  const float keep = t1 ? d[3] : d[0], send = t1 ? d[1] : d[2];
  return keep + dppf<XOR1>(send);
}
DI u32 a_soft(float S, Acc& a) {
  const float t = rbf(rbf(S) * SCALE) * LOG2E;
  float cm = fmaxf(t, dppf<XOR1>(t));
  cm = fmaxf(cm, dppf<ROR4>(cm));
  cm = fmaxf(cm, dppf<ROR8>(cm));
  cm = x32max(x16max(cm));
  const float mn = fmaxf(a.m, cm);
  const float al = ex2(a.m - mn), p = ex2(t - mn);
  a.l = a.l * al + p;
  a.m = mn;
  a.o *= al;
  return pk2(p, dppf<XOR2>(p));
}
template <int J> DI void a_pv1(u32 PP, v4u V, const u32 (&sel)[4], Acc& a) {
  const u32 X = dppu<0x150 + 4 * (J >> 1) + (J & 1)>(PP);
  const v4u B = {__builtin_amdgcn_perm(X, X, sel[0]), __builtin_amdgcn_perm(X, X, sel[1]), __builtin_amdgcn_perm(X, X, sel[2]), __builtin_amdgcn_perm(X, X, sel[3])};
  a.o = mfma16(V, B, a.o);
}
DI void a_pv(u32 PP, const v4u (&V)[8], const u32 (&sel)[4], Acc& a) {
  a_pv1<0>(PP, V[0], sel, a); a_pv1<1>(PP, V[1], sel, a); a_pv1<2>(PP, V[2], sel, a); a_pv1<3>(PP, V[3], sel, a);
  a_pv1<4>(PP, V[4], sel, a); a_pv1<5>(PP, V[5], sel, a); a_pv1<6>(PP, V[6], sel, a); a_pv1<7>(PP, V[7], sel, a);
}
DI void a_loadq(LAS char* L, int lane, v4u (&QF)[8]) {
  const int n = lane & 15, g = lane >> 4;
  const u32 o = L_QS + ((n >> 1) & 1) * 256 + (2 * g + (n & 1)) * 16;
  const v4u F0 = lld128(L, o), F1 = lld128(L, o + 128), Z = {0u, 0u, 0u, 0u};
#pragma unroll
  for (int u = 0; u < 4; ++u) { const bool on = (n >> 2) == u; QF[2 * u] = on ? F0 : Z; QF[2 * u + 1] = on ? F1 : Z; }
}
DI void a_reset(Acc& a) { a.m = -INFINITY; a.l = 0.f; a.o = f32x4{0.f, 0.f, 0.f, 0.f}; }
DI u32 sw2(u32 row) { return (row & 2) | ((row >> 1) & 4); }
DI u32 fsw(u32 row) { return ((row & 3) << 2) | ((row >> 2) & 3); }
template <int N, class F> DI void reduce_oct(rsrc_t rs, u32 o, u32 tA, u16* out_, int lane, F&& gate) {
  GAS u16* out = (GAS u16*)out_ + ((lane >> 5) + ((lane >> 3) & 2)) * 1152 + (lane & 7);
  asm volatile("" : "+v"(out), "+v"(o));
#pragma unroll 1
  for (int pass = DRY ? 0 : 1; pass < 2; ++pass) {
    if (pass) gate();
    v4u v[N];
    poll16<N, true>(rs, pass ? o : OOB, 1024, v, tA, 0u, !pass);
    f32x4 a = untag4(v[0]);
#pragma unroll
    for (int i = 1; i < N; ++i) a += untag4(v[i]);
    const float c = sum_hi3(a);
    if (!(lane & 8) && pass) *out = (u16)pk2(c, c);
  }
}

template <bool FIRST, class F>
DI void ba_pass(LAS char* L, int w, int lane, const v4u (&qf)[16], f32x16 (&acc)[2], float& m, float& l, F&& hook) {
  asm volatile("" : "+v"(lane));
  const int kb = w & 3, ch = w >> 2, hh = lane >> 5, r31 = lane & 31;
  f32x16 S;
#pragma unroll
  for (int i = 0; i < 16; ++i) S[i] = 0.f;
  const u32 rowb = (32 * kb + r31) * 1024 + ch * 512, f = fsw(r31);
#pragma unroll
  for (int ks = 0; ks < 16; ++ks) S = mfma32(lld128(L, rowb + (ks >> 3) * 256 + (((2 * (ks & 7) + hh) ^ f) * 16)), qf[ks], S);
  float s[8];
  {
    v4u a, b;
#pragma unroll
    for (int j = 0; j < 4; ++j) { a[j] = asu(ch ? S[j] : S[8 + j]); b[j] = asu(ch ? S[4 + j] : S[12 + j]); }
    if (FIRST && LATEBAR) { asm volatile("" : "+v"(a), "+v"(b)); BAR(); }
    lst128(L, L_SX + w * 2048 + lane * 16, a);
    lst128(L, L_SX + w * 2048 + 1024 + lane * 16, b);
    asm volatile("" ::: "memory");
    hook(0);
    BAR();
    a = lld128(L, L_SX + (w ^ 4) * 2048 + lane * 16);
    b = lld128(L, L_SX + (w ^ 4) * 2048 + 1024 + lane * 16);
#pragma unroll
    for (int j = 0; j < 4; ++j) {
      s[j] = ((ch ? S[8 + j] : S[j]) + asf(a[j])) * (SCALE * LOG2E);
      s[4 + j] = ((ch ? S[12 + j] : S[4 + j]) + asf(b[j])) * (SCALE * LOG2E);
    }
  }
  float cm = fmaxf(fmaxf(fmaxf(s[0], s[1]), fmaxf(s[2], s[3])), fmaxf(fmaxf(s[4], s[5]), fmaxf(s[6], s[7])));
  cm = x32max(cm);
  if (lane < 32) *(LAS float*)(L + L_WM + (w * 32 + lane) * 4) = cm;
  BAR();
  float mn = FIRST ? -INFINITY : m;
#pragma unroll
  for (int i = 0; i < 8; ++i) mn = fmaxf(mn, *(LAS float*)(L + L_WM + (i * 32 + r31) * 4));
  float ps = 0.f;
#pragma unroll
  for (int j = 0; j < 8; ++j) { s[j] = ex2(s[j] - mn); ps += s[j]; }
  lst128(L, L_P + (2 * kb + ch) * 1024 + lane * 16, v4u{pk2(s[0], s[1]), pk2(s[2], s[3]), pk2(s[4], s[5]), pk2(s[6], s[7])});
  if (FIRST) {
    l = ps;
#pragma unroll
    for (int i = 0; i < 16; ++i) { acc[0][i] = 0.f; acc[1][i] = 0.f; }
  } else {
    const float al = ex2(m - mn);
    l = l * al + ps;
    acc[0] *= al;
    acc[1] *= al;
  }
  m = mn;
  asm volatile("" ::: "memory");
  hook(1);
  BAR();
  const int q = (lane & 15) >> 2, pp = lane & 3, blk = (lane >> 4) & 1;
  u32 ta[2][2];
#pragma unroll
  for (int T = 0; T < 2; ++T)
#pragma unroll
    for (int t = 0; t < 2; ++t) {
      const u32 c = 8 * w + 4 * T + 2 * blk + (pp >> 1);
      ta[T][t] = (8 * t + 4 * hh + q) * 1024 + ((c ^ ((q << 2) | (2 * t + hh))) * 16) + 8 * (pp & 1);
    }
#pragma unroll
  for (int ks = 0; ks < 8; ++ks) {
    const v4u P = lld128(L, L_P + ks * 1024 + lane * 16);
#pragma unroll
    for (int T = 0; T < 2; ++T) acc[T] = mfma32(ltr2(L, ta[T][0] + ks * 16384, ta[T][1] + ks * 16384), P, acc[T]);
  }
  asm volatile("" : "+v"(acc[0]), "+v"(acc[1]));
}

__global__ void __launch_bounds__(512, 2) decode_mega(Params args) {
  extern __shared__ __attribute__((aligned(16))) char smem[];
  LAS char* L = (LAS char*)smem;
  v16u ka;
  v8u kb;
  v2u kc;
  asm volatile("s_load_dwordx16 %0, %3, 0x0\n\ts_load_dwordx8 %1, %3, 0x40\n\ts_load_dwordx2 %2, %3, 0x60"
               : "=&s"(ka), "=&s"(kb), "=&s"(kc) : "s"(__builtin_amdgcn_kernarg_segment_ptr()) : "memory");
  SB();
  const int tid = threadIdx.x, lane = tid & 63;
  int w = __builtin_amdgcn_readfirstlane(tid >> 6), wg = blockIdx.x;
  u32 pf = 0;
  const u32 tbeg = (WO_T + WB_T + PS_T) ? (u32)__builtin_amdgcn_s_memrealtime() : 0u;
  u64 pc0;
  asm volatile("s_getpc_b64 %0" : "=s"(pc0));
  u32 vo, ld, step, lstep;
  const char* src0;
  {
    const int g = wg >> 3, r = (wg + g) & 7, hw = w & 3;
    const u32 koff = r < 4 ? (w < 4 ? 56 : (w & 1) ? 0 : 40) : r < 6 ? 48 : w >= 4 ? 64 : 72;
    asm volatile("s_load_dwordx2 %0, %1, %2" : "=s"(src0) : "s"(__builtin_amdgcn_kernarg_segment_ptr()), "s"(koff) : "memory");
    if (r < 4 && w >= 4) {
      u32 keep, xv = lane * 16 + (w & 2) * 448;
      asm volatile("s_setprio 3\n\ts_mov_b32 %0, m0\n\ts_mov_b32 m0, %2\n\ts_waitcnt lgkmcnt(0)\n\tglobal_load_lds_dwordx4 %1, %3\n\t"
                   "global_load_lds_dwordx4 %1, %3 offset:1792\n\tglobal_load_lds_dwordx4 %1, %3 offset:2688\n\ts_mov_b32 m0, %0"
                   : "=&s"(keep) : "v"(xv), "s"(L_XV + (w & 1) * 4608 + (w & 2) * 448), "s"(src0) : "memory");
    }
    const u32 hoff = ((g & ~7) | (wg & 7)) * 256;
    const bool cA = r < 4, cB = (r & 6) == 4, cK = r >= 6 && w >= 4;
    step = cB ? 16384 : cK ? 16 * 8192 : 32 * 8192;
    lstep = cB ? 16384 : 4096;
    ld = cB ? w * 1024 : hw * 1024 + (cA || cK ? 0 : L_UV);
    const u32 sA = (r >> 1) * (288 * 8192) + g * 256, sB = ((g * 2 + (r & 1)) * 256 + w) * 1024, sK = ((r & 1) * 256 + 4 * hw) * 8192 + hoff;
    const u32 so = cB ? sB : cK ? sK : (cA ? sA : hoff) + 8 * 8192 * w + (r & 1) * 128;
    const u32 sh = cB ? 6 : cK ? 4 : 3, row = (u32)lane >> sh;
    const u32 xs = cB ? fsw(w) : cK ? 4 * hw : 4 * (w & 1), xm = cK ? 3 : 2;
    vo = row * 8192 + ((((u32)lane - (row << sh)) ^ xs ^ (row & xm)) * 16) + so;
    if (TOPPRIO && w >= 4) __builtin_amdgcn_s_setprio(3);
    if (BA_DELAY && cB) __builtin_amdgcn_s_sleep(BA_DELAY);
    if (UVSPLIT == 3 ? (cA ? w < 4 : cB || cK) : (r >= 4 || w < 4)) {
      u32 keep;
#define PIECE "global_load_lds_dwordx4 %1, %3 nt\n\ts_add_u32 m0, m0, %5\n\tv_add_u32 %1, %4, %1\n\t"
      asm volatile("s_mov_b32 %0, m0\n\ts_mov_b32 m0, %2\n\ts_waitcnt lgkmcnt(0)\n\t" PIECE PIECE PIECE PIECE PIECE PIECE PIECE PIECE "s_mov_b32 m0, %0"
                   : "=&s"(keep), "+v"(vo) : "s"(ld), "s"(src0), "s"(step), "s"(lstep) : "memory", "scc");
      ld += 8 * lstep;
    }
  }
  asm volatile("s_waitcnt lgkmcnt(0)" : "+s"(ka), "+s"(kb), "+s"(kc), "+s"(src0), "+s"(w), "+s"(wg)::"memory");
  if (w == 0) STAMP(20);
  Params p;
#define PTR(v, i) ((char*)(((u64)v[(i) + 1] << 32) | v[(i)]))
  p.Ax = PTR(ka, 0); p.AK = PTR(ka, 2); p.AV = PTR(ka, 4); p.Awq = PTR(ka, 6); p.Awo = PTR(ka, 8); p.Bx = PTR(ka, 10); p.BC = PTR(ka, 12); p.Bwq = PTR(ka, 14);
  p.Bwuk = PTR(kb, 0); p.Bwuv = PTR(kb, 2); p.Bwo = PTR(kb, 4); p.out = (u16*)PTR(kb, 6); p.ws = PTR(kc, 0);
  const int hw = w & 3, g = wg >> 3, s = wg & 7, r = (s + g) & 7, kvh = g >> 1;
  const bool isA = r < 4, isBA = r == 4 || r == 5, hlp = w >= 4;
  u64 epoch;
  asm volatile("s_load_dwordx2 %0, %1, 0x0" : "=s"(epoch) : "s"(p.ws) : "memory");
  if (hlp) __builtin_amdgcn_s_setprio(3);
  if (tid < 8) *(LAS u32*)(L + L_SYNC + tid * 4) = 0u;
  const int n16 = lane & 15, g4 = lane >> 4;
  const int ai = (g & 1) * 4 + r;
  const int bi = g * 2 + (r - 4);
  const int h = (g >> 3) * 8 + s, half = r - 6, bw = h * 2 + half;
  u32 vuv;
  {
    int n = isA ? (hlp ? 0 : 1) : isBA ? 0 : (hlp || !UVSPLIT) ? 8 : 0;
    vuv = vo;
#pragma unroll 1
    for (int bt = 0; bt < 2; ++bt) {
#pragma unroll 1
      for (; n > 0; --n) { if (!DBURST) VMWAIT(8); dma16(src0, vo, ld); vo += step; ld += lstep; }
      if (DBURST && bt == 0) VMWAIT(DBURST);
      src0 = isA ? p.Awq : src0;
      ld = isA ? L_QAW + w * 1024 : (8 + w) * 1024;
      n = isA ? (hlp ? 0 : 9) : isBA ? 8 : 0;
      vo = (vo ^ (isA ? 0 : 32)) + (isA ? (u32)(((ai >> 2) - (r >> 1) - 1) * (288 * 8192) + kvh * 512 - g * 256 + (r & 2) * 128) : (u32)(-120 * 1024));
    }
    if (w == 0) STAMP(21);
    asm volatile("s_waitcnt lgkmcnt(0)" : "+s"(epoch)::"memory");
  }
  const rsrc_t rs = __builtin_amdgcn_make_buffer_rsrc((void*)p.ws, 0, WS_BYTES, 0x00020000);
  const u32 trrow = 8 * g4 + (n16 >> 2);
  const u32 tr128 = trrow * 128 + ((2 * hw + ((lane & 3) >> 1)) ^ sw2(trrow)) * 16 + 8 * (lane & 1);
  const u32 tA = (((u32)epoch & 0x7f7f7f7fu) + 0x01010101u) ^ ((u32)epoch & 0x80808080u), tB = (u32)(epoch >> 32) + 1u;

  u32 voffK, voffV, sel[4];
#define STREAM_CONSTS() do { u32 l_; asm volatile("v_mbcnt_lo_u32_b32 %0, -1, 0\n\tv_mbcnt_hi_u32_b32 %0, -1, %0" : "=v"(l_)); \
    const u32 n_ = l_ & 15, g_ = l_ >> 4; \
    voffK = (n_ >> 1) * 256 + (2 * g_ + (n_ & 1)) * 16; voffV = g_ * 512 + n_ * 16; \
    const u32 h2 = (n_ >> 1) & 1, by = (2 * h2) | ((2 * h2 + 1) << 8); \
    const u32 on = (n_ & 1) ? ((by << 16) | 0x0c0cu) : (0x0c0c0000u | by); \
    _Pragma("unroll") for (int i = 0; i < 4; ++i) sel[i] = (n_ >> 2) == i ? on : 0x0c0c0c0cu; } while (0)
  const int pair = isA ? r >> 1 : isBA ? r & 1 : (r & 1) ^ 1, prod = isA ? r & 1 : isBA ? 2 : 3;
  const int dp = (g & 1) ? -DPAR : DPAR, db = (g & 1) ? -DPB : DPB;
  const int c0 = prod == 0 ? 0 : prod == 1 ? C_A + dp : prod == 2 ? 2 * C_A : 2 * C_A + C_BA + (pair ? -db : db);
  const int c1 = prod == 0 ? C_A + dp : prod == 1 ? 2 * C_A : prod == 2 ? 2 * C_A + C_BA + (pair ? -db : db) : 128;
  const size_t kv0 = ((size_t)((g & 1) * 2 + pair) * 16 + kvh) * (4096 * 256) + 4096;
  const u32 q_off = WS_E2 + (g * 2 + pair) * 2048 + lane * 16;
#define LDK(id, j) ldnt(p.AK + kv0 + (size_t)(id) * 8192, voffK, ((j) >> 1) * 2048 + ((j) & 1) * 128 - 4096)
#define LDV(id, j) ldnt(p.AV + kv0 + (size_t)(id) * 8192, voffV, ((j) >> 1) * 2048 + ((j) & 1) * 256 - 4096)
  const bool t1 = lane & 1;
  const int ci = (g & 1) * 4 + (r & 3), nu = (ci & 3) == 3 ? 3 : 2;
#define LOAD_WO(DST, SRC, NU, NI) do { \
    const u32 vx = (lane >> 3) * 1152 + (((lane & 7) ^ sw2((lane >> 3) | ((w & 1) << 3))) * 16); \
    u32 lx = (DST) + hw * 1024; \
    _Pragma("unroll 1") for (int u = 0; u < (NU); ++u) \
      _Pragma("unroll") for (int i = 0; i < (NI); ++i) { dma16((SRC) + (u32)(u * 128 + (32 * i + 8 * hw) * 1152), vx, lx); lx += 4096; } } while (0)

  v4u K[8], V[8], QF[8];
  Acc acc;
  bool chk = false;

  if (!isBA && !hlp) {
    STREAM_CONSTS();
    if (DBURST) VMWAIT(9);
    BAR();
    if (!isA) {
      if (UVSPLIT >= 2) BAR();
      if (UVSPLIT) {
#pragma unroll 1
        for (int i = UVSPLIT == 3 ? 0 : 8; i < 16; ++i) { VMWAIT(8); dma16(p.Bwuv, vuv, L_UV + w * 1024 + i * 4096); vuv += 32 * 8192; }
      }
      if (UVSPLIT < 2) BAR();
    }
    v4u K0[8], V0[8];
#define PACED(R, LD, id) do { _Pragma("unroll") for (int j = 0; j < 8; ++j) { VMWAIT(PACE); R[j] = LD(id, j); SB(); } } while (0)
    PACED(K0, LDK, c0 + w);
    VMWAIT(8);
    if (isA) BAR();
    PACED(V0, LDV, c0 + w);
    if (PARK && isA) {
      const char* kp = p.AK + kv0 + (size_t)(c0 + w + 8) * 8192 - 4096;
      const char* vp = p.AV + kv0 + (size_t)(c0 + w + 8) * 8192 - 4096;
#pragma unroll 1
      for (int j = 0; j < 8; ++j) { VMWAIT(8); dma16(kp + (j >> 1) * 2048 + (j & 1) * 128, voffK, L_PK + w * 8192 + j * 1024); }
#pragma unroll 1
      for (int j = 0; j < 8; ++j) { VMWAIT(8); dma16(vp + (j >> 1) * 2048 + (j & 1) * 256, voffV, L_PV + w * 8192 + j * 1024); }
    }
    PACED(K, LDK, c0 + w + 4);
    PACED(V, LDV, c0 + w + 4);
    a_reset(acc);
    if (w == 0) STAMP(1);
    BAR();
    if (w == 0) STAMP(2);
    a_loadq(L, lane, QF);
#pragma unroll 1
    for (int it = (PARK && isA) ? 0 : 1;; ++it) {
      const u32 PP = a_soft(a_qk(K0, QF, t1), acc);
      a_pv(PP, V0, sel, acc);
      if (it) break;
#pragma unroll
      for (int j = 0; j < 8; ++j) { K0[j] = lld128(L, L_PK + w * 8192 + j * 1024 + lane * 16); V0[j] = lld128(L, L_PV + w * 8192 + j * 1024 + lane * 16); }
    }
    chk = DYNP && !isA;
  } else if (isA) {
    const u32 xo = L_XV + (lane & 3) * 1152 + g4 * 16;
    VMWAIT(0);
    if (w == 4) STAMP(23);
    BAR();
    if (w == 4) STAMP(3);
    f32x4 d = gemv288(L, L_QBW + tr128, xo + (r >> 1) * 576);
    st_sc1(rs, n16 < 4 ? WS_E1 + ((g * 2 + (r >> 1)) * 4 + n16) * 512 + (r & 1) * 256 + hw * 64 + g4 * 16 : OOB, tag4(d, tA));
    if (w == 4) STAMP(4);
    BAR();
    d = gemv288(L, L_QAW + tr128, xo + 4608 + (ai >> 2) * 576);
    st_sc1(rs, n16 < 4 ? WS_E2 + ((kvh * 4 + n16) * 2 + (ai >> 2)) * 1024 + (ai & 3) * 256 + hw * 64 + g4 * 16 : OOB, tag4(d, tA));
    if (w == 4) STAMP(5);
    if (w == 4) *(LAS v2u*)(L + L_QS + lane * 8) = fetch_q(rs, q_off, 1024, tA);
    if (w == 4) STAMP(6);
    BAR();
  } else if (isBA) {
    const int b = bi >> 4, sp = bi & 15;
    const char* cb = p.BC + (u32)((bi * 256 + w + 128) * 1024);
    const u32 vc0 = (lane ^ fsw(w)) * 16;
    v4u qs[4], c2[16];
#pragma unroll
    for (int i = 0; i < C2_EARLY; ++i) c2[i] = ldnt(cb + 8 * i * 1024, vc0 ^ ((i & 1) * 32), 0);
    SB();
    poll_flags<FQ_SLEEP>(rs, WS_FQ + (8 * w + (lane & 7)) * FSTR, tA, tB);
    if (w == 4) STAMP(7);
#pragma unroll
    for (int i = 0; i < 4; ++i) qs[i] = ld_sc1(rs, WS_QABS + (b * 32 + 4 * w + i) * 1024 + (lane ^ fsw(4 * w + i)) * 16);
    SB();
    asm volatile("s_barrier" ::: "memory");
#pragma unroll
    for (int i = C2_EARLY; i < C2B(1); ++i) c2[i] = ldnt(cb + 8 * i * 1024, vc0 ^ ((i & 1) * 32), 0);
    SB();
#pragma unroll
    for (int i = 0; i < 4; ++i) lst128(L, L_X + (4 * w + i) * 1024 + lane * 16, qs[i]);
    asm volatile("" ::: "memory");
    v4u K0[8], V0[8];
    if (EARLYK && !hlp) {
      STREAM_CONSTS();
#pragma unroll
      for (int j = 0; j < 8; ++j) K0[j] = LDK(c0 + w, j);
      SB();
    }
    BAR();
    v4u qf[16];
    {
      const u32 rowb = L_X + (lane & 31) * 1024 + (w >> 2) * 512, f = fsw(lane & 31);
#pragma unroll
      for (int ks = 0; ks < 16; ++ks) qf[ks] = lld128(L, rowb + (ks >> 3) * 256 + (((2 * (ks & 7) + (lane >> 5)) ^ f) * 16));
    }
    if (!LATEBAR) BAR();
#pragma unroll
    for (int i = C2B(1); i < C2B(2); ++i) c2[i] = ldnt(cb + 8 * i * 1024, vc0 ^ ((i & 1) * 32), 0);
    if (w == 0) STAMP(8);
    f32x16 ac[2];
    float m, l;
    SB();
    ba_pass<true>(L, w, lane, qf, ac, m, l, [&](int at) {
      if (at == 0) {
#pragma unroll
        for (int i = C2B(2); i < C2B(3); ++i) c2[i] = ldnt(cb + 8 * i * 1024, vc0 ^ ((i & 1) * 32), 0);
      } else {
#pragma unroll
        for (int i = C2B(3); i < 16; ++i) c2[i] = ldnt(cb + 8 * i * 1024, vc0 ^ ((i & 1) * 32), 0);
      }
      SB();
    });
    if (tid < 8) *(LAS u32*)(L + L_SYNC + tid * 4) = 0u;
    BAR();
    if (w == 0) STAMP(9);
#pragma unroll
    for (int i = 0; i < 16; ++i) lst128(L, (8 * i + w) * 1024 + lane * 16, c2[i]);
    v2u qpk = {0u, 0u};
    if (w == 4) qpk = fetch_q(rs, q_off, 1024, tA);
    BAR();
    if (w == 0) STAMP(10);
    if (!hlp) {
      STREAM_CONSTS();
      if (EARLYK) {
#pragma unroll
        for (int j = 0; j < 8; ++j) V0[j] = LDV(c0 + w, j);
      } else {
#pragma unroll
        for (int j = 0; j < 8; ++j) K0[j] = LDK(c0 + w, j);
      }
      SB();
    }
    ba_pass<false>(L, w, lane, qf, ac, m, l, [&](int at) {
      if (!hlp) {
        STREAM_CONSTS();
        if (at == 0 && !EARLYK && BA_PF >= 2) {
#pragma unroll
          for (int j = 0; j < 8; ++j) V0[j] = LDV(c0 + w, j);
        } else if (BA_PF >= 3 && at == (EARLYK == 1 ? 0 : 1)) {
#pragma unroll
          for (int j = 0; j < 8; ++j) K[j] = LDK(c0 + w + 4, j);
        }
        SB();
      }
    });
    if (w == 0) STAMP(29);
    l = x32sum(l);
    if (lane < 32) *(LAS float*)(L + L_WL + (w * 32 + lane) * 4) = l;
    BAR();
    {
      const u32 hd = lane & 31, hh = lane >> 5;
#pragma unroll
      for (int T = 0; T < 2; ++T)
#pragma unroll
        for (int rq = 0; rq < 4; ++rq)
          *(LAS v2u*)(L + hd * 1024 + (((8 * w + 4 * T + rq) ^ (hd & 15)) * 16) + 8 * hh) =
              v2u{pk2(ac[T][4 * rq], ac[T][4 * rq + 1]), pk2(ac[T][4 * rq + 2], ac[T][4 * rq + 3])};
    }
    if (w == 4) *(LAS v2u*)(L + L_QS + lane * 8) = qpk;
    BAR();
    if (hlp) {
      if (PS_T) while ((u32)__builtin_amdgcn_s_memrealtime() - tbeg < PS_T) __builtin_amdgcn_s_sleep(1);
#pragma unroll
      for (int i = 0; i < 8; ++i) {
        const u32 hd = 8 * hw + i;
        st_sc1(rs, WS_PART + (PLAY ? bi * 32 + hd : (hd * 4 + b) * 16 + sp) * 1024 + lane * 16, lld128(L, hd * 1024 + ((lane ^ (hd & 15)) * 16)));
      }
      if (w == 4 && lane < 32) {
        float ls = 0.f;
#pragma unroll
        for (int i = 0; i < 8; ++i) ls += *(LAS float*)(L + L_WL + (i * 32 + lane) * 4);
        st_sc1_8(rs, WS_ML + (MLAY ? bi * 32 + lane : (lane * 4 + b) * 16 + sp) * 8, v2u{asu(m), asu(ls)});
      }
      VMWAIT(0);
      if (lds_inc(L, SY_A) == 3 && lane == 0) st_sc1_8(rs, WS_FP + bi * FSTR, v2u{tA, tB});
      if (w == 4) STAMP(11);
      lds_wait(L, SY_LAST, LATE_A);
      LOAD_WO(L_UV, p.Awo + (u32)((kvh * 256 + (ci >> 2) * 128) * 1152 + (ci & 3) * 256), nu, 4);
      VMWAIT(0);
      lds_inc(L, SY_C1);
    } else {
      STREAM_CONSTS();
      a_reset(acc);
      a_loadq(L, lane, QF);
      if (BA_PF >= 2) {
        const u32 PP = a_soft(a_qk(K0, QF, t1), acc);
        a_pv(PP, V0, sel, acc);
        asm volatile("" : "+v"(acc.o)::"memory");
      }
      if (BA_WAIT) lds_wait(L, SY_A, 4);
      if (BA_PF < 2) {
#pragma unroll
        for (int j = 0; j < 8; ++j) V0[j] = LDV(c0 + w, j);
      }
      if (BA_PF < 3) {
#pragma unroll
        for (int j = 0; j < 8; ++j) K[j] = LDK(c0 + w + 4, j);
      }
      if (BA_PF < 2) {
        SB();
        const u32 PP = a_soft(a_qk(K0, QF, t1), acc);
        a_pv(PP, V0, sel, acc);
        asm volatile("" : "+v"(acc.o)::"memory");
      }
#pragma unroll
      for (int j = 0; j < 8; ++j) V[j] = LDV(c0 + w + 4, j);
      SB();
    }
  } else {
    if (w < 6) *(LAS v2u*)(L + L_QB + (2 * hw + (lane >> 5)) * 320 + (lane & 31) * 8) = fetch_q(rs, WS_E1 + h * 4096 + hw * 1024 + lane * 16, 2048, tA);
    VMWAIT(0);
    BAR();
    if (w == 4) STAMP(12);
    {
      f32x4 d[4];
#pragma unroll
      for (int T = 0; T < 4; ++T) d[T] = f32x4{0.f, 0.f, 0.f, 0.f};
#pragma unroll
      for (int ks = 0; ks < 4; ++ks) {
        const v4u qb = lld128(L, L_QB + (lane & 3) * 320 + ks * 64 + g4 * 16);
        const u32 a0 = (64 * hw + n16) * 256 + (((4 * ks + g4) ^ n16) * 16);
#pragma unroll
        for (int T = 0; T < 4; ++T) d[T] = mfma16(lld128(L, a0 + T * 4096), qb, d[T]);
      }
      const int T = n16 >> 2;
      const f32x4 e = sel4(T & 2, sel4(T & 1, d[3], d[2]), sel4(T & 1, d[1], d[0]));
      st_sc1_8(rs, WS_QABS + ((lane & 3) * 32 + h) * 1024 + (half * 256 + 64 * hw + 16 * T + 4 * g4) * 2, v2u{pk2(e[0], e[1]), pk2(e[2], e[3])});
    }
    VMWAIT(0);
    BAR();
    if (tid == 256) st_sc1_8(rs, WS_FQ + bw * FSTR, v2u{tA, tB});
    if (w == 4) STAMP(13);
    if (w == 4) *(LAS v2u*)(L + L_QS + lane * 8) = fetch_q(rs, q_off, 1024, tA);
    BAR();
    if (!LATEB) LOAD_WO(L_WO, p.Bwo + (u32)(bw * 64 * 1152), 9, 2);
    v4u uf[16];
#pragma unroll
    for (int ks = 0; ks < 16; ++ks) uf[ks] = ltr2(L, L_UV + tr128 + ks * 4096, L_UV + tr128 + ks * 4096 + 512);
    lds_inc(L, SY_UF);
    if (w == 4) STAMP(24);
    if (GWAIT) lds_wait(L, GWAIT == 1 ? SY_LAST : SY_B, 4);
    poll_flags<FP_SLEEP>(rs, WS_FP + (16 * hw + n16) * FSTR, tA, tB);
    if (lane == 0) *(volatile LAS u32*)(L + SY_HOLD) = 1u;
    if (w == 4) STAMP(25);
    {
      const v2u ml = ld_sc1_8(rs, WS_ML + (MLAY ? (hw * 16 + n16) * 32 + h : (h * 4 + hw) * 16 + n16) * 8);
      v4u pt[16];
#pragma unroll
      for (int i = 0; i < 16; ++i) pt[i] = ld_sc1(rs, WS_PART + (PLAY ? (hw * 16 + i) * 32 + h : (h * 4 + hw) * 16 + i) * 1024 + lane * 16);
      float M = fmaxf(asf(ml.x), dppf<XOR1>(asf(ml.x)));
      M = fmaxf(M, dppf<XOR2>(M)); M = fmaxf(M, dppf<ROR4>(M)); M = fmaxf(M, dppf<ROR8>(M));
      const float wt = ex2(asf(ml.x) - M);
      float Ls = wt * asf(ml.y);
      Ls += dppf<XOR1>(Ls); Ls += dppf<XOR2>(Ls); Ls += dppf<XOR7>(Ls); Ls += dppf<XOR15>(Ls);
      const float wn = wt / Ls;
      float c[8];
#pragma unroll
      for (int k = 0; k < 8; ++k) c[k] = 0.f;
#pragma unroll
      for (int i = 0; i < 16; ++i) {
        const float wi = asf(__builtin_amdgcn_readlane(asu(wn), i));
#pragma unroll
        for (int k = 0; k < 4; ++k) { c[2 * k] += wi * asf(pt[i][k] << 16); c[2 * k + 1] += wi * asf(pt[i][k] & 0xffff0000u); }
      }
      lst128(L, L_CL + hw * 1088 + lane * 16, v4u{pk2(c[0], c[1]), pk2(c[2], c[3]), pk2(c[4], c[5]), pk2(c[6], c[7])});
    }
    if (LATEB == 1) LOAD_WO(L_WO, p.Bwo + (u32)(bw * 64 * 1152), 9, 2);
    lds_inc(L, SY_A);
    lds_wait(L, SY_A, 4);
    if (w == 4) STAMP(26);
    {
      f32x4 d = {0.f, 0.f, 0.f, 0.f};
#pragma unroll
      for (int ks = 0; ks < 16; ++ks) d = mfma16(lld128(L, L_CL + (lane & 3) * 1088 + ks * 64 + g4 * 16), uf[ks], d);
      const float dv = (g4 & 2) ? ((g4 & 1) ? d[3] : d[2]) : ((g4 & 1) ? d[1] : d[0]);
      *(LAS u16*)(L + L_OA + g4 * 320 + (16 * hw + n16) * 2) = (u16)pk2(dv, dv);
    }
    if (w == 4) STAMP(27);
    if (LATEB == 3) {
      while ((u32)__builtin_amdgcn_s_memrealtime() - tbeg < WB_T) __builtin_amdgcn_s_sleep(2);
      LOAD_WO(L_WO, p.Bwo + (u32)(bw * 64 * 1152), 9, 2);
    }
  }

  if (!hlp) {
#define YIELD() do { if (chk && __builtin_amdgcn_readfirstlane(*(volatile LAS u32*)(L + SY_HOLD))) { lds_wait(L, SY_A, 4); chk = false; } } while (0)
    float S = a_qk(K, QF, t1);
    for (int id = c0 + w + ((PARK && isA) ? 12 : 8); id < c1; id += 4) {
      YIELD();
      if (PAUSE_AT && !isA && !isBA && id == c0 + w + 4 * PAUSE_AT) lds_wait(L, SY_A, 4);
      asm volatile("" : "+v"(S)::"memory");
#pragma unroll
      for (int j = 0; j < 8; ++j) K[j] = LDK(id, j);
      SB();
      const u32 PP = a_soft(S, acc);
      a_pv(PP, V, sel, acc);
      asm volatile("" : "+v"(acc.o)::"memory");
      YIELD();
#pragma unroll
      for (int j = 0; j < 8; ++j) V[j] = LDV(id, j);
      SB();
      if (id + 4 >= c1 && lane == 0) __hip_atomic_fetch_add((LAS u32*)(L + SY_LAST), 1u, __ATOMIC_RELAXED, __HIP_MEMORY_SCOPE_WORKGROUP);
      S = a_qk(K, QF, t1);
    }
    const u32 PP = a_soft(S, acc);
    float ls = acc.l + dppf<XOR1>(acc.l);
    ls += dppf<ROR4>(ls);
    ls += dppf<ROR8>(ls);
    ls = x32sum(x16sum(ls));
    u32 fa = L_FL + lane * 16, ma = L_FM + (lane & 2) * 4, sa = SY_B, one = 1;
    u32 eo = WS_E3 + (((g * 2 + pair) * 4 + prod) * 2 + ((lane >> 1) & 1)) * 512 + g4 * 128 + (((n16 >> 2) << 1) | (n16 & 1)) * 16;
    if ((lane & 61) == 0) *(LAS f32x2*)(L + ma + w * 16) = f32x2{acc.m, ls};
    asm volatile("" : "+v"(fa), "+v"(ma), "+v"(sa), "+v"(one), "+v"(eo)::"memory");
    a_pv(PP, V, sel, acc);
    *(LAS f32x4*)(L + fa + w * 1024) = acc.o;
    u32 old;
    u64 ex;
    asm volatile("s_mov_b64 %1, exec\n\ts_mov_b64 exec, 1\n\tds_add_rtn_u32 %0, %2, %3\n\ts_mov_b64 exec, %1" : "=&v"(old), "=&s"(ex) : "v"(sa), "v"(one) : "memory");
    f32x2 ml[4];
    f32x4 aw[4];
#pragma unroll
    for (int i = 0; i < 4; ++i) ml[i] = *(LAS f32x2*)(L + ma + i * 16);
#pragma unroll
    for (int i = 0; i < 4; ++i) aw[i] = *(LAS f32x4*)(L + fa + i * 1024);
    const float M = fmaxf(fmaxf(ml[0].x, ml[1].x), fmaxf(ml[2].x, ml[3].x));
    float wt[4], Ls = 0.f;
#pragma unroll
    for (int i = 0; i < 4; ++i) { wt[i] = ex2(ml[i].x - M); Ls += wt[i] * ml[i].y; }
    asm volatile("" : "+v"(old) : "v"(Ls));
    const bool last = __builtin_amdgcn_readfirstlane(old) == 3;
    if (last) STAMP(16);
    const f32x4 o = (wt[0] * aw[0] + wt[1] * aw[1] + wt[2] * aw[2] + wt[3] * aw[3]) * __builtin_amdgcn_rcpf(Ls);
    const u32 lse = asu(M + __builtin_amdgcn_logf(Ls));
    const u32 p01 = pk2(o[0], o[1]), p23 = pk2(o[2], o[3]);
    st_sc1(rs, last ? eo : OOB, v4u{__builtin_amdgcn_perm(p01, tA, 0x07060500u), __builtin_amdgcn_perm(p23, tA, 0x07060501u), __builtin_amdgcn_perm(lse, tA, 0x07060502u),
                                    __builtin_amdgcn_perm(p01, tA, 0x0c0c0403u) | __builtin_amdgcn_perm(lse, p23, 0x04000c0cu)});
    if (last) STAMP(15);
    if (!isA) {
      if (!isBA) {
        if (LATEB == 2) {
          LOAD_WO(L_WO, p.Bwo + (u32)(bw * 64 * 1152), 9, 2);
          VMWAIT(0);
          lds_inc(L, SY_WO);
        }
        lds_wait(L, WOB_AFTER ? SY_A : SY_UF, 4);
        LOAD_WO(L_UV, p.Awo + (u32)((kvh * 256 + (ci >> 2) * 128) * 1152 + (ci & 3) * 256), nu, 4);
        VMWAIT(0);
        lds_inc(L, SY_C1);
      }
      const int i16 = lane >> 2;
      u32 go = WS_E3 + (((kvh * 4 + hw) * 4 + (lane & 3)) * 2 + (ci >> 2)) * 512 + i16 * 16;
      u32 oa = L_OB + hw * 320 + (4 * (i16 & 8) + (i16 & 7) + 8 * (lane & 3)) * 2, ra = L_OB + (lane & 3) * 320 + g4 * 16;
      u32 e4 = g4 < nu ? WS_E4 + ((16 * (ci & 3) + 8 * g4 + 2 * hw + (n16 >> 3)) * 32 + kvh * 2 + (ci >> 2)) * 128 + (n16 & 7) * 16 : OOB;
      u32 s2 = SY_C2;
      asm volatile("" : "+v"(go), "+v"(oa), "+v"(ra), "+v"(e4), "+v"(s2));
      lds_wait(L, SY_C1, 4);
      if (w == 0) STAMP(28);
      v4u wf[12];
#pragma unroll
      for (int i = 0; i < 12; ++i) wf[i] = ltr2(L, L_UV + tr128 + i * 4096, L_UV + tr128 + i * 4096 + 512);
      if (GATE && !isBA) lds_wait(L, SY_WO, LATEB == 2 ? 8 : 4);
      v4u v[2];
      poll16<2, true>(rs, go, 256, v, tA, tB);
      const float m = asf(__builtin_amdgcn_perm(v[0].z, v[0].w, 0x07060503u));
      float Mx = fmaxf(m, dppf<XOR1>(m));
      Mx = fmaxf(Mx, dppf<XOR2>(Mx));
      float wq = ex2(m - Mx), Lq = wq + dppf<XOR1>(wq);
      Lq += dppf<XOR2>(Lq);
      wq *= __builtin_amdgcn_rcpf(Lq);
      float y[4];
#pragma unroll
      for (int a = 0; a < 4; ++a) {
        const u32 x = a < 2 ? v[a].x : v[a - 2].y;
        const float lo = wq * asf(__builtin_amdgcn_perm(x, v[a & 1].w, a < 2 ? 0x05010c0cu : 0x05020c0cu)), hi = wq * asf(x & 0xffff0000u);
        y[a] = (t1 ? hi : lo) + dppf<XOR1>(t1 ? lo : hi);
      }
      const bool t2 = lane & 2;
      const float z0 = (t2 ? y[2] : y[0]) + dppf<XOR2>(t2 ? y[0] : y[2]), z1 = (t2 ? y[3] : y[1]) + dppf<XOR2>(t2 ? y[1] : y[3]);
      const u32 zz = pk2(z0, z1);
      *(LAS u16*)(L + oa) = (u16)zz;
      *(LAS u16*)(L + oa + 128) = (u16)(zz >> 16);
      asm volatile("s_mov_b64 %0, exec\n\ts_mov_b64 exec, 1\n\tds_add_u32 %1, %2\n\ts_mov_b64 exec, %0" : "=&s"(ex) : "v"(s2), "v"(one) : "memory");
      v4u af[4];
      for (u32 spins = 0;; ++spins) {
        const u32 cnt = *(volatile LAS u32*)(L + s2);
        asm volatile("" ::: "memory");
#pragma unroll
        for (int ks = 0; ks < 4; ++ks) af[ks] = lld128(L, ra + ks * 64);
        asm volatile("" : "+v"(af[0]), "+v"(af[1]), "+v"(af[2]), "+v"(af[3])::"memory");
        if (__builtin_amdgcn_readfirstlane(cnt) >= 4 || spins > (1u << 24)) break;
      }
      if (w == 0) STAMP(17);
      f32x4 e[3];
#pragma unroll
      for (int u = 0; u < 3; ++u) e[u] = f32x4{0.f, 0.f, 0.f, 0.f};
#pragma unroll
      for (int ks = 0; ks < 4; ++ks)
#pragma unroll
        for (int u = 0; u < 3; ++u) e[u] = mfma16(af[ks], wf[4 * u + ks], e[u]);
      st_sc1(rs, e4, tag4(sel4(g4 & 2, e[2], sel4(g4 & 1, e[1], e[0])), tA));
      if (w == 0) STAMP(30);
    }
  } else if (isA) {
    const int oct = r * 32 + g;
    if (w == 4 && oct < 72) {
      reduce_oct<4>(rs, WS_E4 + oct * 4096 + lane * 16, tA, p.out + oct * 8, lane, [&] { lds_wait(L, SY_B, 4); });
      if (oct == 0 && lane == 0) __hip_atomic_store((GAS u64*)(p.ws + WS_EPOCH), ((u64)tB << 32) | tA, RLX_AGENT);
      STAMP(19);
    }
  } else if (!isBA) {
    VMWAIT(0);
    lds_inc(L, SY_WO);
    lds_wait(L, SY_WO, LATEB == 2 ? 8 : 4);
    const v4u af0 = lld128(L, L_OA + (lane & 3) * 320 + g4 * 16), af1 = lld128(L, L_OA + (lane & 3) * 320 + 64 + g4 * 16);
    f32x4 e[9];
#pragma unroll
    for (int u = 0; u < 9; ++u) {
      const u32 t0 = L_WO + tr128 + u * 8192;
      e[u] = mfma16(af1, ltr2(L, t0 + 4096, t0 + 4608), mfma16(af0, ltr2(L, t0, t0 + 512), f32x4{0.f, 0.f, 0.f, 0.f}));
    }
    const u32 eo = WS_E6 + ((8 * g4 + 2 * hw + (n16 >> 3)) * 64 + bw) * 128 + (n16 & 7) * 16;
#pragma unroll
    for (int k = 0; k < 2; ++k)
      st_sc1(rs, eo + k * 32 * 8192, tag4(sel4(g4 & 2, sel4(g4 & 1, e[4 * k + 3], e[4 * k + 2]), sel4(g4 & 1, e[4 * k + 1], e[4 * k])), tA));
    st_sc1(rs, g4 == 0 ? eo + 64 * 8192 : OOB, tag4(e[8], tA));
    if (w == 4) STAMP(18);
    const int oct = hw * 64 + bw;
    if (oct < 72) {
      reduce_oct<8>(rs, WS_E6 + oct * 8192 + lane * 16, tA, p.out + 576 + oct * 8, lane, [] {});
      STAMP(14);
    }
  }
  asm volatile("" ::"v"(pf));
}

extern "C" void kernel_launch(void* const* d_in, const int* in_sizes, int n_in, void* d_out, int out_size, void* d_ws, size_t ws_size,
                              hipStream_t stream) {
  Params p;
  p.Ax = (const char*)d_in[0]; p.AK = (const char*)d_in[1]; p.AV = (const char*)d_in[2]; p.Awq = (const char*)d_in[3]; p.Awo = (const char*)d_in[4];
  p.Bx = (const char*)d_in[5]; p.BC = (const char*)d_in[6]; p.Bwq = (const char*)d_in[7]; p.Bwuk = (const char*)d_in[8]; p.Bwuv = (const char*)d_in[9];
  p.Bwo = (const char*)d_in[10];
  p.out = (u16*)d_out;
  p.ws = (char*)d_ws;
  (void)hipFuncSetAttribute((const void*)decode_mega, hipFuncAttributeMaxDynamicSharedMemorySize, LDS_BYTES);
  decode_mega<<<256, 512, LDS_BYTES, stream>>>(p);
}
```

```cpp
#include <hip/hip_runtime.h>
#include <stdint.h>

typedef unsigned int u32;
typedef unsigned long long u64;
typedef unsigned short u16;
typedef u32 v2u __attribute__((ext_vector_type(2)));
typedef u32 v4u __attribute__((ext_vector_type(4)));
typedef u32 v8u __attribute__((ext_vector_type(8)));
typedef u32 v16u __attribute__((ext_vector_type(16)));
typedef float f32x2 __attribute__((ext_vector_type(2)));
typedef float f32x4 __attribute__((ext_vector_type(4)));
typedef float f32x16 __attribute__((ext_vector_type(16)));
typedef __bf16 bf16x8 __attribute__((ext_vector_type(8)));
typedef __bf16 bf16x2 __attribute__((ext_vector_type(2)));
typedef short s16x4 __attribute__((ext_vector_type(4)));
typedef __amdgpu_buffer_rsrc_t rsrc_t;
#define DI __device__ __forceinline__
#define LAS __attribute__((address_space(3)))
#define GAS __attribute__((address_space(1)))

constexpr int C_A = 44, C_BA = 16;
static_assert(C_A % 4 == 0 && C_BA % 4 == 0 && C_BA >= 12 && 128 - 2 * C_A - C_BA >= 12, "every streamer runs the loop at least once");

constexpr int WS_EPOCH = 0;
constexpr int WS_ML = 16384;
constexpr int WS_FQ = 32768;
constexpr int WS_FP = 49152;
constexpr int WS_QABS = 65536;
constexpr int WS_E1 = 262144;
constexpr int WS_E2 = 393216;
constexpr int WS_E3 = 524288;
constexpr int WS_E6 = 1 << 20;
constexpr int WS_E4 = WS_E6 + 72 * 8192;
constexpr int WS_PART = 2 << 20;
constexpr int WS_BYTES = 4 << 20;
constexpr u32 OOB = 0x7f000000u;
constexpr int LDS_BYTES = 163840;
constexpr int L_QBW = 0, L_QAW = 36864;
constexpr int L_XV = 110592;
constexpr int L_WO = 0;
constexpr int L_UV = 73728;
constexpr int L_X = 131072;
constexpr int L_SX = L_X, L_P = L_X + 16384, L_WM = L_X + 24576, L_WL = L_X + 25600;
constexpr int L_QS = 139264;
constexpr int L_FL = L_QS + 1024;
constexpr int L_FM = L_FL + 4096;
constexpr int L_OA = L_FM + 128;
constexpr int L_QB = L_OA + 1280;
constexpr int L_OB = L_QB;
constexpr int L_CL = L_QB + 1280;
constexpr int L_SYNC = L_X + 26624;
static_assert(L_CL + 4352 <= L_WM, "LDS map");
enum { SY_A = L_SYNC, SY_B = L_SYNC + 4, SY_LAST = L_SYNC + 8, SY_C1 = L_SYNC + 12, SY_C2 = L_SYNC + 16, SY_WO = L_SYNC + 20 };

constexpr float SCALE = 0.08838834764831845f;
constexpr float LOG2E = 1.4426950408889634f;
constexpr u32 SPIN_CAP = 1u << 18;

struct Params {
  const char *Ax, *AK, *AV, *Awq, *Awo, *Bx, *BC, *Bwq, *Bwuk, *Bwuv, *Bwo;
  u16* out;
  char* ws;
};

DI f32x4 mfma16(v4u a, v4u b, f32x4 c) {
  return __builtin_amdgcn_mfma_f32_16x16x32_bf16(__builtin_bit_cast(bf16x8, a), __builtin_bit_cast(bf16x8, b), c, 0, 0, 0);
}
DI f32x16 mfma32(v4u a, v4u b, f32x16 c) {
  return __builtin_amdgcn_mfma_f32_32x32x16_bf16(__builtin_bit_cast(bf16x8, a), __builtin_bit_cast(bf16x8, b), c, 0, 0, 0);
}
DI u32 pk2(float a, float b) { f32x2 v = {a, b}; return __builtin_bit_cast(u32, __builtin_convertvector(v, bf16x2)); }
DI float rbf(float a) { return (float)(__bf16)a; }
DI float asf(u32 x) { return __builtin_bit_cast(float, x); }
DI u32 asu(float x) { return __builtin_bit_cast(u32, x); }
DI float ex2(float x) { return __builtin_amdgcn_exp2f(x); }
DI u32 perm(u32 a, u32 b, u32 s) { return __builtin_amdgcn_perm(a, b, s); }
template <int CTRL> DI float dppf(float x) { return asf(__builtin_amdgcn_mov_dpp(asu(x), CTRL, 0xf, 0xf, true)); }
template <int CTRL> DI u32 dppu(u32 x) { return __builtin_amdgcn_mov_dpp(x, CTRL, 0xf, 0xf, true); }
constexpr int XOR1 = 0xB1, XOR2 = 0x4E, XOR7 = 0x141, XOR15 = 0x140, ROR8 = 0x128, ROR4 = 0x124, BCAST = 0x150;
DI float x16max(float x) { auto s = __builtin_amdgcn_permlane16_swap(asu(x), asu(x), false, false); return fmaxf(asf(s[0]), asf(s[1])); }
DI float x32max(float x) { auto s = __builtin_amdgcn_permlane32_swap(asu(x), asu(x), false, false); return fmaxf(asf(s[0]), asf(s[1])); }
DI float x16sum(float x) { auto s = __builtin_amdgcn_permlane16_swap(asu(x), asu(x), false, false); return asf(s[0]) + asf(s[1]); }
DI float x32sum(float x) { auto s = __builtin_amdgcn_permlane32_swap(asu(x), asu(x), false, false); return asf(s[0]) + asf(s[1]); }
DI float sum_hi3(f32x4 a) {
  auto s0 = __builtin_amdgcn_permlane32_swap(asu(a[0]), asu(a[1]), false, false);
  auto s1 = __builtin_amdgcn_permlane32_swap(asu(a[2]), asu(a[3]), false, false);
  auto t = __builtin_amdgcn_permlane16_swap(asu(asf(s0[0]) + asf(s0[1])), asu(asf(s1[0]) + asf(s1[1])), false, false);
  const float c = asf(t[0]) + asf(t[1]);
  return c + dppf<ROR8>(c);
}
DI f32x4 sel4(bool c, f32x4 a, f32x4 b) { return f32x4{c ? a[0] : b[0], c ? a[1] : b[1], c ? a[2] : b[2], c ? a[3] : b[3]}; }

#define BAR() asm volatile("s_waitcnt lgkmcnt(0)\n\ts_barrier" ::: "memory")
#define VMWAIT(n) asm volatile("s_waitcnt vmcnt(%0)" ::"n"(n) : "memory")
#define SB() __builtin_amdgcn_sched_barrier(0)

DI v4u lld128(LAS char* L, u32 o) { return *(LAS v4u*)(L + o); }
DI void lst128(LAS char* L, u32 o, v4u v) { *(LAS v4u*)(L + o) = v; }
DI v2u ltr(LAS char* L, u32 o) { return __builtin_bit_cast(v2u, __builtin_amdgcn_ds_read_tr16_b64_v4i16((LAS s16x4*)(L + o))); }
DI v4u ltr2(LAS char* L, u32 o0, u32 o1) { v2u a = ltr(L, o0), b = ltr(L, o1); return v4u{a.x, a.y, b.x, b.y}; }
DI void dma16(const char* sbase, u32 voff, u32 lds) {
  u32 keep;
  asm volatile("s_mov_b32 %0, m0\n\ts_mov_b32 m0, %3\n\ts_nop 2\n\tglobal_load_lds_dwordx4 %1, %2 nt\n\ts_mov_b32 m0, %0"
               : "=&s"(keep) : "v"(voff), "s"(sbase), "s"(lds) : "memory");
}
DI v4u ldnt(const char* base, u32 voff, int imm) { return __builtin_nontemporal_load((const GAS v4u*)((const GAS char*)base + voff + imm)); }
DI v4u ld_sc1(rsrc_t rs, u32 o) { return __builtin_amdgcn_raw_buffer_load_b128(rs, o, 0, 16); }
DI v2u ld_sc1_8(rsrc_t rs, u32 o) { return __builtin_amdgcn_raw_buffer_load_b64(rs, o, 0, 16); }
DI void st_sc1(rsrc_t rs, u32 o, v4u v) { __builtin_amdgcn_raw_buffer_store_b128(v, rs, o, 0, 16); }
DI void st_sc1_8(rsrc_t rs, u32 o, v2u v) { __builtin_amdgcn_raw_buffer_store_b64(v, rs, o, 0, 16); }
DI v4u tag4(f32x4 e, u32 tA) {
  v4u v;
#pragma unroll
  for (int k = 0; k < 4; ++k) v[k] = perm(asu(e[k]) + 0x80u, tA, 0x07060500u + k);
  return v;
}
DI f32x4 untag4(v4u v) { return f32x4{asf(v.x & ~0xffu), asf(v.y & ~0xffu), asf(v.z & ~0xffu), asf(v.w & ~0xffu)}; }
template <int N> DI void poll16(rsrc_t rs, u32 o, u32 stride, v4u (&v)[N], u32 tA) {
  for (u32 spins = 0;; ++spins) {
    asm volatile("" ::: "memory");
    bool ok = true;
#pragma unroll
    for (int i = 0; i < N; ++i) v[i] = ld_sc1(rs, o + i * stride);
#pragma unroll
    for (int i = 0; i < N; ++i) ok = ok && (perm(v[i].y, v[i].x, 0x0c0c0400u) | perm(v[i].w, v[i].z, 0x04000c0cu)) == tA;
    if (__all(ok) || spins > SPIN_CAP) break;
    __builtin_amdgcn_s_sleep(1);
  }
}
DI void poll_flags(rsrc_t rs, u32 o, u32 tA, u32 tB) {
  for (u32 spins = 0;; ++spins) {
    asm volatile("" ::: "memory");
    const v2u v = ld_sc1_8(rs, o);
    if (__all(v.x == tA && v.y == tB) || spins > SPIN_CAP) break;
    __builtin_amdgcn_s_sleep(1);
  }
  asm volatile("" ::: "memory");
}
DI u32 lds_inc(LAS char* L, u32 o) {
  asm volatile("s_waitcnt lgkmcnt(0)" ::: "memory");
  u32 old = 0;
  if ((threadIdx.x & 63) == 0) old = __hip_atomic_fetch_add((LAS u32*)(L + o), 1u, __ATOMIC_RELAXED, __HIP_MEMORY_SCOPE_WORKGROUP);
  return __builtin_amdgcn_readfirstlane(old);
}
DI void lds_wait(LAS char* L, u32 o, u32 want) {
#pragma unroll 1
  for (u32 spins = 0; spins < (1u << 24); ++spins) {
    if (__builtin_amdgcn_readfirstlane(__hip_atomic_load((LAS u32*)(L + o), __ATOMIC_RELAXED, __HIP_MEMORY_SCOPE_WORKGROUP)) >= want) break;
    __builtin_amdgcn_s_sleep(1);
  }
  asm volatile("" ::: "memory");
}
DI u32 sw2(u32 row) { return (row & 2) | ((row >> 1) & 4); }
DI u32 fsw(u32 row) { return ((row & 3) << 2) | ((row >> 2) & 3); }

DI f32x4 gemv288(LAS char* L, u32 base, u32 xo) {
  f32x4 d = {0.f, 0.f, 0.f, 0.f};
#pragma unroll
  for (int ks = 0; ks < 9; ++ks) d = mfma16(ltr2(L, base + ks * 4096, base + ks * 4096 + 512), lld128(L, xo + ks * 64), d);
  return d;
}
DI v2u fetch_q(rsrc_t rs, u32 o, u32 stride, u32 tA) {
  v4u v[2];
  poll16<2>(rs, o, stride, v, tA);
  const f32x4 q = untag4(v[0]) + untag4(v[1]);
  return v2u{pk2(q[0], q[1]), pk2(q[2], q[3])};
}
template <int N> DI void reduce_oct(rsrc_t rs, u32 o, u32 tA, u16* out_, int lane) {
  GAS u16* out = (GAS u16*)out_ + ((lane >> 5) + ((lane >> 3) & 2)) * 1152 + (lane & 7);
  asm volatile("" : "+v"(out), "+v"(o));
  v4u v[N];
  poll16<N>(rs, o, 1024, v, tA);
  f32x4 a = untag4(v[0]);
#pragma unroll
  for (int i = 1; i < N; ++i) a += untag4(v[i]);
  const float c = sum_hi3(a);
  if (!(lane & 8)) *out = (u16)pk2(c, c);
}

struct Acc { float m, l; f32x4 o; };
DI float a_qk(const v4u (&K)[8], const v4u (&QF)[8], bool t1) {
  f32x4 d = {0.f, 0.f, 0.f, 0.f};
#pragma unroll
  for (int j = 0; j < 8; ++j) d = mfma16(K[j], QF[j], d);
  const float keep = t1 ? d[3] : d[0], send = t1 ? d[1] : d[2];
  return keep + dppf<XOR1>(send);
}
DI u32 a_soft(float S, Acc& a) {
  const float t = rbf(rbf(S) * SCALE) * LOG2E;
  float cm = fmaxf(t, dppf<XOR1>(t));
  cm = fmaxf(cm, dppf<ROR4>(cm));
  cm = fmaxf(cm, dppf<ROR8>(cm));
  cm = x32max(x16max(cm));
  const float mn = fmaxf(a.m, cm);
  const float al = ex2(a.m - mn), p = ex2(t - mn);
  a.l = a.l * al + p;
  a.m = mn;
  a.o *= al;
  return pk2(p, dppf<XOR2>(p));
}
template <int J> DI void a_pv1(u32 PP, v4u V, const u32 (&sel)[4], Acc& a) {
  const u32 X = dppu<BCAST + 4 * (J >> 1) + (J & 1)>(PP);
  a.o = mfma16(V, v4u{perm(X, X, sel[0]), perm(X, X, sel[1]), perm(X, X, sel[2]), perm(X, X, sel[3])}, a.o);
}
DI void a_pv(u32 PP, const v4u (&V)[8], const u32 (&sel)[4], Acc& a) {
  a_pv1<0>(PP, V[0], sel, a); a_pv1<1>(PP, V[1], sel, a); a_pv1<2>(PP, V[2], sel, a); a_pv1<3>(PP, V[3], sel, a);
  a_pv1<4>(PP, V[4], sel, a); a_pv1<5>(PP, V[5], sel, a); a_pv1<6>(PP, V[6], sel, a); a_pv1<7>(PP, V[7], sel, a);
}
DI void a_begin(LAS char* L, int lane, v4u (&QF)[8], Acc& a) {
  const int n = lane & 15, g = lane >> 4;
  const u32 o = L_QS + ((n >> 1) & 1) * 256 + (2 * g + (n & 1)) * 16;
  const v4u F0 = lld128(L, o), F1 = lld128(L, o + 128), Z = {0u, 0u, 0u, 0u};
#pragma unroll
  for (int u = 0; u < 4; ++u) { const bool on = (n >> 2) == u; QF[2 * u] = on ? F0 : Z; QF[2 * u + 1] = on ? F1 : Z; }
  a.m = -INFINITY; a.l = 0.f; a.o = f32x4{0.f, 0.f, 0.f, 0.f};
}

template <bool FIRST, class F>
DI void ba_pass(LAS char* L, int w, int lane, const v4u (&qf)[16], f32x16 (&acc)[2], float& m, float& l, F&& after_qk) {
  asm volatile("" : "+v"(lane));
  const int kb = w & 3, ch = w >> 2, hh = lane >> 5, r31 = lane & 31;
  f32x16 S;
#pragma unroll
  for (int i = 0; i < 16; ++i) S[i] = 0.f;
  const u32 rowb = (32 * kb + r31) * 1024 + ch * 512, f = fsw(r31);
#pragma unroll
  for (int ks = 0; ks < 16; ++ks) S = mfma32(lld128(L, rowb + (ks >> 3) * 256 + (((2 * (ks & 7) + hh) ^ f) * 16)), qf[ks], S);
  float s[8];
  {
    v4u a, b;
#pragma unroll
    for (int j = 0; j < 4; ++j) { a[j] = asu(ch ? S[j] : S[8 + j]); b[j] = asu(ch ? S[4 + j] : S[12 + j]); }
    if (FIRST) { asm volatile("" : "+v"(a), "+v"(b)); BAR(); }
    lst128(L, L_SX + w * 2048 + lane * 16, a);
    lst128(L, L_SX + w * 2048 + 1024 + lane * 16, b);
    asm volatile("" ::: "memory");
    after_qk();
    BAR();
    a = lld128(L, L_SX + (w ^ 4) * 2048 + lane * 16);
    b = lld128(L, L_SX + (w ^ 4) * 2048 + 1024 + lane * 16);
#pragma unroll
    for (int j = 0; j < 4; ++j) {
      s[j] = ((ch ? S[8 + j] : S[j]) + asf(a[j])) * (SCALE * LOG2E);
      s[4 + j] = ((ch ? S[12 + j] : S[4 + j]) + asf(b[j])) * (SCALE * LOG2E);
    }
  }
  float cm = fmaxf(fmaxf(fmaxf(s[0], s[1]), fmaxf(s[2], s[3])), fmaxf(fmaxf(s[4], s[5]), fmaxf(s[6], s[7])));
  cm = x32max(cm);
  if (lane < 32) *(LAS float*)(L + L_WM + (w * 32 + lane) * 4) = cm;
  BAR();
  float mn = FIRST ? -INFINITY : m;
#pragma unroll
  for (int i = 0; i < 8; ++i) mn = fmaxf(mn, *(LAS float*)(L + L_WM + (i * 32 + r31) * 4));
  float ps = 0.f;
#pragma unroll
  for (int j = 0; j < 8; ++j) { s[j] = ex2(s[j] - mn); ps += s[j]; }
  lst128(L, L_P + (2 * kb + ch) * 1024 + lane * 16, v4u{pk2(s[0], s[1]), pk2(s[2], s[3]), pk2(s[4], s[5]), pk2(s[6], s[7])});
  if (FIRST) {
    l = ps;
#pragma unroll
    for (int i = 0; i < 16; ++i) { acc[0][i] = 0.f; acc[1][i] = 0.f; }
  } else {
    const float al = ex2(m - mn);
    l = l * al + ps;
    acc[0] *= al;
    acc[1] *= al;
  }
  m = mn;
  BAR();
  const int q = (lane & 15) >> 2, pp = lane & 3, blk = (lane >> 4) & 1;
  u32 ta[2][2];
#pragma unroll
  for (int T = 0; T < 2; ++T)
#pragma unroll
    for (int t = 0; t < 2; ++t) {
      const u32 c = 8 * w + 4 * T + 2 * blk + (pp >> 1);
      ta[T][t] = (8 * t + 4 * hh + q) * 1024 + ((c ^ ((q << 2) | (2 * t + hh))) * 16) + 8 * (pp & 1);
    }
#pragma unroll
  for (int ks = 0; ks < 8; ++ks) {
    const v4u P = lld128(L, L_P + ks * 1024 + lane * 16);
#pragma unroll
    for (int T = 0; T < 2; ++T) acc[T] = mfma32(ltr2(L, ta[T][0] + ks * 16384, ta[T][1] + ks * 16384), P, acc[T]);
  }
  asm volatile("" : "+v"(acc[0]), "+v"(acc[1]));
}

__global__ void __launch_bounds__(512, 2) decode_mega(Params args) {
  extern __shared__ __attribute__((aligned(16))) char smem[];
  LAS char* L = (LAS char*)smem;
  v16u ka;
  v8u kb;
  v2u kc;
  asm volatile("s_load_dwordx16 %0, %3, 0x0\n\ts_load_dwordx8 %1, %3, 0x40\n\ts_load_dwordx2 %2, %3, 0x60"
               : "=&s"(ka), "=&s"(kb), "=&s"(kc) : "s"(__builtin_amdgcn_kernarg_segment_ptr()) : "memory");
  SB();
  const int tid = threadIdx.x, lane = tid & 63;
  int w = __builtin_amdgcn_readfirstlane(tid >> 6), wg = blockIdx.x;
  u32 vo, ld, step, lstep;
  const char* src0;
  {
#define PICK(m, a, b) ((b) ^ (((a) ^ (b)) & (m)))
    const u32 g = (u32)wg >> 3, r = (wg + g) & 7, hw = w & 3, r0 = r & 1;
    const u32 mH = -((u32)w >> 2), mA = (r >> 2) - 1, mW = -((r >> 2) & (r >> 1) & 1), mB = ~(mA | mW), mK = mW & mH, mX = mA & mH;
    const u32 koff = PICK(mA, PICK(mH, 40u & ((w & 1) - 1), 56u), PICK(mW, PICK(mH, 64u, 72u), 48u));
    asm volatile("s_load_dwordx2 %0, %1, %2" : "=s"(src0) : "s"(__builtin_amdgcn_kernarg_segment_ptr()), "s"(koff) : "memory");
    const u32 hoff = ((g & ~7u) | (wg & 7)) * 256;
    step = PICK(mB, 16384u, PICK(mK, 16u * 8192, 32u * 8192));
    lstep = PICK(mB, 16384u, 4096u);
    const u32 xo = (w & 2) * 448;
    ld = PICK(mX, L_XV + (w & 1) * 4608 + xo, PICK(mB, w, hw) * 1024 + (L_UV & mW & ~mH));
    const u32 sA = (r >> 1) * (288 * 8192) + g * 256, sB = ((g * 2 + r0) * 256 + w) * 1024, sK = (r0 * 256 + 4 * hw) * 8192 + hoff;
    const u32 so = PICK(mX, xo, PICK(mB, sB, PICK(mK, sK, PICK(mA, sA, hoff) + 8 * 8192 * w + r0 * 128)));
    const u32 sh = PICK(mB | mX, 6u, PICK(mK, 4u, 3u)), row = (u32)lane >> sh;
    const u32 xs = PICK(mB, fsw(w), PICK(mK, 4 * hw, 4 * (w & 1))) & ~mX, xm = PICK(mK, 3u, 2u);
    vo = row * 8192 + ((((u32)lane - (row << sh)) ^ xs ^ (row & xm)) * 16) + so;
    u32 keep;
#define PIECE "global_load_lds_dwordx4 %1, %3 nt\n\ts_add_u32 m0, m0, %5\n\tv_add_u32 %1, %4, %1\n\t"
    asm volatile("s_mov_b32 %0, m0\n\ts_mov_b32 m0, %2\n\ts_cmp_lg_u32 %6, 0\n\ts_waitcnt lgkmcnt(0)\n\ts_cbranch_scc1 1f\n\t"
                 PIECE PIECE PIECE PIECE PIECE PIECE PIECE PIECE "s_branch 2f\n"
                 "1:\n\ts_setprio 3\n\tglobal_load_lds_dwordx4 %1, %3\n\tglobal_load_lds_dwordx4 %1, %3 offset:1792\n\tglobal_load_lds_dwordx4 %1, %3 offset:2688\n"
                 "2:\n\ts_mov_b32 m0, %0"
                 : "=&s"(keep), "+v"(vo) : "s"(ld), "s"(src0), "s"(step), "s"(lstep), "s"(mX) : "memory", "scc");
    ld += 8 * lstep;
  }
  asm volatile("s_waitcnt lgkmcnt(0)" : "+s"(ka), "+s"(kb), "+s"(kc), "+s"(src0), "+s"(w), "+s"(wg)::"memory");
  Params p;
#define PTR(v, i) ((char*)(((u64)v[(i) + 1] << 32) | v[(i)]))
  p.AK = PTR(ka, 2); p.AV = PTR(ka, 4); p.Awq = PTR(ka, 6); p.Awo = PTR(ka, 8); p.BC = PTR(ka, 12);
  p.Bwo = PTR(kb, 4); p.out = (u16*)PTR(kb, 6); p.ws = PTR(kc, 0);
  const int hw = w & 3, g = wg >> 3, s = wg & 7, r = (s + g) & 7, kvh = g >> 1;
  const bool isA = r < 4, isBA = r == 4 || r == 5, hlp = w >= 4;
  u64 epoch;
  asm volatile("s_load_dwordx2 %0, %1, 0x0" : "=s"(epoch) : "s"(p.ws) : "memory");
  if (hlp) __builtin_amdgcn_s_setprio(3);
  if (tid < 8 && !isBA) *(LAS u32*)(L + L_SYNC + tid * 4) = 0u;
  const int n16 = lane & 15, g4 = lane >> 4;
  const int ai = (g & 1) * 4 + r;
  const int bi = g * 2 + (r - 4);
  const int h = (g >> 3) * 8 + s, half = r - 6, bw = h * 2 + half;
  {
    int n = isA ? (hlp ? 0 : 1) : isBA ? 0 : 8;
#pragma unroll 1
    for (int bt = 0; bt < 2; ++bt) {
#pragma unroll 1
      for (; n > 0; --n) { VMWAIT(8); dma16(src0, vo, ld); vo += step; ld += lstep; }
      src0 = isA ? p.Awq : src0;
      ld = isA ? L_QAW + w * 1024 : (8 + w) * 1024;
      n = isA ? (hlp ? 0 : 9) : isBA ? 8 : 0;
      vo = (vo ^ (isA ? 0 : 32)) + (isA ? (u32)(((ai >> 2) - (r >> 1) - 1) * (288 * 8192) + kvh * 512 - g * 256 + (r & 2) * 128) : (u32)(-120 * 1024));
    }
    asm volatile("s_waitcnt lgkmcnt(0)" : "+s"(epoch)::"memory");
  }
  const rsrc_t rs = __builtin_amdgcn_make_buffer_rsrc((void*)p.ws, 0, WS_BYTES, 0x00020000);
  const u32 trrow = 8 * g4 + (n16 >> 2);
  const u32 tr128 = trrow * 128 + ((2 * hw + ((lane & 3) >> 1)) ^ sw2(trrow)) * 16 + 8 * (lane & 1);
  const u32 tA = (((u32)epoch & 0x7f7f7f7fu) + 0x01010101u) ^ ((u32)epoch & 0x80808080u), tB = (u32)(epoch >> 32) + 1u;

  u32 voffK, voffV, sel[4];
#define STREAM_CONSTS() do { u32 l_; asm volatile("v_mbcnt_lo_u32_b32 %0, -1, 0\n\tv_mbcnt_hi_u32_b32 %0, -1, %0" : "=v"(l_)); \
    const u32 n_ = l_ & 15, g_ = l_ >> 4; \
    voffK = (n_ >> 1) * 256 + (2 * g_ + (n_ & 1)) * 16; voffV = g_ * 512 + n_ * 16; \
    const u32 h2 = (n_ >> 1) & 1, by = (2 * h2) | ((2 * h2 + 1) << 8); \
    const u32 on = (n_ & 1) ? ((by << 16) | 0x0c0cu) : (0x0c0c0000u | by); \
    _Pragma("unroll") for (int i = 0; i < 4; ++i) sel[i] = (n_ >> 2) == i ? on : 0x0c0c0c0cu; } while (0)
  const int pair = isA ? r >> 1 : isBA ? r & 1 : (r & 1) ^ 1, prod = isA ? r & 1 : isBA ? 2 : 3;
  const int cs = isA ? 8 : 4;
  const int c0 = (isA ? 4 * prod : isBA ? 2 * C_A : 2 * C_A + C_BA) + w;
  const int c1 = isA ? 2 * C_A : isBA ? 2 * C_A + C_BA : 128;
  const size_t kv0 = ((size_t)((g & 1) * 2 + pair) * 16 + kvh) * (4096 * 256) + 4096;
  const u32 q_off = WS_E2 + (g * 2 + pair) * 2048 + lane * 16;
#define LDK(id, j) ldnt(p.AK + kv0 + (size_t)(id) * 8192, voffK, ((j) >> 1) * 2048 + ((j) & 1) * 128 - 4096)
#define LDV(id, j) ldnt(p.AV + kv0 + (size_t)(id) * 8192, voffV, ((j) >> 1) * 2048 + ((j) & 1) * 256 - 4096)
#define LOAD8(R, LD, id) do { _Pragma("unroll") for (int j = 0; j < 8; ++j) R[j] = LD(id, j); SB(); } while (0)
#define PACED8(R, LD, id) do { _Pragma("unroll") for (int j = 0; j < 8; ++j) { VMWAIT(8); R[j] = LD(id, j); SB(); } } while (0)
  const bool t1 = lane & 1;
  const int ci = (g & 1) * 4 + (r & 3), nu = (ci & 3) == 3 ? 3 : 2;
#define LOAD_WO(DST, SRC, NU, NI) do { \
    const u32 vx = (lane >> 3) * 1152 + (((lane & 7) ^ sw2((lane >> 3) | ((w & 1) << 3))) * 16); \
    u32 lx = (DST) + hw * 1024; \
    _Pragma("unroll 1") for (int u = 0; u < (NU); ++u) \
      _Pragma("unroll") for (int i = 0; i < (NI); ++i) { dma16((SRC) + (u32)(u * 128 + (32 * i + 8 * hw) * 1152), vx, lx); lx += 4096; } } while (0)
#define LOAD_WO_A() do { \
    LOAD_WO(L_UV, p.Awo + (u32)((kvh * 256 + (ci >> 2) * 128) * 1152 + (ci & 3) * 256), nu, 4); \
    VMWAIT(0); \
    lds_inc(L, SY_C1); } while (0)

  v4u K[8], V[8], QF[8];
  Acc acc;

  if (!isBA && !hlp) {
    STREAM_CONSTS();
    BAR();
    if (!isA) BAR();
    v4u K0[8], V0[8];
    PACED8(K0, LDK, c0);
    VMWAIT(8);
    if (isA) BAR();
    PACED8(V0, LDV, c0);
    PACED8(K, LDK, c0 + cs);
    PACED8(V, LDV, c0 + cs);
    BAR();
    a_begin(L, lane, QF, acc);
    const u32 PP = a_soft(a_qk(K0, QF, t1), acc);
    a_pv(PP, V0, sel, acc);
  } else if (isA) {
    const u32 xo = L_XV + (lane & 3) * 1152 + g4 * 16;
    VMWAIT(0);
    BAR();
    f32x4 d = gemv288(L, L_QBW + tr128, xo + (r >> 1) * 576);
    st_sc1(rs, n16 < 4 ? WS_E1 + ((g * 2 + (r >> 1)) * 4 + n16) * 512 + (r & 1) * 256 + hw * 64 + g4 * 16 : OOB, tag4(d, tA));
    BAR();
    d = gemv288(L, L_QAW + tr128, xo + 4608 + (ai >> 2) * 576);
    st_sc1(rs, n16 < 4 ? WS_E2 + ((kvh * 4 + n16) * 2 + (ai >> 2)) * 1024 + (ai & 3) * 256 + hw * 64 + g4 * 16 : OOB, tag4(d, tA));
    if (w == 4) *(LAS v2u*)(L + L_QS + lane * 8) = fetch_q(rs, q_off, 1024, tA);
    BAR();
  } else if (isBA) {
    const char* cb = p.BC + (u32)((bi * 256 + w + 128) * 1024);
    const u32 vc0 = (lane ^ fsw(w)) * 16;
    v4u qs[4], c2[16];
#pragma unroll
    for (int i = 0; i < 16; ++i) c2[i] = ldnt(cb + 8 * i * 1024, vc0 ^ ((i & 1) * 32), 0);
    SB();
    poll_flags(rs, WS_FQ + (8 * w + (lane & 7)) * 128, tA, tB);
#pragma unroll
    for (int i = 0; i < 4; ++i) qs[i] = ld_sc1(rs, WS_QABS + ((bi >> 4) * 32 + 4 * w + i) * 1024 + (lane ^ fsw(4 * w + i)) * 16);
#pragma unroll
    for (int i = 0; i < 4; ++i) lst128(L, L_X + (4 * w + i) * 1024 + lane * 16, qs[i]);
    VMWAIT(0);
    BAR();
    v4u qf[16];
    {
      const u32 rowb = L_X + (lane & 31) * 1024 + (w >> 2) * 512, f = fsw(lane & 31);
#pragma unroll
      for (int ks = 0; ks < 16; ++ks) qf[ks] = lld128(L, rowb + (ks >> 3) * 256 + (((2 * (ks & 7) + (lane >> 5)) ^ f) * 16));
    }
    f32x16 ac[2];
    float m, l;
    SB();
    ba_pass<true>(L, w, lane, qf, ac, m, l, [] {});
    if (tid < 8) *(LAS u32*)(L + L_SYNC + tid * 4) = 0u;
    BAR();
#pragma unroll
    for (int i = 0; i < 16; ++i) lst128(L, (8 * i + w) * 1024 + lane * 16, c2[i]);
    v2u qpk = {0u, 0u};
    if (w == 4) qpk = fetch_q(rs, q_off, 1024, tA);
    BAR();
    v4u K0[8], V0[8];
    if (!hlp) {
      STREAM_CONSTS();
      LOAD8(K0, LDK, c0);
    }
    ba_pass<false>(L, w, lane, qf, ac, m, l, [&] {
      if (!hlp) {
        STREAM_CONSTS();
        LOAD8(V0, LDV, c0);
      }
    });
    l = x32sum(l);
    if (lane < 32) *(LAS float*)(L + L_WL + (w * 32 + lane) * 4) = l;
    BAR();
    {
      const u32 hd = lane & 31, hh = lane >> 5;
#pragma unroll
      for (int T = 0; T < 2; ++T)
#pragma unroll
        for (int rq = 0; rq < 4; ++rq)
          *(LAS v2u*)(L + hd * 1024 + (((8 * w + 4 * T + rq) ^ (hd & 15)) * 16) + 8 * hh) =
              v2u{pk2(ac[T][4 * rq], ac[T][4 * rq + 1]), pk2(ac[T][4 * rq + 2], ac[T][4 * rq + 3])};
    }
    if (w == 4) *(LAS v2u*)(L + L_QS + lane * 8) = qpk;
    BAR();
    if (hlp) {
#pragma unroll
      for (int i = 0; i < 8; ++i) {
        const u32 hd = 8 * hw + i;
        st_sc1(rs, WS_PART + (bi * 32 + hd) * 1024 + lane * 16, lld128(L, hd * 1024 + ((lane ^ (hd & 15)) * 16)));
      }
      if (w == 4 && lane < 32) {
        float ls = 0.f;
#pragma unroll
        for (int i = 0; i < 8; ++i) ls += *(LAS float*)(L + L_WL + (i * 32 + lane) * 4);
        st_sc1_8(rs, WS_ML + (bi * 32 + lane) * 8, v2u{asu(m), asu(ls)});
      }
      VMWAIT(0);
      if (lds_inc(L, SY_A) == 3 && lane == 0) st_sc1_8(rs, WS_FP + bi * 128, v2u{tA, tB});
      lds_wait(L, SY_LAST, 4);
      LOAD_WO_A();
    } else {
      STREAM_CONSTS();
      a_begin(L, lane, QF, acc);
      const u32 PP = a_soft(a_qk(K0, QF, t1), acc);
      a_pv(PP, V0, sel, acc);
      asm volatile("" : "+v"(acc.o)::"memory");
      lds_wait(L, SY_A, 4);
      LOAD8(K, LDK, c0 + cs);
      LOAD8(V, LDV, c0 + cs);
    }
  } else {
    if (w < 6) *(LAS v2u*)(L + L_QB + (2 * hw + (lane >> 5)) * 320 + (lane & 31) * 8) = fetch_q(rs, WS_E1 + h * 4096 + hw * 1024 + lane * 16, 2048, tA);
    VMWAIT(0);
    BAR();
    {
      f32x4 d[4];
#pragma unroll
      for (int T = 0; T < 4; ++T) d[T] = f32x4{0.f, 0.f, 0.f, 0.f};
#pragma unroll
      for (int ks = 0; ks < 4; ++ks) {
        const v4u qb = lld128(L, L_QB + (lane & 3) * 320 + ks * 64 + g4 * 16);
        const u32 a0 = (64 * hw + n16) * 256 + (((4 * ks + g4) ^ n16) * 16);
#pragma unroll
        for (int T = 0; T < 4; ++T) d[T] = mfma16(lld128(L, a0 + T * 4096), qb, d[T]);
      }
      const int T = n16 >> 2;
      const f32x4 e = sel4(T & 2, sel4(T & 1, d[3], d[2]), sel4(T & 1, d[1], d[0]));
      st_sc1_8(rs, WS_QABS + ((lane & 3) * 32 + h) * 1024 + (half * 256 + 64 * hw + 16 * T + 4 * g4) * 2, v2u{pk2(e[0], e[1]), pk2(e[2], e[3])});
    }
    VMWAIT(0);
    BAR();
    if (tid == 256) st_sc1_8(rs, WS_FQ + bw * 128, v2u{tA, tB});
    if (w == 4) *(LAS v2u*)(L + L_QS + lane * 8) = fetch_q(rs, q_off, 1024, tA);
    BAR();
    v4u uf[16];
#pragma unroll
    for (int ks = 0; ks < 16; ++ks) uf[ks] = ltr2(L, L_UV + tr128 + ks * 4096, L_UV + tr128 + ks * 4096 + 512);
    poll_flags(rs, WS_FP + (16 * hw + n16) * 128, tA, tB);
    {
      const v2u ml = ld_sc1_8(rs, WS_ML + ((hw * 16 + n16) * 32 + h) * 8);
      v4u pt[16];
#pragma unroll
      for (int i = 0; i < 16; ++i) pt[i] = ld_sc1(rs, WS_PART + ((hw * 16 + i) * 32 + h) * 1024 + lane * 16);
      float M = fmaxf(asf(ml.x), dppf<XOR1>(asf(ml.x)));
      M = fmaxf(M, dppf<XOR2>(M)); M = fmaxf(M, dppf<ROR4>(M)); M = fmaxf(M, dppf<ROR8>(M));
      const float wt = ex2(asf(ml.x) - M);
      float Ls = wt * asf(ml.y);
      Ls += dppf<XOR1>(Ls); Ls += dppf<XOR2>(Ls); Ls += dppf<XOR7>(Ls); Ls += dppf<XOR15>(Ls);
      const float wn = wt / Ls;
      float c[8];
#pragma unroll
      for (int k = 0; k < 8; ++k) c[k] = 0.f;
#pragma unroll
      for (int i = 0; i < 16; ++i) {
        const float wi = asf(__builtin_amdgcn_readlane(asu(wn), i));
#pragma unroll
        for (int k = 0; k < 4; ++k) { c[2 * k] += wi * asf(pt[i][k] << 16); c[2 * k + 1] += wi * asf(pt[i][k] & 0xffff0000u); }
      }
      lst128(L, L_CL + hw * 1088 + lane * 16, v4u{pk2(c[0], c[1]), pk2(c[2], c[3]), pk2(c[4], c[5]), pk2(c[6], c[7])});
    }
    LOAD_WO(L_WO, p.Bwo + (u32)(bw * 64 * 1152), 9, 2);
    lds_inc(L, SY_A);
    lds_wait(L, SY_A, 4);
    {
      f32x4 d = {0.f, 0.f, 0.f, 0.f};
#pragma unroll
      for (int ks = 0; ks < 16; ++ks) d = mfma16(lld128(L, L_CL + (lane & 3) * 1088 + ks * 64 + g4 * 16), uf[ks], d);
      const float dv = (g4 & 2) ? ((g4 & 1) ? d[3] : d[2]) : ((g4 & 1) ? d[1] : d[0]);
      *(LAS u16*)(L + L_OA + g4 * 320 + (16 * hw + n16) * 2) = (u16)pk2(dv, dv);
    }
  }

  if (!hlp) {
    float S = a_qk(K, QF, t1);
    for (int id = c0 + 2 * cs; id < c1; id += cs) {
      asm volatile("" : "+v"(S)::"memory");
      LOAD8(K, LDK, id);
      const u32 PP = a_soft(S, acc);
      a_pv(PP, V, sel, acc);
      asm volatile("" : "+v"(acc.o)::"memory");
      LOAD8(V, LDV, id);
      if (id + cs >= c1 && lane == 0) __hip_atomic_fetch_add((LAS u32*)(L + SY_LAST), 1u, __ATOMIC_RELAXED, __HIP_MEMORY_SCOPE_WORKGROUP);
      S = a_qk(K, QF, t1);
    }
    const u32 PP = a_soft(S, acc);
    float ls = acc.l + dppf<XOR1>(acc.l);
    ls += dppf<ROR4>(ls);
    ls += dppf<ROR8>(ls);
    ls = x32sum(x16sum(ls));
    u32 fa = L_FL + lane * 16, ma = L_FM + (lane & 2) * 4, sa = SY_B, one = 1;
    u32 eo = WS_E3 + (((g * 2 + pair) * 4 + prod) * 2 + ((lane >> 1) & 1)) * 512 + g4 * 128 + (((n16 >> 2) << 1) | (n16 & 1)) * 16;
    if ((lane & 61) == 0) *(LAS f32x2*)(L + ma + w * 16) = f32x2{acc.m, ls};
    asm volatile("" : "+v"(fa), "+v"(ma), "+v"(sa), "+v"(one), "+v"(eo)::"memory");
    a_pv(PP, V, sel, acc);
    *(LAS f32x4*)(L + fa + w * 1024) = acc.o;
    u32 old;
    u64 ex;
    asm volatile("s_mov_b64 %1, exec\n\ts_mov_b64 exec, 1\n\tds_add_rtn_u32 %0, %2, %3\n\ts_mov_b64 exec, %1" : "=&v"(old), "=&s"(ex) : "v"(sa), "v"(one) : "memory");
    f32x2 ml[4];
    f32x4 aw[4];
#pragma unroll
    for (int i = 0; i < 4; ++i) ml[i] = *(LAS f32x2*)(L + ma + i * 16);
#pragma unroll
    for (int i = 0; i < 4; ++i) aw[i] = *(LAS f32x4*)(L + fa + i * 1024);
    const float M = fmaxf(fmaxf(ml[0].x, ml[1].x), fmaxf(ml[2].x, ml[3].x));
    float wt[4], Ls = 0.f;
#pragma unroll
    for (int i = 0; i < 4; ++i) { wt[i] = ex2(ml[i].x - M); Ls += wt[i] * ml[i].y; }
    asm volatile("" : "+v"(old) : "v"(Ls));
    const bool last = __builtin_amdgcn_readfirstlane(old) == 3;
    const f32x4 o = (wt[0] * aw[0] + wt[1] * aw[1] + wt[2] * aw[2] + wt[3] * aw[3]) * __builtin_amdgcn_rcpf(Ls);
    const u32 lse = asu(M + __builtin_amdgcn_logf(Ls));
    const u32 p01 = pk2(o[0], o[1]), p23 = pk2(o[2], o[3]);
    st_sc1(rs, last ? eo : OOB, v4u{perm(p01, tA, 0x07060500u), perm(p23, tA, 0x07060501u), perm(lse, tA, 0x07060502u),
                                    perm(p01, tA, 0x0c0c0403u) | perm(lse, p23, 0x04000c0cu)});
    if (!isA) {
      if (!isBA) {
        lds_wait(L, SY_A, 4);
        LOAD_WO_A();
      }
      const int i16 = lane >> 2;
      u32 go = WS_E3 + (((kvh * 4 + hw) * 4 + (lane & 3)) * 2 + (ci >> 2)) * 512 + i16 * 16;
      u32 oa = L_OB + hw * 320 + (4 * (i16 & 8) + (i16 & 7) + 8 * (lane & 3)) * 2, ra = L_OB + (lane & 3) * 320 + g4 * 16;
      u32 e4 = g4 < nu ? WS_E4 + ((16 * (ci & 3) + 8 * g4 + 2 * hw + (n16 >> 3)) * 32 + kvh * 2 + (ci >> 2)) * 128 + (n16 & 7) * 16 : OOB;
      u32 s2 = SY_C2;
      asm volatile("" : "+v"(go), "+v"(oa), "+v"(ra), "+v"(e4), "+v"(s2));
      lds_wait(L, SY_C1, 4);
      v4u wf[12];
#pragma unroll
      for (int i = 0; i < 12; ++i) wf[i] = ltr2(L, L_UV + tr128 + i * 4096, L_UV + tr128 + i * 4096 + 512);
      v4u v[2];
      poll16<2>(rs, go, 256, v, tA);
      const float m = asf(perm(v[0].z, v[0].w, 0x07060503u));
      float Mx = fmaxf(m, dppf<XOR1>(m));
      Mx = fmaxf(Mx, dppf<XOR2>(Mx));
      float wq = ex2(m - Mx), Lq = wq + dppf<XOR1>(wq);
      Lq += dppf<XOR2>(Lq);
      wq *= __builtin_amdgcn_rcpf(Lq);
      float y[4];
#pragma unroll
      for (int a = 0; a < 4; ++a) {
        const u32 x = a < 2 ? v[a].x : v[a - 2].y;
        const float lo = wq * asf(perm(x, v[a & 1].w, a < 2 ? 0x05010c0cu : 0x05020c0cu)), hi = wq * asf(x & 0xffff0000u);
        y[a] = (t1 ? hi : lo) + dppf<XOR1>(t1 ? lo : hi);
      }
      const bool t2 = lane & 2;
      const float z0 = (t2 ? y[2] : y[0]) + dppf<XOR2>(t2 ? y[0] : y[2]), z1 = (t2 ? y[3] : y[1]) + dppf<XOR2>(t2 ? y[1] : y[3]);
      const u32 zz = pk2(z0, z1);
      *(LAS u16*)(L + oa) = (u16)zz;
      *(LAS u16*)(L + oa + 128) = (u16)(zz >> 16);
      asm volatile("s_mov_b64 %0, exec\n\ts_mov_b64 exec, 1\n\tds_add_u32 %1, %2\n\ts_mov_b64 exec, %0" : "=&s"(ex) : "v"(s2), "v"(one) : "memory");
      v4u af[4];
      for (u32 spins = 0;; ++spins) {
        const u32 cnt = *(volatile LAS u32*)(L + s2);
        asm volatile("" ::: "memory");
#pragma unroll
        for (int ks = 0; ks < 4; ++ks) af[ks] = lld128(L, ra + ks * 64);
        asm volatile("" : "+v"(af[0]), "+v"(af[1]), "+v"(af[2]), "+v"(af[3])::"memory");
        if (__builtin_amdgcn_readfirstlane(cnt) >= 4 || spins > (1u << 24)) break;
      }
      f32x4 e[3];
#pragma unroll
      for (int u = 0; u < 3; ++u) e[u] = f32x4{0.f, 0.f, 0.f, 0.f};
#pragma unroll
      for (int ks = 0; ks < 4; ++ks)
#pragma unroll
        for (int u = 0; u < 3; ++u) e[u] = mfma16(af[ks], wf[4 * u + ks], e[u]);
      st_sc1(rs, e4, tag4(sel4(g4 & 2, e[2], sel4(g4 & 1, e[1], e[0])), tA));
    }
  } else if (isA) {
    const int oct = r * 32 + g;
    if (w == 4 && oct < 72) {
      lds_wait(L, SY_B, 4);
      reduce_oct<4>(rs, WS_E4 + oct * 4096 + lane * 16, tA, p.out + oct * 8, lane);
      if (oct == 0 && lane == 0) __hip_atomic_store((GAS u64*)(p.ws + WS_EPOCH), ((u64)tB << 32) | tA, __ATOMIC_RELAXED, __HIP_MEMORY_SCOPE_AGENT);
    }
  } else if (!isBA) {
    VMWAIT(0);
    lds_inc(L, SY_WO);
    lds_wait(L, SY_WO, 4);
    const v4u af0 = lld128(L, L_OA + (lane & 3) * 320 + g4 * 16), af1 = lld128(L, L_OA + (lane & 3) * 320 + 64 + g4 * 16);
    f32x4 e[9];
#pragma unroll
    for (int u = 0; u < 9; ++u) {
      const u32 t0 = L_WO + tr128 + u * 8192;
      e[u] = mfma16(af1, ltr2(L, t0 + 4096, t0 + 4608), mfma16(af0, ltr2(L, t0, t0 + 512), f32x4{0.f, 0.f, 0.f, 0.f}));
    }
    const u32 eo = WS_E6 + ((8 * g4 + 2 * hw + (n16 >> 3)) * 64 + bw) * 128 + (n16 & 7) * 16;
#pragma unroll
    for (int k = 0; k < 2; ++k)
      st_sc1(rs, eo + k * 32 * 8192, tag4(sel4(g4 & 2, sel4(g4 & 1, e[4 * k + 3], e[4 * k + 2]), sel4(g4 & 1, e[4 * k + 1], e[4 * k])), tA));
    st_sc1(rs, g4 == 0 ? eo + 64 * 8192 : OOB, tag4(e[8], tA));
    const int oct = hw * 64 + bw;
    if (oct < 72) reduce_oct<8>(rs, WS_E6 + oct * 8192 + lane * 16, tA, p.out + 576 + oct * 8, lane);
  }
}

extern "C" void kernel_launch(void* const* d_in, const int* in_sizes, int n_in, void* d_out, int out_size, void* d_ws, size_t ws_size,
                              hipStream_t stream) {
  Params p;
  p.Ax = (const char*)d_in[0]; p.AK = (const char*)d_in[1]; p.AV = (const char*)d_in[2]; p.Awq = (const char*)d_in[3]; p.Awo = (const char*)d_in[4];
  p.Bx = (const char*)d_in[5]; p.BC = (const char*)d_in[6]; p.Bwq = (const char*)d_in[7]; p.Bwuk = (const char*)d_in[8]; p.Bwuv = (const char*)d_in[9];
  p.Bwo = (const char*)d_in[10];
  p.out = (u16*)d_out;
  p.ws = (char*)d_ws;
  (void)hipFuncSetAttribute((const void*)decode_mega, hipFuncAttributeMaxDynamicSharedMemorySize, LDS_BYTES);
  decode_mega<<<256, 512, LDS_BYTES, stream>>>(p);
}
```

```cpp
#include <hip/hip_runtime.h>
#include <stdint.h>

typedef unsigned int u32;
typedef unsigned long long u64;
typedef unsigned short u16;
typedef u32 v2u __attribute__((ext_vector_type(2)));
typedef u32 v4u __attribute__((ext_vector_type(4)));
typedef u32 v8u __attribute__((ext_vector_type(8)));
typedef u32 v16u __attribute__((ext_vector_type(16)));
typedef float f32x2 __attribute__((ext_vector_type(2)));
typedef float f32x4 __attribute__((ext_vector_type(4)));
typedef float f32x16 __attribute__((ext_vector_type(16)));
typedef __bf16 bf16x8 __attribute__((ext_vector_type(8)));
typedef __bf16 bf16x2 __attribute__((ext_vector_type(2)));
typedef short s16x4 __attribute__((ext_vector_type(4)));
typedef __amdgpu_buffer_rsrc_t rsrc_t;
#define DI __device__ __forceinline__
#define LAS __attribute__((address_space(3)))
#define GAS __attribute__((address_space(1)))

constexpr int R_A = 11, R_BA = 4, R_BW = 6;
static_assert(2 * R_A + R_BA + R_BW == 32 && R_BA + R_BW + 1 == R_A && R_BA >= 3, "layout of the rounds; every streamer runs the loop");

constexpr int WS_EPOCH = 0;
constexpr int WS_ML = 16384;
constexpr int WS_FQ = 32768;
constexpr int WS_FP = 49152;
constexpr int WS_QABS = 65536;
constexpr int WS_E1 = 262144;
constexpr int WS_E2 = 393216;
constexpr int WS_E3 = 524288;
constexpr int WS_E6 = 1 << 20;
constexpr int WS_E4 = WS_E6 + 72 * 8192;
constexpr int WS_PART = 2 << 20;
constexpr int WS_BYTES = 4 << 20;
constexpr u32 OOB = 0x7f000000u;
constexpr int LDS_BYTES = 163840;
constexpr int L_QBW = 0, L_QAW = 36864;
constexpr int L_XV = 110592;
constexpr int L_WO = 0;
constexpr int L_UV = 73728;
constexpr int L_X = 131072;
constexpr int L_SX = L_X, L_P = L_X + 16384, L_WM = L_X + 24576, L_WL = L_X + 25600;
constexpr int L_QS = 139264;
constexpr int L_FL = L_QS + 1024;
constexpr int L_FM = L_FL + 4096;
constexpr int L_OA = L_FM + 128;
constexpr int L_QB = L_OA + 1280;
constexpr int L_OB = L_QB;
constexpr int L_CL = L_QB + 1280;
constexpr int L_SYNC = L_X + 26624;
static_assert(L_CL + 4352 <= L_WM, "LDS map");
enum { SY_A = L_SYNC, SY_B = L_SYNC + 4, SY_LAST = L_SYNC + 8, SY_C1 = L_SYNC + 12, SY_C2 = L_SYNC + 16, SY_WO = L_SYNC + 20 };

constexpr float SCALE = 0.08838834764831845f;
constexpr float LOG2E = 1.4426950408889634f;
constexpr u32 SPIN_CAP = 1u << 20;

struct Params {
  const char *Ax, *AK, *AV, *Awq, *Awo, *Bx, *BC, *Bwq, *Bwuk, *Bwuv, *Bwo;
  u16* out;
  char* ws;
};

DI f32x4 mfma16(v4u a, v4u b, f32x4 c) {
  return __builtin_amdgcn_mfma_f32_16x16x32_bf16(__builtin_bit_cast(bf16x8, a), __builtin_bit_cast(bf16x8, b), c, 0, 0, 0);
}
DI f32x16 mfma32(v4u a, v4u b, f32x16 c) {
  return __builtin_amdgcn_mfma_f32_32x32x16_bf16(__builtin_bit_cast(bf16x8, a), __builtin_bit_cast(bf16x8, b), c, 0, 0, 0);
}
DI u32 pk2(float a, float b) { f32x2 v = {a, b}; return __builtin_bit_cast(u32, __builtin_convertvector(v, bf16x2)); }
DI float rbf(float a) { return (float)(__bf16)a; }
DI float asf(u32 x) { return __builtin_bit_cast(float, x); }
DI u32 asu(float x) { return __builtin_bit_cast(u32, x); }
DI float ex2(float x) { return __builtin_amdgcn_exp2f(x); }
DI u32 perm(u32 a, u32 b, u32 s) { return __builtin_amdgcn_perm(a, b, s); }
template <int CTRL> DI float dppf(float x) { return asf(__builtin_amdgcn_mov_dpp(asu(x), CTRL, 0xf, 0xf, true)); }
template <int CTRL> DI u32 dppu(u32 x) { return __builtin_amdgcn_mov_dpp(x, CTRL, 0xf, 0xf, true); }
constexpr int XOR1 = 0xB1, XOR2 = 0x4E, XOR7 = 0x141, XOR15 = 0x140, ROR8 = 0x128, ROR4 = 0x124, BCAST = 0x150;
DI float x16max(float x) { auto s = __builtin_amdgcn_permlane16_swap(asu(x), asu(x), false, false); return fmaxf(asf(s[0]), asf(s[1])); }
DI float x32max(float x) { auto s = __builtin_amdgcn_permlane32_swap(asu(x), asu(x), false, false); return fmaxf(asf(s[0]), asf(s[1])); }
DI float x16sum(float x) { auto s = __builtin_amdgcn_permlane16_swap(asu(x), asu(x), false, false); return asf(s[0]) + asf(s[1]); }
DI float x32sum(float x) { auto s = __builtin_amdgcn_permlane32_swap(asu(x), asu(x), false, false); return asf(s[0]) + asf(s[1]); }
DI float sum_hi3(f32x4 a) {
  auto s0 = __builtin_amdgcn_permlane32_swap(asu(a[0]), asu(a[1]), false, false);
  auto s1 = __builtin_amdgcn_permlane32_swap(asu(a[2]), asu(a[3]), false, false);
  auto t = __builtin_amdgcn_permlane16_swap(asu(asf(s0[0]) + asf(s0[1])), asu(asf(s1[0]) + asf(s1[1])), false, false);
  const float c = asf(t[0]) + asf(t[1]);
  return c + dppf<ROR8>(c);
}
DI f32x4 sel4(bool c, f32x4 a, f32x4 b) { return f32x4{c ? a[0] : b[0], c ? a[1] : b[1], c ? a[2] : b[2], c ? a[3] : b[3]}; }

#define BAR() asm volatile("s_waitcnt lgkmcnt(0)\n\ts_barrier" ::: "memory")
#define VMWAIT(n) asm volatile("s_waitcnt vmcnt(%0)" ::"n"(n) : "memory")
#define SB() __builtin_amdgcn_sched_barrier(0)

DI v4u lld128(LAS char* L, u32 o) { return *(LAS v4u*)(L + o); }
DI void lst128(LAS char* L, u32 o, v4u v) { *(LAS v4u*)(L + o) = v; }
DI v2u ltr(LAS char* L, u32 o) { return __builtin_bit_cast(v2u, __builtin_amdgcn_ds_read_tr16_b64_v4i16((LAS s16x4*)(L + o))); }
DI v4u ltr2(LAS char* L, u32 o0, u32 o1) { v2u a = ltr(L, o0), b = ltr(L, o1); return v4u{a.x, a.y, b.x, b.y}; }
DI void dma16(const char* sbase, u32 voff, u32 lds) {
  u32 keep;
  asm volatile("s_mov_b32 %0, m0\n\ts_mov_b32 m0, %3\n\ts_nop 2\n\tglobal_load_lds_dwordx4 %1, %2 nt\n\ts_mov_b32 m0, %0"
               : "=&s"(keep) : "v"(voff), "s"(sbase), "s"(lds) : "memory");
}
DI v4u ldnt(const char* base, u32 voff, int imm) { return __builtin_nontemporal_load((const GAS v4u*)((const GAS char*)base + voff + imm)); }
DI v4u ld_sc1(rsrc_t rs, u32 o) { return __builtin_amdgcn_raw_buffer_load_b128(rs, o, 0, 16); }
DI v2u ld_sc1_8(rsrc_t rs, u32 o) { return __builtin_amdgcn_raw_buffer_load_b64(rs, o, 0, 16); }
DI void st_sc1(rsrc_t rs, u32 o, v4u v) { __builtin_amdgcn_raw_buffer_store_b128(v, rs, o, 0, 16); }
DI void st_sc1_8(rsrc_t rs, u32 o, v2u v) { __builtin_amdgcn_raw_buffer_store_b64(v, rs, o, 0, 16); }
DI v4u tag4(f32x4 e, u32 tA) {
  v4u v;
#pragma unroll
  for (int k = 0; k < 4; ++k) v[k] = perm(asu(e[k]) + 0x80u, tA, 0x07060500u + k);
  return v;
}
DI f32x4 untag4(v4u v) { return f32x4{asf(v.x & ~0xffu), asf(v.y & ~0xffu), asf(v.z & ~0xffu), asf(v.w & ~0xffu)}; }
template <int N> DI void poll16(rsrc_t rs, u32 o, u32 stride, v4u (&v)[N], u32 tA) {
  for (u32 spins = 0;; ++spins) {
    asm volatile("" ::: "memory");
    bool ok = true;
#pragma unroll
    for (int i = 0; i < N; ++i) v[i] = ld_sc1(rs, o + i * stride);
#pragma unroll
    for (int i = 0; i < N; ++i) ok = ok && (perm(v[i].y, v[i].x, 0x0c0c0400u) | perm(v[i].w, v[i].z, 0x04000c0cu)) == tA;
    if (__all(ok) || spins > SPIN_CAP) break;
    __builtin_amdgcn_s_sleep(1);
  }
}
DI void poll_flags(rsrc_t rs, u32 o, u32 tA, u32 tB) {
  for (u32 spins = 0;; ++spins) {
    asm volatile("" ::: "memory");
    const v2u v = ld_sc1_8(rs, o);
    if (__all(v.x == tA && v.y == tB) || spins > SPIN_CAP) break;
    __builtin_amdgcn_s_sleep(1);
  }
  asm volatile("" ::: "memory");
}
DI u32 lds_inc(LAS char* L, u32 o) {
  asm volatile("s_waitcnt lgkmcnt(0)" ::: "memory");
  u32 old = 0;
  if ((threadIdx.x & 63) == 0) old = __hip_atomic_fetch_add((LAS u32*)(L + o), 1u, __ATOMIC_RELAXED, __HIP_MEMORY_SCOPE_WORKGROUP);
  return __builtin_amdgcn_readfirstlane(old);
}
DI void lds_wait(LAS char* L, u32 o, u32 want) {
#pragma unroll 1
  for (u32 spins = 0; spins < (1u << 24); ++spins) {
    if (__builtin_amdgcn_readfirstlane(__hip_atomic_load((LAS u32*)(L + o), __ATOMIC_RELAXED, __HIP_MEMORY_SCOPE_WORKGROUP)) >= want) break;
    __builtin_amdgcn_s_sleep(1);
  }
  asm volatile("" ::: "memory");
}
DI u32 sw2(u32 row) { return (row & 2) | ((row >> 1) & 4); }
DI u32 fsw(u32 row) { return ((row & 3) << 2) | ((row >> 2) & 3); }

DI f32x4 gemv288(LAS char* L, u32 base, u32 xo) {
  f32x4 d = {0.f, 0.f, 0.f, 0.f};
#pragma unroll
  for (int ks = 0; ks < 9; ++ks) d = mfma16(ltr2(L, base + ks * 4096, base + ks * 4096 + 512), lld128(L, xo + ks * 64), d);
  return d;
}
DI v2u fetch_q(rsrc_t rs, u32 o, u32 stride, u32 tA) {
  v4u v[2];
  poll16<2>(rs, o, stride, v, tA);
  const f32x4 q = untag4(v[0]) + untag4(v[1]);
  return v2u{pk2(q[0], q[1]), pk2(q[2], q[3])};
}
template <int N> DI void reduce_oct(rsrc_t rs, u32 o, u32 tA, u16* out_, int lane) {
  GAS u16* out = (GAS u16*)out_ + ((lane >> 5) + ((lane >> 3) & 2)) * 1152 + (lane & 7);
  asm volatile("" : "+v"(out), "+v"(o));
  v4u v[N];
  poll16<N>(rs, o, 1024, v, tA);
  f32x4 a = untag4(v[0]);
#pragma unroll
  for (int i = 1; i < N; ++i) a += untag4(v[i]);
  const float c = sum_hi3(a);
  if (!(lane & 8)) *out = (u16)pk2(c, c);
}

struct Acc { float m, l; f32x4 o; };
DI float a_qk(const v4u (&K)[8], const v4u (&QF)[8], bool t1) {
  f32x4 d = {0.f, 0.f, 0.f, 0.f};
#pragma unroll
  for (int j = 0; j < 8; ++j) d = mfma16(K[j], QF[j], d);
  const float keep = t1 ? d[3] : d[0], send = t1 ? d[1] : d[2];
  return keep + dppf<XOR1>(send);
}
DI u32 a_soft(float S, Acc& a) {
  const float t = rbf(rbf(S) * SCALE) * LOG2E;
  float cm = fmaxf(t, dppf<XOR1>(t));
  cm = fmaxf(cm, dppf<ROR4>(cm));
  cm = fmaxf(cm, dppf<ROR8>(cm));
  cm = x32max(x16max(cm));
  const float mn = fmaxf(a.m, cm);
  const float al = ex2(a.m - mn), p = ex2(t - mn);
  a.l = a.l * al + p;
  a.m = mn;
  a.o *= al;
  return pk2(p, dppf<XOR2>(p));
}
template <int J> DI void a_pv1(u32 PP, v4u V, const u32 (&sel)[4], Acc& a) {
  const u32 X = dppu<BCAST + 4 * (J >> 1) + (J & 1)>(PP);
  a.o = mfma16(V, v4u{perm(X, X, sel[0]), perm(X, X, sel[1]), perm(X, X, sel[2]), perm(X, X, sel[3])}, a.o);
}
DI void a_pv(u32 PP, const v4u (&V)[8], const u32 (&sel)[4], Acc& a) {
  a_pv1<0>(PP, V[0], sel, a); a_pv1<1>(PP, V[1], sel, a); a_pv1<2>(PP, V[2], sel, a); a_pv1<3>(PP, V[3], sel, a);
  a_pv1<4>(PP, V[4], sel, a); a_pv1<5>(PP, V[5], sel, a); a_pv1<6>(PP, V[6], sel, a); a_pv1<7>(PP, V[7], sel, a);
}
DI void a_begin(LAS char* L, int lane, v4u (&QF)[8], Acc& a) {
  const int n = lane & 15, g = lane >> 4;
  const u32 o = L_QS + ((n >> 1) & 1) * 256 + (2 * g + (n & 1)) * 16;
  const v4u F0 = lld128(L, o), F1 = lld128(L, o + 128), Z = {0u, 0u, 0u, 0u};
#pragma unroll
  for (int u = 0; u < 4; ++u) { const bool on = (n >> 2) == u; QF[2 * u] = on ? F0 : Z; QF[2 * u + 1] = on ? F1 : Z; }
  a.m = -INFINITY; a.l = 0.f; a.o = f32x4{0.f, 0.f, 0.f, 0.f};
}

template <bool FIRST, class F>
DI void ba_pass(LAS char* L, int w, int lane, const v4u (&qf)[16], f32x16 (&acc)[2], float& m, float& l, F&& after_qk) {
  asm volatile("" : "+v"(lane));
  const int kb = w & 3, ch = w >> 2, hh = lane >> 5, r31 = lane & 31;
  f32x16 S;
#pragma unroll
  for (int i = 0; i < 16; ++i) S[i] = 0.f;
  const u32 rowb = (32 * kb + r31) * 1024 + ch * 512, f = fsw(r31);
#pragma unroll
  for (int ks = 0; ks < 16; ++ks) S = mfma32(lld128(L, rowb + (ks >> 3) * 256 + (((2 * (ks & 7) + hh) ^ f) * 16)), qf[ks], S);
  float s[8];
  {
    v4u a, b;
#pragma unroll
    for (int j = 0; j < 4; ++j) { a[j] = asu(ch ? S[j] : S[8 + j]); b[j] = asu(ch ? S[4 + j] : S[12 + j]); }
    if (FIRST) { asm volatile("" : "+v"(a), "+v"(b)); BAR(); }
    lst128(L, L_SX + w * 2048 + lane * 16, a);
    lst128(L, L_SX + w * 2048 + 1024 + lane * 16, b);
    asm volatile("" ::: "memory");
    after_qk();
    BAR();
    a = lld128(L, L_SX + (w ^ 4) * 2048 + lane * 16);
    b = lld128(L, L_SX + (w ^ 4) * 2048 + 1024 + lane * 16);
#pragma unroll
    for (int j = 0; j < 4; ++j) {
      s[j] = ((ch ? S[8 + j] : S[j]) + asf(a[j])) * (SCALE * LOG2E);
      s[4 + j] = ((ch ? S[12 + j] : S[4 + j]) + asf(b[j])) * (SCALE * LOG2E);
    }
  }
  float cm = fmaxf(fmaxf(fmaxf(s[0], s[1]), fmaxf(s[2], s[3])), fmaxf(fmaxf(s[4], s[5]), fmaxf(s[6], s[7])));
  cm = x32max(cm);
  if (lane < 32) *(LAS float*)(L + L_WM + (w * 32 + lane) * 4) = cm;
  BAR();
  float mn = FIRST ? -INFINITY : m;
#pragma unroll
  for (int i = 0; i < 8; ++i) mn = fmaxf(mn, *(LAS float*)(L + L_WM + (i * 32 + r31) * 4));
  float ps = 0.f;
#pragma unroll
  for (int j = 0; j < 8; ++j) { s[j] = ex2(s[j] - mn); ps += s[j]; }
  lst128(L, L_P + (2 * kb + ch) * 1024 + lane * 16, v4u{pk2(s[0], s[1]), pk2(s[2], s[3]), pk2(s[4], s[5]), pk2(s[6], s[7])});
  if (FIRST) {
    l = ps;
#pragma unroll
    for (int i = 0; i < 16; ++i) { acc[0][i] = 0.f; acc[1][i] = 0.f; }
  } else {
    const float al = ex2(m - mn);
    l = l * al + ps;
    acc[0] *= al;
    acc[1] *= al;
  }
  m = mn;
  BAR();
  const int q = (lane & 15) >> 2, pp = lane & 3, blk = (lane >> 4) & 1;
  u32 ta[2][2];
#pragma unroll
  for (int T = 0; T < 2; ++T)
#pragma unroll
    for (int t = 0; t < 2; ++t) {
      const u32 c = 8 * w + 4 * T + 2 * blk + (pp >> 1);
      ta[T][t] = (8 * t + 4 * hh + q) * 1024 + ((c ^ ((q << 2) | (2 * t + hh))) * 16) + 8 * (pp & 1);
    }
#pragma unroll
  for (int ks = 0; ks < 8; ++ks) {
    const v4u P = lld128(L, L_P + ks * 1024 + lane * 16);
#pragma unroll
    for (int T = 0; T < 2; ++T) acc[T] = mfma32(ltr2(L, ta[T][0] + ks * 16384, ta[T][1] + ks * 16384), P, acc[T]);
  }
  asm volatile("" : "+v"(acc[0]), "+v"(acc[1]));
}

__global__ void __launch_bounds__(512, 2) decode_mega(Params args) {
  extern __shared__ __attribute__((aligned(16))) char smem[];
  LAS char* L = (LAS char*)smem;
  v16u ka;
  v8u kb;
  v2u kc;
  asm volatile("s_load_dwordx16 %0, %3, 0x0\n\ts_load_dwordx8 %1, %3, 0x40\n\ts_load_dwordx2 %2, %3, 0x60"
               : "=&s"(ka), "=&s"(kb), "=&s"(kc) : "s"(__builtin_amdgcn_kernarg_segment_ptr()) : "memory");
  SB();
  const int tid = threadIdx.x, lane = tid & 63;
  int w = __builtin_amdgcn_readfirstlane(tid >> 6), wg = blockIdx.x;
  u32 vo, ld, step, lstep;
  const char* src0;
  {
#define PICK(m, a, b) ((b) ^ (((a) ^ (b)) & (m)))
    const u32 g = (u32)wg >> 3, r = (wg + g) & 7, hw = w & 3, r0 = r & 1;
    const u32 mH = -((u32)w >> 2), mA = (r >> 2) - 1, mW = -((r >> 2) & (r >> 1) & 1), mB = ~(mA | mW), mK = mW & mH, mX = mA & mH;
    const u32 koff = PICK(mA, PICK(mH, 40u & ((w & 1) - 1), 56u), PICK(mW, PICK(mH, 64u, 72u), 48u));
    asm volatile("s_load_dwordx2 %0, %1, %2" : "=s"(src0) : "s"(__builtin_amdgcn_kernarg_segment_ptr()), "s"(koff) : "memory");
    const u32 hoff = ((g & ~7u) | (wg & 7)) * 256;
    step = PICK(mB, 16384u, PICK(mK, 16u * 8192, 32u * 8192));
    lstep = PICK(mB, 16384u, 4096u);
    const u32 xo = (w & 2) * 448;
    ld = PICK(mX, L_XV + (w & 1) * 4608 + xo, PICK(mB, w, hw) * 1024 + (L_UV & mW & ~mH));
    const u32 sA = (r >> 1) * (288 * 8192) + g * 256, sB = (((wg & 7) * 8 + (g >> 3) * 2 + r0) * 256 + w) * 1024, sK = (r0 * 256 + 4 * hw) * 8192 + hoff;
    const u32 so = PICK(mX, xo, PICK(mB, sB, PICK(mK, sK, PICK(mA, sA, hoff) + 8 * 8192 * w + r0 * 128)));
    const u32 sh = PICK(mB | mX, 6u, PICK(mK, 4u, 3u)), row = (u32)lane >> sh;
    const u32 xs = PICK(mB, fsw(w), PICK(mK, 4 * hw, 4 * (w & 1))) & ~mX, xm = PICK(mK, 3u, 2u);
    vo = row * 8192 + ((((u32)lane - (row << sh)) ^ xs ^ (row & xm)) * 16) + so;
    u32 keep;
#define PIECE "global_load_lds_dwordx4 %1, %3 nt\n\ts_add_u32 m0, m0, %5\n\tv_add_u32 %1, %4, %1\n\t"
    asm volatile("s_mov_b32 %0, m0\n\ts_mov_b32 m0, %2\n\ts_cmp_lg_u32 %6, 0\n\ts_waitcnt lgkmcnt(0)\n\ts_cbranch_scc1 1f\n\t"
                 PIECE PIECE PIECE PIECE PIECE PIECE PIECE PIECE "s_branch 2f\n"
                 "1:\n\ts_setprio 3\n\tglobal_load_lds_dwordx4 %1, %3\n\tglobal_load_lds_dwordx4 %1, %3 offset:1792\n\tglobal_load_lds_dwordx4 %1, %3 offset:2688\n"
                 "2:\n\ts_mov_b32 m0, %0"
                 : "=&s"(keep), "+v"(vo) : "s"(ld), "s"(src0), "s"(step), "s"(lstep), "s"(mX) : "memory", "scc");
    ld += 8 * lstep;
  }
  asm volatile("s_waitcnt lgkmcnt(0)" : "+s"(ka), "+s"(kb), "+s"(kc), "+s"(src0), "+s"(w), "+s"(wg)::"memory");
  Params p;
#define PTR(v, i) ((char*)(((u64)v[(i) + 1] << 32) | v[(i)]))
  p.AK = PTR(ka, 2); p.AV = PTR(ka, 4); p.Awq = PTR(ka, 6); p.Awo = PTR(ka, 8); p.BC = PTR(ka, 12);
  p.Bwo = PTR(kb, 4); p.out = (u16*)PTR(kb, 6); p.ws = PTR(kc, 0);
  const int hw = w & 3, g = wg >> 3, s = wg & 7, r = (s + g) & 7, kvh = g >> 1;
  const bool isA = r < 4, isBA = r == 4 || r == 5, hlp = w >= 4;
  u64 epoch;
  asm volatile("s_load_dwordx2 %0, %1, 0x0" : "=s"(epoch) : "s"(p.ws) : "memory");
  if (hlp) __builtin_amdgcn_s_setprio(3);
  if (tid < 8 && !isBA) *(LAS u32*)(L + L_SYNC + tid * 4) = 0u;
  const int n16 = lane & 15, g4 = lane >> 4;
  const int ai = (g & 1) * 4 + r;
  const int bi = s * 8 + (g >> 3) * 2 + (r & 1);
  const int h = (g >> 3) * 8 + s, half = r - 6, bw = h * 2 + half;
  {
    int n = isA ? (hlp ? 0 : 1) : isBA ? 0 : 8;
#pragma unroll 1
    for (int bt = 0; bt < 2; ++bt) {
#pragma unroll 1
      for (; n > 0; --n) { VMWAIT(8); dma16(src0, vo, ld); vo += step; ld += lstep; }
      src0 = isA ? p.Awq : src0;
      ld = isA ? L_QAW + w * 1024 : (8 + w) * 1024;
      n = isA ? (hlp ? 0 : 9) : isBA ? 8 : 0;
      vo = (vo ^ (isA ? 0 : 32)) + (isA ? (u32)(((ai >> 2) - (r >> 1) - 1) * (288 * 8192) + kvh * 512 - g * 256 + (r & 2) * 128) : (u32)(-120 * 1024));
    }
    asm volatile("s_waitcnt lgkmcnt(0)" : "+s"(epoch)::"memory");
  }
  const rsrc_t rs = __builtin_amdgcn_make_buffer_rsrc((void*)p.ws, 0, WS_BYTES, 0x00020000);
  const u32 trrow = 8 * g4 + (n16 >> 2);
  const u32 tr128 = trrow * 128 + ((2 * hw + ((lane & 3) >> 1)) ^ sw2(trrow)) * 16 + 8 * (lane & 1);
  const u32 tA = (((u32)epoch & 0x7f7f7f7fu) + 0x01010101u) ^ ((u32)epoch & 0x80808080u), tB = (u32)(epoch >> 32) + 1u;

  u32 voffK, voffV, sel[4];
#define STREAM_CONSTS() do { u32 l_; asm volatile("v_mbcnt_lo_u32_b32 %0, -1, 0\n\tv_mbcnt_hi_u32_b32 %0, -1, %0" : "=v"(l_)); \
    const u32 n_ = l_ & 15, g_ = l_ >> 4; \
    voffK = (n_ >> 1) * 4096 + (2 * g_ + (n_ & 1)) * 16; voffV = g_ * 8192 + n_ * 16; \
    const u32 h2 = (n_ >> 1) & 1, by = (2 * h2) | ((2 * h2 + 1) << 8); \
    const u32 on = (n_ & 1) ? ((by << 16) | 0x0c0cu) : (0x0c0c0000u | by); \
    _Pragma("unroll") for (int i = 0; i < 4; ++i) sel[i] = (n_ >> 2) == i ? on : 0x0c0c0c0cu; } while (0)
  const int pair = isA ? r >> 1 : isBA ? r & 1 : (r & 1) ^ 1, prod = isA ? r & 1 : isBA ? 2 : 3;
  const int nr = isA ? R_A : isBA ? R_BA : R_BW;
  const int p1 = isA ? 2 + prod : isBA ? 3 * R_BW + 7 : 7;
  auto roff = [&](int t) { return (u32)(t == 0 ? (isA ? prod : p1 - 3) : p1 + 3 * (t - 1)) * 32768u; };
  const size_t kv0 = ((size_t)((g & 1) * 2 + pair) * 16 + kvh) * (4096 * 256) + 4096 + w * 1024;
  const u32 q_off = WS_E2 + (g * 2 + pair) * 2048 + lane * 16;
#define LDK(o, j) ldnt(p.AK + kv0 + (o), voffK, ((j) >> 1) * 256 + ((j) & 1) * 128 - 4096)
#define LDV(o, j) ldnt(p.AV + kv0 + (o), voffV, ((j) >> 1) * 256 + ((j) & 1) * 4096 - 4096)
#define LOAD8(R, LD, id) do { _Pragma("unroll") for (int j = 0; j < 8; ++j) R[j] = LD(id, j); SB(); } while (0)
#define PACED8(R, LD, id) do { _Pragma("unroll") for (int j = 0; j < 8; ++j) { VMWAIT(8); R[j] = LD(id, j); SB(); } } while (0)
  const bool t1 = lane & 1;
  const int nu = r == 7 ? 3 : 2;
#define LOAD_WO(DST, SRC, NU, NI) do { \
    const u32 vx = (lane >> 3) * 1152 + (((lane & 7) ^ sw2((lane >> 3) | ((w & 1) << 3))) * 16); \
    u32 lx = (DST) + hw * 1024; \
    _Pragma("unroll 1") for (int u = 0; u < (NU); ++u) \
      _Pragma("unroll") for (int i = 0; i < (NI); ++i) { dma16((SRC) + (u32)(u * 128 + (32 * i + 8 * hw) * 1152), vx, lx); lx += 4096; } } while (0)
#define LOAD_WO_A() do { \
    LOAD_WO(L_UV, p.Awo + (u32)(h * 128 * 1152 + (r & 3) * 256), nu, 4); \
    VMWAIT(0); \
    lds_inc(L, SY_C1); } while (0)

  const u32 o0 = roff(0), o1 = roff(1);
  v4u K[8], V[8], QF[8];
  Acc acc;

  if (!isBA && !hlp) {
    STREAM_CONSTS();
    BAR();
    if (!isA) BAR();
    v4u K0[8], V0[8];
    PACED8(K0, LDK, o0);
    VMWAIT(8);
    if (isA) BAR();
    PACED8(V0, LDV, o0);
    PACED8(K, LDK, o1);
    PACED8(V, LDV, o1);
    BAR();
    a_begin(L, lane, QF, acc);
    const u32 PP = a_soft(a_qk(K0, QF, t1), acc);
    a_pv(PP, V0, sel, acc);
  } else if (isA) {
    const u32 xo = L_XV + (lane & 3) * 1152 + g4 * 16;
    VMWAIT(0);
    BAR();
    f32x4 d = gemv288(L, L_QBW + tr128, xo + (r >> 1) * 576);
    st_sc1(rs, n16 < 4 ? WS_E1 + ((g * 2 + (r >> 1)) * 4 + n16) * 512 + (r & 1) * 256 + hw * 64 + g4 * 16 : OOB, tag4(d, tA));
    BAR();
    d = gemv288(L, L_QAW + tr128, xo + 4608 + (ai >> 2) * 576);
    st_sc1(rs, n16 < 4 ? WS_E2 + ((kvh * 4 + n16) * 2 + (ai >> 2)) * 1024 + (ai & 3) * 256 + hw * 64 + g4 * 16 : OOB, tag4(d, tA));
    if (w == 4) *(LAS v2u*)(L + L_QS + lane * 8) = fetch_q(rs, q_off, 1024, tA);
    BAR();
  } else if (isBA) {
    const char* cb = p.BC + (u32)((bi * 256 + w + 128) * 1024);
    const u32 vc0 = (lane ^ fsw(w)) * 16;
    v4u qs[4], c2[16];
#pragma unroll
    for (int i = 0; i < 16; ++i) c2[i] = ldnt(cb + 8 * i * 1024, vc0 ^ ((i & 1) * 32), 0);
    SB();
    poll_flags(rs, WS_FQ + (8 * w + (lane & 7)) * 128, tA, tB);
#pragma unroll
    for (int i = 0; i < 4; ++i) qs[i] = ld_sc1(rs, WS_QABS + ((bi >> 4) * 32 + 4 * w + i) * 1024 + (lane ^ fsw(4 * w + i)) * 16);
#pragma unroll
    for (int i = 0; i < 4; ++i) lst128(L, L_X + (4 * w + i) * 1024 + lane * 16, qs[i]);
    VMWAIT(0);
    BAR();
    v4u qf[16];
    {
      const u32 rowb = L_X + (lane & 31) * 1024 + (w >> 2) * 512, f = fsw(lane & 31);
#pragma unroll
      for (int ks = 0; ks < 16; ++ks) qf[ks] = lld128(L, rowb + (ks >> 3) * 256 + (((2 * (ks & 7) + (lane >> 5)) ^ f) * 16));
    }
    f32x16 ac[2];
    float m, l;
    SB();
    ba_pass<true>(L, w, lane, qf, ac, m, l, [] {});
    if (tid < 8) *(LAS u32*)(L + L_SYNC + tid * 4) = 0u;
    BAR();
#pragma unroll
    for (int i = 0; i < 16; ++i) lst128(L, (8 * i + w) * 1024 + lane * 16, c2[i]);
    v2u qpk = {0u, 0u};
    if (w == 4) qpk = fetch_q(rs, q_off, 1024, tA);
    BAR();
    v4u K0[8], V0[8];
    if (!hlp) {
      STREAM_CONSTS();
      LOAD8(K0, LDK, o0);
    }
    ba_pass<false>(L, w, lane, qf, ac, m, l, [&] {
      if (!hlp) {
        STREAM_CONSTS();
        LOAD8(V0, LDV, o0);
      }
    });
    l = x32sum(l);
    if (lane < 32) *(LAS float*)(L + L_WL + (w * 32 + lane) * 4) = l;
    BAR();
    {
      const u32 hd = lane & 31, hh = lane >> 5;
#pragma unroll
      for (int T = 0; T < 2; ++T)
#pragma unroll
        for (int rq = 0; rq < 4; ++rq)
          *(LAS v2u*)(L + hd * 1024 + (((8 * w + 4 * T + rq) ^ (hd & 15)) * 16) + 8 * hh) =
              v2u{pk2(ac[T][4 * rq], ac[T][4 * rq + 1]), pk2(ac[T][4 * rq + 2], ac[T][4 * rq + 3])};
    }
    if (w == 4) *(LAS v2u*)(L + L_QS + lane * 8) = qpk;
    BAR();
    if (hlp) {
#pragma unroll
      for (int i = 0; i < 8; ++i) {
        const u32 hd = 8 * hw + i;
        st_sc1(rs, WS_PART + (bi * 32 + hd) * 1024 + lane * 16, lld128(L, hd * 1024 + ((lane ^ (hd & 15)) * 16)));
      }
      if (w == 4 && lane < 32) {
        float ls = 0.f;
#pragma unroll
        for (int i = 0; i < 8; ++i) ls += *(LAS float*)(L + L_WL + (i * 32 + lane) * 4);
        st_sc1_8(rs, WS_ML + (bi * 32 + lane) * 8, v2u{asu(m), asu(ls)});
      }
      VMWAIT(0);
      if (lds_inc(L, SY_A) == 3 && lane == 0) st_sc1_8(rs, WS_FP + bi * 128, v2u{tA, tB});
      lds_wait(L, SY_LAST, 4);
      LOAD_WO_A();
    } else {
      STREAM_CONSTS();
      a_begin(L, lane, QF, acc);
      const u32 PP = a_soft(a_qk(K0, QF, t1), acc);
      a_pv(PP, V0, sel, acc);
      asm volatile("" : "+v"(acc.o)::"memory");
      lds_wait(L, SY_A, 4);
      LOAD8(K, LDK, o1);
      LOAD8(V, LDV, o1);
    }
  } else {
    if (w < 6) *(LAS v2u*)(L + L_QB + (2 * hw + (lane >> 5)) * 320 + (lane & 31) * 8) = fetch_q(rs, WS_E1 + h * 4096 + hw * 1024 + lane * 16, 2048, tA);
    VMWAIT(0);
    BAR();
    {
      f32x4 d[4];
#pragma unroll
      for (int T = 0; T < 4; ++T) d[T] = f32x4{0.f, 0.f, 0.f, 0.f};
#pragma unroll
      for (int ks = 0; ks < 4; ++ks) {
        const v4u qb = lld128(L, L_QB + (lane & 3) * 320 + ks * 64 + g4 * 16);
        const u32 a0 = (64 * hw + n16) * 256 + (((4 * ks + g4) ^ n16) * 16);
#pragma unroll
        for (int T = 0; T < 4; ++T) d[T] = mfma16(lld128(L, a0 + T * 4096), qb, d[T]);
      }
      const int T = n16 >> 2;
      const f32x4 e = sel4(T & 2, sel4(T & 1, d[3], d[2]), sel4(T & 1, d[1], d[0]));
      st_sc1_8(rs, WS_QABS + ((lane & 3) * 32 + h) * 1024 + (half * 256 + 64 * hw + 16 * T + 4 * g4) * 2, v2u{pk2(e[0], e[1]), pk2(e[2], e[3])});
    }
    VMWAIT(0);
    BAR();
    if (tid == 256) st_sc1_8(rs, WS_FQ + bw * 128, v2u{tA, tB});
    if (w == 4) *(LAS v2u*)(L + L_QS + lane * 8) = fetch_q(rs, q_off, 1024, tA);
    BAR();
    v4u uf[16];
#pragma unroll
    for (int ks = 0; ks < 16; ++ks) uf[ks] = ltr2(L, L_UV + tr128 + ks * 4096, L_UV + tr128 + ks * 4096 + 512);
    poll_flags(rs, WS_FP + (16 * hw + n16) * 128, tA, tB);
    {
      const v2u ml = ld_sc1_8(rs, WS_ML + ((hw * 16 + n16) * 32 + h) * 8);
      v4u pt[16];
#pragma unroll
      for (int i = 0; i < 16; ++i) pt[i] = ld_sc1(rs, WS_PART + ((hw * 16 + i) * 32 + h) * 1024 + lane * 16);
      float M = fmaxf(asf(ml.x), dppf<XOR1>(asf(ml.x)));
      M = fmaxf(M, dppf<XOR2>(M)); M = fmaxf(M, dppf<ROR4>(M)); M = fmaxf(M, dppf<ROR8>(M));
      const float wt = ex2(asf(ml.x) - M);
      float Ls = wt * asf(ml.y);
      Ls += dppf<XOR1>(Ls); Ls += dppf<XOR2>(Ls); Ls += dppf<XOR7>(Ls); Ls += dppf<XOR15>(Ls);
      const float wn = wt / Ls;
      float c[8];
#pragma unroll
      for (int k = 0; k < 8; ++k) c[k] = 0.f;
#pragma unroll
      for (int i = 0; i < 16; ++i) {
        const float wi = asf(__builtin_amdgcn_readlane(asu(wn), i));
#pragma unroll
        for (int k = 0; k < 4; ++k) { c[2 * k] += wi * asf(pt[i][k] << 16); c[2 * k + 1] += wi * asf(pt[i][k] & 0xffff0000u); }
      }
      lst128(L, L_CL + hw * 1088 + lane * 16, v4u{pk2(c[0], c[1]), pk2(c[2], c[3]), pk2(c[4], c[5]), pk2(c[6], c[7])});
    }
    LOAD_WO(L_WO, p.Bwo + (u32)(bw * 64 * 1152), 9, 2);
    lds_inc(L, SY_A);
    lds_wait(L, SY_A, 4);
    {
      f32x4 d = {0.f, 0.f, 0.f, 0.f};
#pragma unroll
      for (int ks = 0; ks < 16; ++ks) d = mfma16(lld128(L, L_CL + (lane & 3) * 1088 + ks * 64 + g4 * 16), uf[ks], d);
      const float dv = (g4 & 2) ? ((g4 & 1) ? d[3] : d[2]) : ((g4 & 1) ? d[1] : d[0]);
      *(LAS u16*)(L + L_OA + g4 * 320 + (16 * hw + n16) * 2) = (u16)pk2(dv, dv);
    }
  }

  if (!hlp) {
    float S = a_qk(K, QF, t1);
    for (int t = 2; t < nr; ++t) {
      const u32 id = roff(t);
      asm volatile("" : "+v"(S)::"memory");
      LOAD8(K, LDK, id);
      const u32 PP = a_soft(S, acc);
      a_pv(PP, V, sel, acc);
      asm volatile("" : "+v"(acc.o)::"memory");
      LOAD8(V, LDV, id);
      if (t + 1 == nr && lane == 0) __hip_atomic_fetch_add((LAS u32*)(L + SY_LAST), 1u, __ATOMIC_RELAXED, __HIP_MEMORY_SCOPE_WORKGROUP);
      S = a_qk(K, QF, t1);
    }
    const u32 PP = a_soft(S, acc);
    float ls = acc.l + dppf<XOR1>(acc.l);
    ls += dppf<ROR4>(ls);
    ls += dppf<ROR8>(ls);
    ls = x32sum(x16sum(ls));
    u32 fa = L_FL + lane * 16, ma = L_FM + (lane & 2) * 4, sa = SY_B, one = 1;
    u32 eo = WS_E3 + (((g * 2 + pair) * 4 + prod) * 2 + ((lane >> 1) & 1)) * 512 + g4 * 128 + (((n16 >> 2) << 1) | (n16 & 1)) * 16;
    if ((lane & 61) == 0) *(LAS f32x2*)(L + ma + w * 16) = f32x2{acc.m, ls};
    asm volatile("" : "+v"(fa), "+v"(ma), "+v"(sa), "+v"(one), "+v"(eo)::"memory");
    a_pv(PP, V, sel, acc);
    *(LAS f32x4*)(L + fa + w * 1024) = acc.o;
    u32 old;
    u64 ex;
    asm volatile("s_mov_b64 %1, exec\n\ts_mov_b64 exec, 1\n\tds_add_rtn_u32 %0, %2, %3\n\ts_mov_b64 exec, %1" : "=&v"(old), "=&s"(ex) : "v"(sa), "v"(one) : "memory");
    f32x2 ml[4];
    f32x4 aw[4];
#pragma unroll
    for (int i = 0; i < 4; ++i) ml[i] = *(LAS f32x2*)(L + ma + i * 16);
#pragma unroll
    for (int i = 0; i < 4; ++i) aw[i] = *(LAS f32x4*)(L + fa + i * 1024);
    const float M = fmaxf(fmaxf(ml[0].x, ml[1].x), fmaxf(ml[2].x, ml[3].x));
    float wt[4], Ls = 0.f;
#pragma unroll
    for (int i = 0; i < 4; ++i) { wt[i] = ex2(ml[i].x - M); Ls += wt[i] * ml[i].y; }
    asm volatile("" : "+v"(old) : "v"(Ls));
    const bool last = __builtin_amdgcn_readfirstlane(old) == 3;
    const f32x4 o = (wt[0] * aw[0] + wt[1] * aw[1] + wt[2] * aw[2] + wt[3] * aw[3]) * __builtin_amdgcn_rcpf(Ls);
    const u32 lse = asu(M + __builtin_amdgcn_logf(Ls));
    const u32 p01 = pk2(o[0], o[1]), p23 = pk2(o[2], o[3]);
    st_sc1(rs, last ? eo : OOB, v4u{perm(p01, tA, 0x07060500u), perm(p23, tA, 0x07060501u), perm(lse, tA, 0x07060502u),
                                    perm(p01, tA, 0x0c0c0403u) | perm(lse, p23, 0x04000c0cu)});
    if (!isA) {
      if (!isBA) {
        lds_wait(L, SY_A, 4);
        LOAD_WO_A();
      }
      const int i16 = lane >> 2;
      u32 go = WS_E3 + ((((h >> 1) * 4 + hw) * 4 + (lane & 3)) * 2 + (h & 1)) * 512 + i16 * 16;
      u32 oa = L_OB + hw * 320 + (4 * (i16 & 8) + (i16 & 7) + 8 * (lane & 3)) * 2, ra = L_OB + (lane & 3) * 320 + g4 * 16;
      u32 e4 = g4 < nu ? WS_E4 + ((16 * (r & 3) + 8 * g4 + 2 * hw + (n16 >> 3)) * 32 + h) * 128 + (n16 & 7) * 16 : OOB;
      u32 s2 = SY_C2;
      asm volatile("" : "+v"(go), "+v"(oa), "+v"(ra), "+v"(e4), "+v"(s2));
      lds_wait(L, SY_C1, 4);
      v4u wf[12];
#pragma unroll
      for (int i = 0; i < 12; ++i) wf[i] = ltr2(L, L_UV + tr128 + i * 4096, L_UV + tr128 + i * 4096 + 512);
      v4u v[2];
      poll16<2>(rs, go, 256, v, tA);
      const float m = asf(perm(v[0].z, v[0].w, 0x07060503u));
      float Mx = fmaxf(m, dppf<XOR1>(m));
      Mx = fmaxf(Mx, dppf<XOR2>(Mx));
      float wq = ex2(m - Mx), Lq = wq + dppf<XOR1>(wq);
      Lq += dppf<XOR2>(Lq);
      wq *= __builtin_amdgcn_rcpf(Lq);
      float y[4];
#pragma unroll
      for (int a = 0; a < 4; ++a) {
        const u32 x = a < 2 ? v[a].x : v[a - 2].y;
        const float lo = wq * asf(perm(x, v[a & 1].w, a < 2 ? 0x05010c0cu : 0x05020c0cu)), hi = wq * asf(x & 0xffff0000u);
        y[a] = (t1 ? hi : lo) + dppf<XOR1>(t1 ? lo : hi);
      }
      const bool t2 = lane & 2;
      const float z0 = (t2 ? y[2] : y[0]) + dppf<XOR2>(t2 ? y[0] : y[2]), z1 = (t2 ? y[3] : y[1]) + dppf<XOR2>(t2 ? y[1] : y[3]);
      const u32 zz = pk2(z0, z1);
      *(LAS u16*)(L + oa) = (u16)zz;
      *(LAS u16*)(L + oa + 128) = (u16)(zz >> 16);
      asm volatile("s_mov_b64 %0, exec\n\ts_mov_b64 exec, 1\n\tds_add_u32 %1, %2\n\ts_mov_b64 exec, %0" : "=&s"(ex) : "v"(s2), "v"(one) : "memory");
      v4u af[4];
      for (u32 spins = 0;; ++spins) {
        const u32 cnt = *(volatile LAS u32*)(L + s2);
        asm volatile("" ::: "memory");
#pragma unroll
        for (int ks = 0; ks < 4; ++ks) af[ks] = lld128(L, ra + ks * 64);
        asm volatile("" : "+v"(af[0]), "+v"(af[1]), "+v"(af[2]), "+v"(af[3])::"memory");
        if (__builtin_amdgcn_readfirstlane(cnt) >= 4 || spins > (1u << 24)) break;
      }
      f32x4 e[3];
#pragma unroll
      for (int u = 0; u < 3; ++u) e[u] = f32x4{0.f, 0.f, 0.f, 0.f};
#pragma unroll
      for (int ks = 0; ks < 4; ++ks)
#pragma unroll
        for (int u = 0; u < 3; ++u) e[u] = mfma16(af[ks], wf[4 * u + ks], e[u]);
      st_sc1(rs, e4, tag4(sel4(g4 & 2, e[2], sel4(g4 & 1, e[1], e[0])), tA));
    }
  } else if (isBA) {
    const int oct = hw * 64 + bi;
    if (oct < 72) {
      lds_wait(L, SY_C2, 4);
      __builtin_amdgcn_s_sleep(10);
      reduce_oct<4>(rs, WS_E4 + oct * 4096 + lane * 16, tA, p.out + oct * 8, lane);
      if (oct == 0 && lane == 0) __hip_atomic_store((GAS u64*)(p.ws + WS_EPOCH), ((u64)tB << 32) | tA, __ATOMIC_RELAXED, __HIP_MEMORY_SCOPE_AGENT);
    }
  } else if (!isA) {
    VMWAIT(0);
    lds_inc(L, SY_WO);
    lds_wait(L, SY_WO, 4);
    const v4u af0 = lld128(L, L_OA + (lane & 3) * 320 + g4 * 16), af1 = lld128(L, L_OA + (lane & 3) * 320 + 64 + g4 * 16);
    f32x4 e[9];
#pragma unroll
    for (int u = 0; u < 9; ++u) {
      const u32 t0 = L_WO + tr128 + u * 8192;
      e[u] = mfma16(af1, ltr2(L, t0 + 4096, t0 + 4608), mfma16(af0, ltr2(L, t0, t0 + 512), f32x4{0.f, 0.f, 0.f, 0.f}));
    }
    const u32 eo = WS_E6 + ((8 * g4 + 2 * hw + (n16 >> 3)) * 64 + bw) * 128 + (n16 & 7) * 16;
#pragma unroll
    for (int k = 0; k < 2; ++k)
      st_sc1(rs, eo + k * 32 * 8192, tag4(sel4(g4 & 2, sel4(g4 & 1, e[4 * k + 3], e[4 * k + 2]), sel4(g4 & 1, e[4 * k + 1], e[4 * k])), tA));
    st_sc1(rs, g4 == 0 ? eo + 64 * 8192 : OOB, tag4(e[8], tA));
    const int oct = hw * 64 + bw;
    if (oct < 72) reduce_oct<8>(rs, WS_E6 + oct * 8192 + lane * 16, tA, p.out + 576 + oct * 8, lane);
  }
}

extern "C" void kernel_launch(void* const* d_in, const int* in_sizes, int n_in, void* d_out, int out_size, void* d_ws, size_t ws_size,
                              hipStream_t stream) {
  Params p;
  p.Ax = (const char*)d_in[0]; p.AK = (const char*)d_in[1]; p.AV = (const char*)d_in[2]; p.Awq = (const char*)d_in[3]; p.Awo = (const char*)d_in[4];
  p.Bx = (const char*)d_in[5]; p.BC = (const char*)d_in[6]; p.Bwq = (const char*)d_in[7]; p.Bwuk = (const char*)d_in[8]; p.Bwuv = (const char*)d_in[9];
  p.Bwo = (const char*)d_in[10];
  p.out = (u16*)d_out;
  p.ws = (char*)d_ws;
  (void)hipFuncSetAttribute((const void*)decode_mega, hipFuncAttributeMaxDynamicSharedMemorySize, LDS_BYTES);
  decode_mega<<<256, 512, LDS_BYTES, stream>>>(p);
}
```

```cpp
#include <hip/hip_runtime.h>
#include <stdint.h>

typedef unsigned int u32;
typedef unsigned long long u64;
typedef unsigned short u16;
typedef u32 v2u __attribute__((ext_vector_type(2)));
typedef u32 v4u __attribute__((ext_vector_type(4)));
typedef u32 v8u __attribute__((ext_vector_type(8)));
typedef u32 v16u __attribute__((ext_vector_type(16)));
typedef float f32x2 __attribute__((ext_vector_type(2)));
typedef float f32x4 __attribute__((ext_vector_type(4)));
typedef float f32x16 __attribute__((ext_vector_type(16)));
typedef __bf16 bf16x8 __attribute__((ext_vector_type(8)));
typedef __bf16 bf16x2 __attribute__((ext_vector_type(2)));
typedef short s16x4 __attribute__((ext_vector_type(4)));
typedef __amdgpu_buffer_rsrc_t rsrc_t;
#define DI __device__ __forceinline__
#define LAS __attribute__((address_space(3)))
#define GAS __attribute__((address_space(1)))

constexpr int R_A = 11, R_BA = 4, R_BW = 6;
static_assert(2 * R_A + R_BA + R_BW == 32 && R_BA + R_BW + 1 == R_A && R_BA >= 3, "layout of the rounds; every streamer runs the loop");

constexpr int WS_EPOCH = 0;
constexpr int WS_FQ = 32768;
constexpr int WS_QABS = 65536;
constexpr int WS_MF = 196608;
constexpr int WS_E1 = 262144;
constexpr int WS_E2 = 393216;
constexpr int WS_E3 = 524288;
constexpr int WS_E6 = 1 << 20;
constexpr int WS_E4 = WS_E6 + 72 * 8192;
constexpr int WS_PART = 2 << 20;
constexpr int WS_BYTES = 4 << 20;
constexpr u32 OOB = 0x7f000000u;
constexpr int LDS_BYTES = 163840;
constexpr int L_QBW = 0, L_QAW = 36864;
constexpr int L_XV = 110592;
constexpr int L_WO = 0;
constexpr int L_UV = 73728;
constexpr int L_X = 131072;
constexpr int L_SX = L_X, L_P = L_X + 16384, L_WM = L_X + 24576, L_WL = L_X + 25600;
constexpr int L_QS = 139264;
constexpr int L_FL = L_QS + 1024;
constexpr int L_FM = L_FL + 4096;
constexpr int L_OA = L_FM + 128;
constexpr int L_QB = L_OA + 1280;
constexpr int L_OB = L_QB;
constexpr int L_CL = L_QB + 1280;
constexpr int L_SYNC = L_X + 26624;
static_assert(L_CL + 4352 <= L_WM, "LDS map");
enum { SY_A = L_SYNC, SY_B = L_SYNC + 4, SY_LAST = L_SYNC + 8, SY_C1 = L_SYNC + 12, SY_C2 = L_SYNC + 16, SY_WO = L_SYNC + 20 };

constexpr float SCALE = 0.08838834764831845f;
constexpr float LOG2E = 1.4426950408889634f;
constexpr u32 SPIN_CAP = 1u << 20;

struct Params {
  const char *Ax, *AK, *AV, *Awq, *Awo, *Bx, *BC, *Bwq, *Bwuk, *Bwuv, *Bwo;
  u16* out;
  char* ws;
};

DI f32x4 mfma16(v4u a, v4u b, f32x4 c) {
  return __builtin_amdgcn_mfma_f32_16x16x32_bf16(__builtin_bit_cast(bf16x8, a), __builtin_bit_cast(bf16x8, b), c, 0, 0, 0);
}
DI f32x16 mfma32(v4u a, v4u b, f32x16 c) {
  return __builtin_amdgcn_mfma_f32_32x32x16_bf16(__builtin_bit_cast(bf16x8, a), __builtin_bit_cast(bf16x8, b), c, 0, 0, 0);
}
DI u32 pk2(float a, float b) { f32x2 v = {a, b}; return __builtin_bit_cast(u32, __builtin_convertvector(v, bf16x2)); }
DI float rbf(float a) { return (float)(__bf16)a; }
DI float asf(u32 x) { return __builtin_bit_cast(float, x); }
DI u32 asu(float x) { return __builtin_bit_cast(u32, x); }
DI float ex2(float x) { return __builtin_amdgcn_exp2f(x); }
DI u32 perm(u32 a, u32 b, u32 s) { return __builtin_amdgcn_perm(a, b, s); }
template <int CTRL> DI float dppf(float x) { return asf(__builtin_amdgcn_mov_dpp(asu(x), CTRL, 0xf, 0xf, true)); }
template <int CTRL> DI u32 dppu(u32 x) { return __builtin_amdgcn_mov_dpp(x, CTRL, 0xf, 0xf, true); }
constexpr int XOR1 = 0xB1, XOR2 = 0x4E, XOR7 = 0x141, XOR15 = 0x140, ROR8 = 0x128, ROR4 = 0x124, BCAST = 0x150;
DI float x16max(float x) { auto s = __builtin_amdgcn_permlane16_swap(asu(x), asu(x), false, false); return fmaxf(asf(s[0]), asf(s[1])); }
DI float x32max(float x) { auto s = __builtin_amdgcn_permlane32_swap(asu(x), asu(x), false, false); return fmaxf(asf(s[0]), asf(s[1])); }
DI float x16sum(float x) { auto s = __builtin_amdgcn_permlane16_swap(asu(x), asu(x), false, false); return asf(s[0]) + asf(s[1]); }
DI float x32sum(float x) { auto s = __builtin_amdgcn_permlane32_swap(asu(x), asu(x), false, false); return asf(s[0]) + asf(s[1]); }
DI float sum_hi3(f32x4 a) {
  auto s0 = __builtin_amdgcn_permlane32_swap(asu(a[0]), asu(a[1]), false, false);
  auto s1 = __builtin_amdgcn_permlane32_swap(asu(a[2]), asu(a[3]), false, false);
  auto t = __builtin_amdgcn_permlane16_swap(asu(asf(s0[0]) + asf(s0[1])), asu(asf(s1[0]) + asf(s1[1])), false, false);
  const float c = asf(t[0]) + asf(t[1]);
  return c + dppf<ROR8>(c);
}
DI f32x4 sel4(bool c, f32x4 a, f32x4 b) { return f32x4{c ? a[0] : b[0], c ? a[1] : b[1], c ? a[2] : b[2], c ? a[3] : b[3]}; }

#define BAR() asm volatile("s_waitcnt lgkmcnt(0)\n\ts_barrier" ::: "memory")
#define VMWAIT(n) asm volatile("s_waitcnt vmcnt(%0)" ::"n"(n) : "memory")
#define SB() __builtin_amdgcn_sched_barrier(0)

DI v4u lld128(LAS char* L, u32 o) { return *(LAS v4u*)(L + o); }
DI void lst128(LAS char* L, u32 o, v4u v) { *(LAS v4u*)(L + o) = v; }
DI v2u ltr(LAS char* L, u32 o) { return __builtin_bit_cast(v2u, __builtin_amdgcn_ds_read_tr16_b64_v4i16((LAS s16x4*)(L + o))); }
DI v4u ltr2(LAS char* L, u32 o0, u32 o1) { v2u a = ltr(L, o0), b = ltr(L, o1); return v4u{a.x, a.y, b.x, b.y}; }
DI void dma16(const char* sbase, u32 voff, u32 lds) {
  u32 keep;
  asm volatile("s_mov_b32 %0, m0\n\ts_mov_b32 m0, %3\n\ts_nop 2\n\tglobal_load_lds_dwordx4 %1, %2 nt\n\ts_mov_b32 m0, %0"
               : "=&s"(keep) : "v"(voff), "s"(sbase), "s"(lds) : "memory");
}
DI v4u ldnt(const char* base, u32 voff, int imm) { return __builtin_nontemporal_load((const GAS v4u*)((const GAS char*)base + voff + imm)); }
DI v4u ld_sc1(rsrc_t rs, u32 o) { return __builtin_amdgcn_raw_buffer_load_b128(rs, o, 0, 16); }
DI v2u ld_sc1_8(rsrc_t rs, u32 o) { return __builtin_amdgcn_raw_buffer_load_b64(rs, o, 0, 16); }
DI void st_sc1(rsrc_t rs, u32 o, v4u v) { __builtin_amdgcn_raw_buffer_store_b128(v, rs, o, 0, 16); }
DI void st_sc1_8(rsrc_t rs, u32 o, v2u v) { __builtin_amdgcn_raw_buffer_store_b64(v, rs, o, 0, 16); }
DI v4u tag4(f32x4 e, u32 tA) {
  v4u v;
#pragma unroll
  for (int k = 0; k < 4; ++k) v[k] = perm(asu(e[k]) + 0x80u, tA, 0x07060500u + k);
  return v;
}
DI f32x4 untag4(v4u v) { return f32x4{asf(v.x & ~0xffu), asf(v.y & ~0xffu), asf(v.z & ~0xffu), asf(v.w & ~0xffu)}; }
template <int N> DI void poll16(rsrc_t rs, u32 o, u32 stride, v4u (&v)[N], u32 tA) {
  for (u32 spins = 0;; ++spins) {
    asm volatile("" ::: "memory");
    bool ok = true;
#pragma unroll
    for (int i = 0; i < N; ++i) v[i] = ld_sc1(rs, o + i * stride);
#pragma unroll
    for (int i = 0; i < N; ++i) ok = ok && (perm(v[i].y, v[i].x, 0x0c0c0400u) | perm(v[i].w, v[i].z, 0x04000c0cu)) == tA;
    if (__all(ok) || spins > SPIN_CAP) break;
    __builtin_amdgcn_s_sleep(1);
  }
}
DI void poll_flags(rsrc_t rs, u32 o, u32 tA, u32 tB) {
  for (u32 spins = 0;; ++spins) {
    asm volatile("" ::: "memory");
    const v2u v = ld_sc1_8(rs, o);
    if (__all(v.x == tA && v.y == tB) || spins > SPIN_CAP) break;
    __builtin_amdgcn_s_sleep(1);
  }
  asm volatile("" ::: "memory");
}
DI u32 lds_inc(LAS char* L, u32 o) {
  asm volatile("s_waitcnt lgkmcnt(0)" ::: "memory");
  u32 old = 0;
  if ((threadIdx.x & 63) == 0) old = __hip_atomic_fetch_add((LAS u32*)(L + o), 1u, __ATOMIC_RELAXED, __HIP_MEMORY_SCOPE_WORKGROUP);
  return __builtin_amdgcn_readfirstlane(old);
}
DI void lds_wait(LAS char* L, u32 o, u32 want) {
#pragma unroll 1
  for (u32 spins = 0; spins < (1u << 24); ++spins) {
    if (__builtin_amdgcn_readfirstlane(__hip_atomic_load((LAS u32*)(L + o), __ATOMIC_RELAXED, __HIP_MEMORY_SCOPE_WORKGROUP)) >= want) break;
    __builtin_amdgcn_s_sleep(1);
  }
  asm volatile("" ::: "memory");
}
DI u32 sw2(u32 row) { return (row & 2) | ((row >> 1) & 4); }
DI u32 fsw(u32 row) { return ((row & 3) << 2) | ((row >> 2) & 3); }

DI f32x4 gemv288(LAS char* L, u32 base, u32 xo) {
  f32x4 d = {0.f, 0.f, 0.f, 0.f};
#pragma unroll
  for (int ks = 0; ks < 9; ++ks) d = mfma16(ltr2(L, base + ks * 4096, base + ks * 4096 + 512), lld128(L, xo + ks * 64), d);
  return d;
}
DI v2u fetch_q(rsrc_t rs, u32 o, u32 stride, u32 tA) {
  v4u v[2];
  poll16<2>(rs, o, stride, v, tA);
  const f32x4 q = untag4(v[0]) + untag4(v[1]);
  return v2u{pk2(q[0], q[1]), pk2(q[2], q[3])};
}
template <int N> DI void reduce_oct(rsrc_t rs, u32 o, u32 tA, u16* out_, int lane) {
  GAS u16* out = (GAS u16*)out_ + ((lane >> 5) + ((lane >> 3) & 2)) * 1152 + (lane & 7);
  asm volatile("" : "+v"(out), "+v"(o));
  v4u v[N];
  poll16<N>(rs, o, 1024, v, tA);
  f32x4 a = untag4(v[0]);
#pragma unroll
  for (int i = 1; i < N; ++i) a += untag4(v[i]);
  const float c = sum_hi3(a);
  if (!(lane & 8)) *out = (u16)pk2(c, c);
}

struct Acc { float m, l; f32x4 o; };
DI float a_qk(const v4u (&K)[8], const v4u (&QF)[8], bool t1) {
  f32x4 d = {0.f, 0.f, 0.f, 0.f};
#pragma unroll
  for (int j = 0; j < 8; ++j) d = mfma16(K[j], QF[j], d);
  const float keep = t1 ? d[3] : d[0], send = t1 ? d[1] : d[2];
  return keep + dppf<XOR1>(send);
}
DI u32 a_soft(float S, Acc& a) {
  const float t = rbf(rbf(S) * SCALE) * LOG2E;
  float cm = fmaxf(t, dppf<XOR1>(t));
  cm = fmaxf(cm, dppf<ROR4>(cm));
  cm = fmaxf(cm, dppf<ROR8>(cm));
  cm = x32max(x16max(cm));
  const float mn = fmaxf(a.m, cm);
  const float al = ex2(a.m - mn), p = ex2(t - mn);
  a.l = a.l * al + p;
  a.m = mn;
  a.o *= al;
  return pk2(p, dppf<XOR2>(p));
}
template <int J> DI void a_pv1(u32 PP, v4u V, const u32 (&sel)[4], Acc& a) {
  const u32 X = dppu<BCAST + 4 * (J >> 1) + (J & 1)>(PP);
  a.o = mfma16(V, v4u{perm(X, X, sel[0]), perm(X, X, sel[1]), perm(X, X, sel[2]), perm(X, X, sel[3])}, a.o);
}
DI void a_pv(u32 PP, const v4u (&V)[8], const u32 (&sel)[4], Acc& a) {
  a_pv1<0>(PP, V[0], sel, a); a_pv1<1>(PP, V[1], sel, a); a_pv1<2>(PP, V[2], sel, a); a_pv1<3>(PP, V[3], sel, a);
  a_pv1<4>(PP, V[4], sel, a); a_pv1<5>(PP, V[5], sel, a); a_pv1<6>(PP, V[6], sel, a); a_pv1<7>(PP, V[7], sel, a);
}
DI void a_begin(LAS char* L, int lane, v4u (&QF)[8], Acc& a) {
  const int n = lane & 15, g = lane >> 4;
  const u32 o = L_QS + ((n >> 1) & 1) * 256 + (2 * g + (n & 1)) * 16;
  const v4u F0 = lld128(L, o), F1 = lld128(L, o + 128), Z = {0u, 0u, 0u, 0u};
#pragma unroll
  for (int u = 0; u < 4; ++u) { const bool on = (n >> 2) == u; QF[2 * u] = on ? F0 : Z; QF[2 * u + 1] = on ? F1 : Z; }
  a.m = -INFINITY; a.l = 0.f; a.o = f32x4{0.f, 0.f, 0.f, 0.f};
}

template <bool FIRST, class F>
DI void ba_pass(LAS char* L, int w, int lane, const v4u (&qf)[16], f32x16 (&acc)[2], float& m, float& l, F&& after_qk) {
  asm volatile("" : "+v"(lane));
  const int kb = w & 3, ch = w >> 2, hh = lane >> 5, r31 = lane & 31;
  f32x16 S;
#pragma unroll
  for (int i = 0; i < 16; ++i) S[i] = 0.f;
  const u32 rowb = (32 * kb + r31) * 1024 + ch * 512, f = fsw(r31);
#pragma unroll
  for (int ks = 0; ks < 16; ++ks) S = mfma32(lld128(L, rowb + (ks >> 3) * 256 + (((2 * (ks & 7) + hh) ^ f) * 16)), qf[ks], S);
  float s[8];
  {
    v4u a, b;
#pragma unroll
    for (int j = 0; j < 4; ++j) { a[j] = asu(ch ? S[j] : S[8 + j]); b[j] = asu(ch ? S[4 + j] : S[12 + j]); }
    if (FIRST) { asm volatile("" : "+v"(a), "+v"(b)); BAR(); }
    lst128(L, L_SX + w * 2048 + lane * 16, a);
    lst128(L, L_SX + w * 2048 + 1024 + lane * 16, b);
    asm volatile("" ::: "memory");
    after_qk();
    BAR();
    a = lld128(L, L_SX + (w ^ 4) * 2048 + lane * 16);
    b = lld128(L, L_SX + (w ^ 4) * 2048 + 1024 + lane * 16);
#pragma unroll
    for (int j = 0; j < 4; ++j) {
      s[j] = ((ch ? S[8 + j] : S[j]) + asf(a[j])) * (SCALE * LOG2E);
      s[4 + j] = ((ch ? S[12 + j] : S[4 + j]) + asf(b[j])) * (SCALE * LOG2E);
    }
  }
  float cm = fmaxf(fmaxf(fmaxf(s[0], s[1]), fmaxf(s[2], s[3])), fmaxf(fmaxf(s[4], s[5]), fmaxf(s[6], s[7])));
  cm = x32max(cm);
  if (lane < 32) *(LAS float*)(L + L_WM + (w * 32 + lane) * 4) = cm;
  BAR();
  float mn = FIRST ? -INFINITY : m;
#pragma unroll
  for (int i = 0; i < 8; ++i) mn = fmaxf(mn, *(LAS float*)(L + L_WM + (i * 32 + r31) * 4));
  float ps = 0.f;
#pragma unroll
  for (int j = 0; j < 8; ++j) { s[j] = ex2(s[j] - mn); ps += s[j]; }
  lst128(L, L_P + (2 * kb + ch) * 1024 + lane * 16, v4u{pk2(s[0], s[1]), pk2(s[2], s[3]), pk2(s[4], s[5]), pk2(s[6], s[7])});
  if (FIRST) {
    l = ps;
#pragma unroll
    for (int i = 0; i < 16; ++i) { acc[0][i] = 0.f; acc[1][i] = 0.f; }
  } else {
    const float al = ex2(m - mn);
    l = l * al + ps;
    acc[0] *= al;
    acc[1] *= al;
  }
  m = mn;
  BAR();
  const int q = (lane & 15) >> 2, pp = lane & 3, blk = (lane >> 4) & 1;
  u32 ta[2][2];
#pragma unroll
  for (int T = 0; T < 2; ++T)
#pragma unroll
    for (int t = 0; t < 2; ++t) {
      const u32 c = 8 * w + 4 * T + 2 * blk + (pp >> 1);
      ta[T][t] = (8 * t + 4 * hh + q) * 1024 + ((c ^ ((q << 2) | (2 * t + hh))) * 16) + 8 * (pp & 1);
    }
#pragma unroll
  for (int ks = 0; ks < 8; ++ks) {
    const v4u P = lld128(L, L_P + ks * 1024 + lane * 16);
#pragma unroll
    for (int T = 0; T < 2; ++T) acc[T] = mfma32(ltr2(L, ta[T][0] + ks * 16384, ta[T][1] + ks * 16384), P, acc[T]);
  }
  asm volatile("" : "+v"(acc[0]), "+v"(acc[1]));
}

__global__ void __launch_bounds__(512, 2) decode_mega(Params args) {
  extern __shared__ __attribute__((aligned(16))) char smem[];
  LAS char* L = (LAS char*)smem;
  v16u ka;
  v8u kb;
  v2u kc;
  asm volatile("s_load_dwordx16 %0, %3, 0x0\n\ts_load_dwordx8 %1, %3, 0x40\n\ts_load_dwordx2 %2, %3, 0x60"
               : "=&s"(ka), "=&s"(kb), "=&s"(kc) : "s"(__builtin_amdgcn_kernarg_segment_ptr()) : "memory");
  SB();
  const int tid = threadIdx.x, lane = tid & 63;
  int w = __builtin_amdgcn_readfirstlane(tid >> 6), wg = blockIdx.x;
  u32 vo, ld, step, lstep;
  const char* src0;
  {
#define PICK(m, a, b) ((b) ^ (((a) ^ (b)) & (m)))
    const u32 g = (u32)wg >> 3, r = (wg + g) & 7, hw = w & 3, r0 = r & 1;
    const u32 mH = -((u32)w >> 2), mA = (r >> 2) - 1, mW = -((r >> 2) & (r >> 1) & 1), mB = ~(mA | mW), mK = mW & mH, mX = mA & mH;
    const u32 koff = PICK(mA, PICK(mH, 40u & ((w & 1) - 1), 56u), PICK(mW, PICK(mH, 64u, 72u), 48u));
    asm volatile("s_load_dwordx2 %0, %1, %2" : "=s"(src0) : "s"(__builtin_amdgcn_kernarg_segment_ptr()), "s"(koff) : "memory");
    const u32 hoff = ((g & ~7u) | (wg & 7)) * 256;
    step = PICK(mB, 16384u, PICK(mK, 16u * 8192, 32u * 8192));
    lstep = PICK(mB, 16384u, 4096u);
    const u32 xo = (w & 2) * 448;
    ld = PICK(mX, L_XV + (w & 1) * 4608 + xo, PICK(mB, w, hw) * 1024 + (L_UV & mW & ~mH));
    const u32 sA = (r >> 1) * (288 * 8192) + g * 256, sB = (((wg & 7) * 8 + (g >> 3) * 2 + r0) * 256 + w) * 1024, sK = (r0 * 256 + 4 * hw) * 8192 + hoff;
    const u32 so = PICK(mX, xo, PICK(mB, sB, PICK(mK, sK, PICK(mA, sA, hoff) + 8 * 8192 * w + r0 * 128)));
    const u32 sh = PICK(mB | mX, 6u, PICK(mK, 4u, 3u)), row = (u32)lane >> sh;
    const u32 xs = PICK(mB, fsw(w), PICK(mK, 4 * hw, 4 * (w & 1))) & ~mX, xm = PICK(mK, 3u, 2u);
    vo = row * 8192 + ((((u32)lane - (row << sh)) ^ xs ^ (row & xm)) * 16) + so;
    u32 keep;
#define PIECE "global_load_lds_dwordx4 %1, %3 nt\n\ts_add_u32 m0, m0, %5\n\tv_add_u32 %1, %4, %1\n\t"
    asm volatile("s_mov_b32 %0, m0\n\ts_mov_b32 m0, %2\n\ts_cmp_lg_u32 %6, 0\n\ts_waitcnt lgkmcnt(0)\n\ts_cbranch_scc1 1f\n\t"
                 PIECE PIECE PIECE PIECE PIECE PIECE PIECE PIECE "s_branch 2f\n"
                 "1:\n\ts_setprio 3\n\tglobal_load_lds_dwordx4 %1, %3\n\tglobal_load_lds_dwordx4 %1, %3 offset:1792\n\tglobal_load_lds_dwordx4 %1, %3 offset:2688\n"
                 "2:\n\ts_mov_b32 m0, %0"
                 : "=&s"(keep), "+v"(vo) : "s"(ld), "s"(src0), "s"(step), "s"(lstep), "s"(mX) : "memory", "scc");
    ld += 8 * lstep;
  }
  asm volatile("s_waitcnt lgkmcnt(0)" : "+s"(ka), "+s"(kb), "+s"(kc), "+s"(src0), "+s"(w), "+s"(wg)::"memory");
  Params p;
#define PTR(v, i) ((char*)(((u64)v[(i) + 1] << 32) | v[(i)]))
  p.AK = PTR(ka, 2); p.AV = PTR(ka, 4); p.Awq = PTR(ka, 6); p.Awo = PTR(ka, 8); p.BC = PTR(ka, 12);
  p.Bwo = PTR(kb, 4); p.out = (u16*)PTR(kb, 6); p.ws = PTR(kc, 0);
  const int hw = w & 3, g = wg >> 3, s = wg & 7, r = (s + g) & 7, kvh = g >> 1;
  const bool isA = r < 4, isBA = r == 4 || r == 5, hlp = w >= 4;
  u64 epoch;
  asm volatile("s_load_dwordx2 %0, %1, 0x0" : "=s"(epoch) : "s"(p.ws) : "memory");
  if (hlp) __builtin_amdgcn_s_setprio(3);
  if (tid < 8 && !isBA) *(LAS u32*)(L + L_SYNC + tid * 4) = 0u;
  const int n16 = lane & 15, g4 = lane >> 4;
  const int bi = s * 8 + (g >> 3) * 2 + (r & 1);
  const int h = (g >> 3) * 8 + s, half = r - 6, bw = h * 2 + half;
  {
    int n = isA ? (hlp ? 0 : 1) : isBA ? 0 : 8;
#pragma unroll 1
    for (int bt = 0; bt < 2; ++bt) {
#pragma unroll 1
      for (; n > 0; --n) { VMWAIT(8); dma16(src0, vo, ld); vo += step; ld += lstep; }
      src0 = isA ? p.Awq : src0;
      ld = isA ? L_QAW + w * 1024 : (8 + w) * 1024;
      n = isA ? (hlp ? 0 : 9) : isBA ? 8 : 0;
      vo = (vo ^ (isA ? 0 : 32)) + (isA ? (u32)(((g & 1) - (r >> 1) - 1) * (288 * 8192) + kvh * 512 - g * 256 + ((r ^ 1) - (r & 1)) * 128) : (u32)(-120 * 1024));
    }
    asm volatile("s_waitcnt lgkmcnt(0)" : "+s"(epoch)::"memory");
  }
  const rsrc_t rs = __builtin_amdgcn_make_buffer_rsrc((void*)p.ws, 0, WS_BYTES, 0x00020000);
  const u32 trrow = 8 * g4 + (n16 >> 2);
  const u32 tr128 = trrow * 128 + ((2 * hw + ((lane & 3) >> 1)) ^ sw2(trrow)) * 16 + 8 * (lane & 1);
  const u32 tA = (((u32)epoch & 0x7f7f7f7fu) + 0x01010101u) ^ ((u32)epoch & 0x80808080u), tB = (u32)(epoch >> 32) + 1u;

  u32 voffK, voffV, sel[4];
#define STREAM_CONSTS() do { u32 l_; asm volatile("v_mbcnt_lo_u32_b32 %0, -1, 0\n\tv_mbcnt_hi_u32_b32 %0, -1, %0" : "=v"(l_)); \
    const u32 n_ = l_ & 15, g_ = l_ >> 4; \
    voffK = (n_ >> 1) * 4096 + (2 * g_ + (n_ & 1)) * 16; voffV = g_ * 8192 + n_ * 16; \
    const u32 h2 = (n_ >> 1) & 1, by = (2 * h2) | ((2 * h2 + 1) << 8); \
    const u32 on = (n_ & 1) ? ((by << 16) | 0x0c0cu) : (0x0c0c0000u | by); \
    _Pragma("unroll") for (int i = 0; i < 4; ++i) sel[i] = (n_ >> 2) == i ? on : 0x0c0c0c0cu; } while (0)
  const int pair = isA ? r >> 1 : isBA ? r & 1 : (r & 1) ^ 1, prod = isA ? r & 1 : isBA ? 2 : 3;
  const int nr = isA ? R_A : isBA ? R_BA : R_BW;
  const int p1 = isA ? 2 + prod : isBA ? 3 * R_BW + 7 : 7;
  auto roff = [&](int t) { return (u32)(t == 0 ? (isA ? prod : p1 - 3) : p1 + 3 * (t - 1)) * 32768u; };
  const size_t kv0 = ((size_t)((g & 1) * 2 + pair) * 16 + kvh) * (4096 * 256) + 4096 + w * 1024;
  const u32 q_off = WS_E2 + (g * 2 + pair) * 2048 + lane * 16;
#define LDK(o, j) ldnt(p.AK + kv0 + (o), voffK, ((j) >> 1) * 256 + ((j) & 1) * 128 - 4096)
#define LDV(o, j) ldnt(p.AV + kv0 + (o), voffV, ((j) >> 1) * 256 + ((j) & 1) * 4096 - 4096)
#define LOAD8(R, LD, id) do { _Pragma("unroll") for (int j = 0; j < 8; ++j) R[j] = LD(id, j); SB(); } while (0)
#define PACED8(R, LD, id) do { _Pragma("unroll") for (int j = 0; j < 8; ++j) { VMWAIT(8); R[j] = LD(id, j); SB(); } } while (0)
  const bool t1 = lane & 1;
  const int nu = r == 7 ? 3 : 2;
#define LOAD_WO(DST, SRC, NU, NI) do { \
    const u32 vx = (lane >> 3) * 1152 + (((lane & 7) ^ sw2((lane >> 3) | ((w & 1) << 3))) * 16); \
    u32 lx = (DST) + hw * 1024; \
    _Pragma("unroll 1") for (int u = 0; u < (NU); ++u) \
      _Pragma("unroll") for (int i = 0; i < (NI); ++i) { dma16((SRC) + (u32)(u * 128 + (32 * i + 8 * hw) * 1152), vx, lx); lx += 4096; } } while (0)
#define LOAD_WO_A() do { \
    LOAD_WO(L_UV, p.Awo + (u32)(h * 128 * 1152 + (r & 3) * 256), nu, 4); \
    VMWAIT(0); \
    lds_inc(L, SY_C1); } while (0)

  const u32 o0 = roff(0), o1 = roff(1);
  v4u K[8], V[8], QF[8];
  Acc acc;

  if (!isBA && !hlp) {
    STREAM_CONSTS();
    BAR();
    if (!isA) BAR();
    v4u K0[8], V0[8];
    PACED8(K0, LDK, o0);
    VMWAIT(8);
    if (isA) BAR();
    PACED8(V0, LDV, o0);
    PACED8(K, LDK, o1);
    PACED8(V, LDV, o1);
    BAR();
    a_begin(L, lane, QF, acc);
    const u32 PP = a_soft(a_qk(K0, QF, t1), acc);
    a_pv(PP, V0, sel, acc);
  } else if (isA) {
    const u32 xo = L_XV + (lane & 3) * 1152 + g4 * 16;
    VMWAIT(0);
    BAR();
    f32x4 d = gemv288(L, L_QBW + tr128, xo + (r >> 1) * 576);
    st_sc1(rs, n16 < 4 ? WS_E1 + ((g * 2 + (r >> 1)) * 4 + n16) * 512 + (r & 1) * 256 + hw * 64 + g4 * 16 : OOB, tag4(d, tA));
    BAR();
    d = gemv288(L, L_QAW + tr128, xo + 4608 + (g & 1) * 576);
    st_sc1(rs, n16 < 4 ? WS_E2 + ((kvh * 4 + n16) * 2 + (g & 1)) * 1024 + (r ^ 1) * 256 + hw * 64 + g4 * 16 : OOB, tag4(d, tA));
    __builtin_amdgcn_s_sleep(45);
    if (w == 4) *(LAS v2u*)(L + L_QS + lane * 8) = fetch_q(rs, q_off, 1024, tA);
    BAR();
  } else if (isBA) {
    const char* cb = p.BC + (u32)((bi * 256 + w + 128) * 1024);
    const u32 vc0 = (lane ^ fsw(w)) * 16;
    v4u qs[4], c2[16];
#pragma unroll
    for (int i = 0; i < 16; ++i) c2[i] = ldnt(cb + 8 * i * 1024, vc0 ^ ((i & 1) * 32), 0);
    SB();
    VMWAIT(0);
    poll_flags(rs, WS_FQ + (8 * w + (lane & 7)) * 128, tA, tB);
#pragma unroll
    for (int i = 0; i < 4; ++i) qs[i] = ld_sc1(rs, WS_QABS + ((bi >> 4) * 32 + 4 * w + i) * 1024 + (lane ^ fsw(4 * w + i)) * 16);
#pragma unroll
    for (int i = 0; i < 4; ++i) lst128(L, L_X + (4 * w + i) * 1024 + lane * 16, qs[i]);
    BAR();
    v4u qf[16];
    {
      const u32 rowb = L_X + (lane & 31) * 1024 + (w >> 2) * 512, f = fsw(lane & 31);
#pragma unroll
      for (int ks = 0; ks < 16; ++ks) qf[ks] = lld128(L, rowb + (ks >> 3) * 256 + (((2 * (ks & 7) + (lane >> 5)) ^ f) * 16));
    }
    f32x16 ac[2];
    float m, l;
    SB();
    ba_pass<true>(L, w, lane, qf, ac, m, l, [] {});
    if (tid < 8) *(LAS u32*)(L + L_SYNC + tid * 4) = 0u;
    BAR();
#pragma unroll
    for (int i = 0; i < 16; ++i) lst128(L, (8 * i + w) * 1024 + lane * 16, c2[i]);
    v2u qpk = {0u, 0u};
    if (w == 4) qpk = fetch_q(rs, q_off, 1024, tA);
    BAR();
    v4u K0[8], V0[8];
    if (!hlp) {
      STREAM_CONSTS();
      LOAD8(K0, LDK, o0);
    }
    ba_pass<false>(L, w, lane, qf, ac, m, l, [&] {
      if (!hlp) {
        STREAM_CONSTS();
        LOAD8(V0, LDV, o0);
      }
    });
    l = x32sum(l);
    if (lane < 32) *(LAS float*)(L + L_WL + (w * 32 + lane) * 4) = l;
    BAR();
    {
      const u32 hd = lane & 31, hh = lane >> 5;
#pragma unroll
      for (int T = 0; T < 2; ++T)
#pragma unroll
        for (int rq = 0; rq < 4; ++rq)
          *(LAS v2u*)(L + hd * 1024 + (((8 * w + 4 * T + rq) ^ (hd & 15)) * 16) + 8 * hh) =
              v2u{pk2(ac[T][4 * rq], ac[T][4 * rq + 1]), pk2(ac[T][4 * rq + 2], ac[T][4 * rq + 3])};
    }
    if (w == 4) *(LAS v2u*)(L + L_QS + lane * 8) = qpk;
    BAR();
    if (hlp) {
#pragma unroll
      for (int i = 0; i < 8; ++i) {
        const u32 hd = 8 * hw + i;
        st_sc1(rs, WS_PART + (bi * 32 + hd) * 1024 + lane * 16, lld128(L, hd * 1024 + ((lane ^ (hd & 15)) * 16)));
      }
      float ls = 0.f;
#pragma unroll
      for (int i = 0; i < 8; ++i) ls += *(LAS float*)(L + L_WL + (i * 32 + (lane & 31)) * 4);
      VMWAIT(0);
      if (lds_inc(L, SY_A) == 3) st_sc1(rs, lane < 32 ? WS_MF + (bi * 8 + (lane & 7)) * 128 + (lane >> 3) * 16 : OOB, v4u{asu(m), tA, asu(ls), tB});
      lds_wait(L, SY_LAST, 4);
      LOAD_WO_A();
    } else {
      STREAM_CONSTS();
      a_begin(L, lane, QF, acc);
      const u32 PP = a_soft(a_qk(K0, QF, t1), acc);
      a_pv(PP, V0, sel, acc);
      asm volatile("" : "+v"(acc.o)::"memory");
      lds_wait(L, SY_A, 4);
      LOAD8(K, LDK, o1);
      LOAD8(V, LDV, o1);
    }
  } else {
    VMWAIT(0);
    if (w < 6) *(LAS v2u*)(L + L_QB + (2 * hw + (lane >> 5)) * 320 + (lane & 31) * 8) = fetch_q(rs, WS_E1 + h * 4096 + hw * 1024 + lane * 16, 2048, tA);
    BAR();
    {
      f32x4 d[4];
#pragma unroll
      for (int T = 0; T < 4; ++T) d[T] = f32x4{0.f, 0.f, 0.f, 0.f};
#pragma unroll
      for (int ks = 0; ks < 4; ++ks) {
        const v4u qb = lld128(L, L_QB + (lane & 3) * 320 + ks * 64 + g4 * 16);
        const u32 a0 = (64 * hw + n16) * 256 + (((4 * ks + g4) ^ n16) * 16);
#pragma unroll
        for (int T = 0; T < 4; ++T) d[T] = mfma16(lld128(L, a0 + T * 4096), qb, d[T]);
      }
      const int T = n16 >> 2;
      const f32x4 e = sel4(T & 2, sel4(T & 1, d[3], d[2]), sel4(T & 1, d[1], d[0]));
      st_sc1_8(rs, WS_QABS + ((lane & 3) * 32 + h) * 1024 + (half * 256 + 64 * hw + 16 * T + 4 * g4) * 2, v2u{pk2(e[0], e[1]), pk2(e[2], e[3])});
    }
    VMWAIT(0);
    BAR();
    if (tid == 256) st_sc1_8(rs, WS_FQ + bw * 128, v2u{tA, tB});
    if (w == 4) *(LAS v2u*)(L + L_QS + lane * 8) = fetch_q(rs, q_off, 1024, tA);
    BAR();
    v4u uf[16];
#pragma unroll
    for (int ks = 0; ks < 16; ++ks) uf[ks] = ltr2(L, L_UV + tr128 + ks * 4096, L_UV + tr128 + ks * 4096 + 512);
    lds_wait(L, SY_LAST, 4);
    v2u ml;
    for (u32 spins = 0;; ++spins) {
      asm volatile("" ::: "memory");
      const v4u v = ld_sc1(rs, WS_MF + ((hw * 16 + n16) * 8 + s) * 128 + (g >> 3) * 16);
      ml = v2u{v.x, v.z};
      if (__all(v.y == tA && v.w == tB) || spins > SPIN_CAP) break;
      __builtin_amdgcn_s_sleep(1);
    }
    asm volatile("" ::: "memory");
    {
      v4u pt[16];
#pragma unroll
      for (int i = 0; i < 16; ++i) pt[i] = ld_sc1(rs, WS_PART + ((hw * 16 + i) * 32 + h) * 1024 + lane * 16);
      float M = fmaxf(asf(ml.x), dppf<XOR1>(asf(ml.x)));
      M = fmaxf(M, dppf<XOR2>(M)); M = fmaxf(M, dppf<ROR4>(M)); M = fmaxf(M, dppf<ROR8>(M));
      const float wt = ex2(asf(ml.x) - M);
      float Ls = wt * asf(ml.y);
      Ls += dppf<XOR1>(Ls); Ls += dppf<XOR2>(Ls); Ls += dppf<XOR7>(Ls); Ls += dppf<XOR15>(Ls);
      const float wn = wt / Ls;
      float c[8];
#pragma unroll
      for (int k = 0; k < 8; ++k) c[k] = 0.f;
#pragma unroll
      for (int i = 0; i < 16; ++i) {
        const float wi = asf(__builtin_amdgcn_readlane(asu(wn), i));
#pragma unroll
        for (int k = 0; k < 4; ++k) { c[2 * k] += wi * asf(pt[i][k] << 16); c[2 * k + 1] += wi * asf(pt[i][k] & 0xffff0000u); }
      }
      lst128(L, L_CL + hw * 1088 + lane * 16, v4u{pk2(c[0], c[1]), pk2(c[2], c[3]), pk2(c[4], c[5]), pk2(c[6], c[7])});
    }
    LOAD_WO(L_WO, p.Bwo + (u32)(bw * 64 * 1152), 9, 2);
    lds_inc(L, SY_A);
    lds_wait(L, SY_A, 4);
    {
      f32x4 d = {0.f, 0.f, 0.f, 0.f};
#pragma unroll
      for (int ks = 0; ks < 16; ++ks) d = mfma16(lld128(L, L_CL + (lane & 3) * 1088 + ks * 64 + g4 * 16), uf[ks], d);
      const float dv = (g4 & 2) ? ((g4 & 1) ? d[3] : d[2]) : ((g4 & 1) ? d[1] : d[0]);
      *(LAS u16*)(L + L_OA + g4 * 320 + (16 * hw + n16) * 2) = (u16)pk2(dv, dv);
    }
  }

  if (!hlp) {
    float S = a_qk(K, QF, t1);
    for (int t = 2; t < nr; ++t) {
      const u32 id = roff(t);
      asm volatile("" : "+v"(S)::"memory");
      LOAD8(K, LDK, id);
      const u32 PP = a_soft(S, acc);
      a_pv(PP, V, sel, acc);
      asm volatile("" : "+v"(acc.o)::"memory");
      LOAD8(V, LDV, id);
      if (t + 1 == nr && lane == 0) __hip_atomic_fetch_add((LAS u32*)(L + SY_LAST), 1u, __ATOMIC_RELAXED, __HIP_MEMORY_SCOPE_WORKGROUP);
      S = a_qk(K, QF, t1);
    }
    const u32 PP = a_soft(S, acc);
    float ls = acc.l + dppf<XOR1>(acc.l);
    ls += dppf<ROR4>(ls);
    ls += dppf<ROR8>(ls);
    ls = x32sum(x16sum(ls));
    u32 fa = L_FL + lane * 16, ma = L_FM + (lane & 2) * 4, sa = SY_B, one = 1;
    u32 eo = WS_E3 + (((g * 2 + pair) * 4 + prod) * 2 + ((lane >> 1) & 1)) * 512 + g4 * 128 + (((n16 >> 2) << 1) | (n16 & 1)) * 16;
    if ((lane & 61) == 0) *(LAS f32x2*)(L + ma + w * 16) = f32x2{acc.m, ls};
    asm volatile("" : "+v"(fa), "+v"(ma), "+v"(sa), "+v"(one), "+v"(eo)::"memory");
    a_pv(PP, V, sel, acc);
    *(LAS f32x4*)(L + fa + w * 1024) = acc.o;
    u32 old;
    u64 ex;
    asm volatile("s_mov_b64 %1, exec\n\ts_mov_b64 exec, 1\n\tds_add_rtn_u32 %0, %2, %3\n\ts_mov_b64 exec, %1" : "=&v"(old), "=&s"(ex) : "v"(sa), "v"(one) : "memory");
    f32x2 ml[4];
    f32x4 aw[4];
#pragma unroll
    for (int i = 0; i < 4; ++i) ml[i] = *(LAS f32x2*)(L + ma + i * 16);
#pragma unroll
    for (int i = 0; i < 4; ++i) aw[i] = *(LAS f32x4*)(L + fa + i * 1024);
    const float M = fmaxf(fmaxf(ml[0].x, ml[1].x), fmaxf(ml[2].x, ml[3].x));
    float wt[4], Ls = 0.f;
#pragma unroll
    for (int i = 0; i < 4; ++i) { wt[i] = ex2(ml[i].x - M); Ls += wt[i] * ml[i].y; }
    asm volatile("" : "+v"(old) : "v"(Ls));
    const bool last = __builtin_amdgcn_readfirstlane(old) == 3;
    const f32x4 o = (wt[0] * aw[0] + wt[1] * aw[1] + wt[2] * aw[2] + wt[3] * aw[3]) * __builtin_amdgcn_rcpf(Ls);
    const u32 lse = asu(M + __builtin_amdgcn_logf(Ls));
    const u32 p01 = pk2(o[0], o[1]), p23 = pk2(o[2], o[3]);
    st_sc1(rs, last ? eo : OOB, v4u{perm(p01, tA, 0x07060500u), perm(p23, tA, 0x07060501u), perm(lse, tA, 0x07060502u),
                                    perm(p01, tA, 0x0c0c0403u) | perm(lse, p23, 0x04000c0cu)});
    if (!isA) {
      if (!isBA) {
        lds_wait(L, SY_A, 4);
        LOAD_WO_A();
      }
      const int i16 = lane >> 2;
      u32 go = WS_E3 + ((((h >> 1) * 4 + hw) * 4 + (lane & 3)) * 2 + (h & 1)) * 512 + i16 * 16;
      u32 oa = L_OB + hw * 320 + (4 * (i16 & 8) + (i16 & 7) + 8 * (lane & 3)) * 2, ra = L_OB + (lane & 3) * 320 + g4 * 16;
      u32 e4 = g4 < nu ? WS_E4 + ((16 * (r & 3) + 8 * g4 + 2 * hw + (n16 >> 3)) * 32 + h) * 128 + (n16 & 7) * 16 : OOB;
      u32 s2 = SY_C2;
      asm volatile("" : "+v"(go), "+v"(oa), "+v"(ra), "+v"(e4), "+v"(s2));
      lds_wait(L, SY_C1, 4);
      v4u wf[12];
#pragma unroll
      for (int i = 0; i < 12; ++i) wf[i] = ltr2(L, L_UV + tr128 + i * 4096, L_UV + tr128 + i * 4096 + 512);
      v4u v[2];
      poll16<2>(rs, go, 256, v, tA);
      const float m = asf(perm(v[0].z, v[0].w, 0x07060503u));
      float Mx = fmaxf(m, dppf<XOR1>(m));
      Mx = fmaxf(Mx, dppf<XOR2>(Mx));
      float wq = ex2(m - Mx), Lq = wq + dppf<XOR1>(wq);
      Lq += dppf<XOR2>(Lq);
      wq *= __builtin_amdgcn_rcpf(Lq);
      float y[4];
#pragma unroll
      for (int a = 0; a < 4; ++a) {
        const u32 x = a < 2 ? v[a].x : v[a - 2].y;
        const float lo = wq * asf(perm(x, v[a & 1].w, a < 2 ? 0x05010c0cu : 0x05020c0cu)), hi = wq * asf(x & 0xffff0000u);
        y[a] = (t1 ? hi : lo) + dppf<XOR1>(t1 ? lo : hi);
      }
      const bool t2 = lane & 2;
      const float z0 = (t2 ? y[2] : y[0]) + dppf<XOR2>(t2 ? y[0] : y[2]), z1 = (t2 ? y[3] : y[1]) + dppf<XOR2>(t2 ? y[1] : y[3]);
      const u32 zz = pk2(z0, z1);
      *(LAS u16*)(L + oa) = (u16)zz;
      *(LAS u16*)(L + oa + 128) = (u16)(zz >> 16);
      asm volatile("s_mov_b64 %0, exec\n\ts_mov_b64 exec, 1\n\tds_add_u32 %1, %2\n\ts_mov_b64 exec, %0" : "=&s"(ex) : "v"(s2), "v"(one) : "memory");
      v4u af[4];
      for (u32 spins = 0;; ++spins) {
        const u32 cnt = *(volatile LAS u32*)(L + s2);
        asm volatile("" ::: "memory");
#pragma unroll
        for (int ks = 0; ks < 4; ++ks) af[ks] = lld128(L, ra + ks * 64);
        asm volatile("" : "+v"(af[0]), "+v"(af[1]), "+v"(af[2]), "+v"(af[3])::"memory");
        if (__builtin_amdgcn_readfirstlane(cnt) >= 4 || spins > (1u << 24)) break;
      }
      f32x4 e[3];
#pragma unroll
      for (int u = 0; u < 3; ++u) e[u] = f32x4{0.f, 0.f, 0.f, 0.f};
#pragma unroll
      for (int ks = 0; ks < 4; ++ks)
#pragma unroll
        for (int u = 0; u < 3; ++u) e[u] = mfma16(af[ks], wf[4 * u + ks], e[u]);
      st_sc1(rs, e4, tag4(sel4(g4 & 2, e[2], sel4(g4 & 1, e[1], e[0])), tA));
    }
  } else if (isBA) {
    const int oct = hw * 64 + bi;
    if (oct < 72) {
      lds_wait(L, SY_C2, 4);
      __builtin_amdgcn_s_sleep(10);
      reduce_oct<4>(rs, WS_E4 + oct * 4096 + lane * 16, tA, p.out + oct * 8, lane);
      if (oct == 0 && lane == 0) __hip_atomic_store((GAS u64*)(p.ws + WS_EPOCH), ((u64)tB << 32) | tA, __ATOMIC_RELAXED, __HIP_MEMORY_SCOPE_AGENT);
    }
  } else if (!isA) {
    VMWAIT(0);
    lds_inc(L, SY_WO);
    lds_wait(L, SY_WO, 4);
    const v4u af0 = lld128(L, L_OA + (lane & 3) * 320 + g4 * 16), af1 = lld128(L, L_OA + (lane & 3) * 320 + 64 + g4 * 16);
    f32x4 e[9];
#pragma unroll
    for (int u = 0; u < 9; ++u) {
      const u32 t0 = L_WO + tr128 + u * 8192;
      e[u] = mfma16(af1, ltr2(L, t0 + 4096, t0 + 4608), mfma16(af0, ltr2(L, t0, t0 + 512), f32x4{0.f, 0.f, 0.f, 0.f}));
    }
    const u32 eo = WS_E6 + ((8 * g4 + 2 * hw + (n16 >> 3)) * 64 + bw) * 128 + (n16 & 7) * 16;
#pragma unroll
    for (int k = 0; k < 2; ++k)
      st_sc1(rs, eo + k * 32 * 8192, tag4(sel4(g4 & 2, sel4(g4 & 1, e[4 * k + 3], e[4 * k + 2]), sel4(g4 & 1, e[4 * k + 1], e[4 * k])), tA));
    st_sc1(rs, g4 == 0 ? eo + 64 * 8192 : OOB, tag4(e[8], tA));
    const int oct = hw * 64 + bw;
    if (oct < 72) reduce_oct<8>(rs, WS_E6 + oct * 8192 + lane * 16, tA, p.out + 576 + oct * 8, lane);
  }
}

extern "C" void kernel_launch(void* const* d_in, const int* in_sizes, int n_in, void* d_out, int out_size, void* d_ws, size_t ws_size,
                              hipStream_t stream) {
  Params p;
  p.Ax = (const char*)d_in[0]; p.AK = (const char*)d_in[1]; p.AV = (const char*)d_in[2]; p.Awq = (const char*)d_in[3]; p.Awo = (const char*)d_in[4];
  p.Bx = (const char*)d_in[5]; p.BC = (const char*)d_in[6]; p.Bwq = (const char*)d_in[7]; p.Bwuk = (const char*)d_in[8]; p.Bwuv = (const char*)d_in[9];
  p.Bwo = (const char*)d_in[10];
  p.out = (u16*)d_out;
  p.ws = (char*)d_ws;
  (void)hipFuncSetAttribute((const void*)decode_mega, hipFuncAttributeMaxDynamicSharedMemorySize, LDS_BYTES);
  decode_mega<<<256, 512, LDS_BYTES, stream>>>(p);
}
```

```cpp
#include <hip/hip_runtime.h>
#include <stdint.h>

typedef unsigned int u32;
typedef unsigned long long u64;
typedef unsigned short u16;
typedef u32 v2u __attribute__((ext_vector_type(2)));
typedef u32 v4u __attribute__((ext_vector_type(4)));
typedef u32 v8u __attribute__((ext_vector_type(8)));
typedef u32 v16u __attribute__((ext_vector_type(16)));
typedef float f32x2 __attribute__((ext_vector_type(2)));
typedef float f32x4 __attribute__((ext_vector_type(4)));
typedef float f32x16 __attribute__((ext_vector_type(16)));
typedef __bf16 bf16x8 __attribute__((ext_vector_type(8)));
typedef __bf16 bf16x2 __attribute__((ext_vector_type(2)));
typedef short s16x4 __attribute__((ext_vector_type(4)));
typedef __amdgpu_buffer_rsrc_t rsrc_t;
#define DI __device__ __forceinline__
#define LAS __attribute__((address_space(3)))
#define GAS __attribute__((address_space(1)))

constexpr int R_A = 11, R_BA = 4, R_BW = 6;
static_assert(2 * R_A + R_BA + R_BW == 32 && R_BA + R_BW + 1 == R_A && R_BA >= 3, "layout of the rounds; every streamer runs the loop");

constexpr int WS_EPOCH = 0;
constexpr int WS_FQ = 32768;
constexpr int WS_QABS = 65536;
constexpr int WS_MF = 196608;
constexpr int WS_E1 = 262144;
constexpr int WS_E2 = 393216;
constexpr int WS_E3 = 524288;
constexpr int WS_E6 = 1 << 20;
constexpr int WS_E4 = WS_E6 + 72 * 8192;
constexpr int WS_PART = 2 << 20;
constexpr int WS_BYTES = 4 << 20;
constexpr u32 OOB = 0x7f000000u;
constexpr int LDS_BYTES = 163840;
constexpr int L_QBW = 0, L_QAW = 36864;
constexpr int L_XV = 110592;
constexpr int L_WO = 0;
constexpr int L_UV = 73728;
constexpr int L_X = 131072;
constexpr int L_SX = L_X, L_P = L_X + 16384, L_WM = L_X + 24576, L_WL = L_X + 25600;
constexpr int L_QS = 139264;
constexpr int L_FL = L_QS + 1024;
constexpr int L_FM = L_FL + 4096;
constexpr int L_OA = L_FM + 128;
constexpr int L_QB = L_OA + 1280;
constexpr int L_OB = L_QB;
constexpr int L_CL = L_QB + 1280;
constexpr int L_SYNC = L_X + 26624;
static_assert(L_CL + 4352 <= L_WM, "LDS map");
enum { SY_A = L_SYNC, SY_B = L_SYNC + 4, SY_LAST = L_SYNC + 8, SY_C1 = L_SYNC + 12, SY_C2 = L_SYNC + 16, SY_WO = L_SYNC + 20 };

constexpr float SCALE = 0.08838834764831845f;
constexpr float LOG2E = 1.4426950408889634f;
constexpr u32 SPIN_CAP = 1u << 20;

struct Params {
  const char *Ax, *AK, *AV, *Awq, *Awo, *Bx, *BC, *Bwq, *Bwuk, *Bwuv, *Bwo;
  u16* out;
  char* ws;
};

DI f32x4 mfma16(v4u a, v4u b, f32x4 c) {
  return __builtin_amdgcn_mfma_f32_16x16x32_bf16(__builtin_bit_cast(bf16x8, a), __builtin_bit_cast(bf16x8, b), c, 0, 0, 0);
}
DI f32x16 mfma32(v4u a, v4u b, f32x16 c) {
  return __builtin_amdgcn_mfma_f32_32x32x16_bf16(__builtin_bit_cast(bf16x8, a), __builtin_bit_cast(bf16x8, b), c, 0, 0, 0);
}
DI u32 pk2(float a, float b) { f32x2 v = {a, b}; return __builtin_bit_cast(u32, __builtin_convertvector(v, bf16x2)); }
DI float rbf(float a) { return (float)(__bf16)a; }
DI float asf(u32 x) { return __builtin_bit_cast(float, x); }
DI u32 asu(float x) { return __builtin_bit_cast(u32, x); }
DI float ex2(float x) { return __builtin_amdgcn_exp2f(x); }
DI u32 perm(u32 a, u32 b, u32 s) { return __builtin_amdgcn_perm(a, b, s); }
template <int CTRL> DI float dppf(float x) { return asf(__builtin_amdgcn_mov_dpp(asu(x), CTRL, 0xf, 0xf, true)); }
template <int CTRL> DI u32 dppu(u32 x) { return __builtin_amdgcn_mov_dpp(x, CTRL, 0xf, 0xf, true); }
constexpr int XOR1 = 0xB1, XOR2 = 0x4E, XOR7 = 0x141, XOR15 = 0x140, ROR8 = 0x128, ROR4 = 0x124, BCAST = 0x150;
DI float x16max(float x) { auto s = __builtin_amdgcn_permlane16_swap(asu(x), asu(x), false, false); return fmaxf(asf(s[0]), asf(s[1])); }
DI float x32max(float x) { auto s = __builtin_amdgcn_permlane32_swap(asu(x), asu(x), false, false); return fmaxf(asf(s[0]), asf(s[1])); }
DI float x16sum(float x) { auto s = __builtin_amdgcn_permlane16_swap(asu(x), asu(x), false, false); return asf(s[0]) + asf(s[1]); }
DI float x32sum(float x) { auto s = __builtin_amdgcn_permlane32_swap(asu(x), asu(x), false, false); return asf(s[0]) + asf(s[1]); }
DI float sum_hi3(f32x4 a) {
  auto s0 = __builtin_amdgcn_permlane32_swap(asu(a[0]), asu(a[1]), false, false);
  auto s1 = __builtin_amdgcn_permlane32_swap(asu(a[2]), asu(a[3]), false, false);
  auto t = __builtin_amdgcn_permlane16_swap(asu(asf(s0[0]) + asf(s0[1])), asu(asf(s1[0]) + asf(s1[1])), false, false);
  const float c = asf(t[0]) + asf(t[1]);
  return c + dppf<ROR8>(c);
}
DI f32x4 sel4(bool c, f32x4 a, f32x4 b) { return f32x4{c ? a[0] : b[0], c ? a[1] : b[1], c ? a[2] : b[2], c ? a[3] : b[3]}; }

#define BAR() asm volatile("s_waitcnt lgkmcnt(0)\n\ts_barrier" ::: "memory")
#define VMWAIT(n) asm volatile("s_waitcnt vmcnt(%0)" ::"n"(n) : "memory")
#define SB() __builtin_amdgcn_sched_barrier(0)

DI v4u lld128(LAS char* L, u32 o) { return *(LAS v4u*)(L + o); }
DI void lst128(LAS char* L, u32 o, v4u v) { *(LAS v4u*)(L + o) = v; }
DI v2u ltr(LAS char* L, u32 o) { return __builtin_bit_cast(v2u, __builtin_amdgcn_ds_read_tr16_b64_v4i16((LAS s16x4*)(L + o))); }
DI v4u ltr2(LAS char* L, u32 o0, u32 o1) { v2u a = ltr(L, o0), b = ltr(L, o1); return v4u{a.x, a.y, b.x, b.y}; }
DI void dma16(const char* sbase, u32 voff, u32 lds) {
  u32 keep;
  asm volatile("s_mov_b32 %0, m0\n\ts_mov_b32 m0, %3\n\ts_nop 2\n\tglobal_load_lds_dwordx4 %1, %2 nt\n\ts_mov_b32 m0, %0"
               : "=&s"(keep) : "v"(voff), "s"(sbase), "s"(lds) : "memory");
}
DI v4u ldnt(const char* base, u32 voff, int imm) { return __builtin_nontemporal_load((const GAS v4u*)((const GAS char*)base + voff + imm)); }
DI v4u ld_sc1(rsrc_t rs, u32 o) { return __builtin_amdgcn_raw_buffer_load_b128(rs, o, 0, 16); }
DI v2u ld_sc1_8(rsrc_t rs, u32 o) { return __builtin_amdgcn_raw_buffer_load_b64(rs, o, 0, 16); }
DI void st_sc1(rsrc_t rs, u32 o, v4u v) { __builtin_amdgcn_raw_buffer_store_b128(v, rs, o, 0, 16); }
DI void st_sc1_8(rsrc_t rs, u32 o, v2u v) { __builtin_amdgcn_raw_buffer_store_b64(v, rs, o, 0, 16); }
DI v4u tag4(f32x4 e, u32 tA) {
  v4u v;
#pragma unroll
  for (int k = 0; k < 4; ++k) v[k] = perm(asu(e[k]) + 0x80u, tA, 0x07060500u + k);
  return v;
}
DI f32x4 untag4(v4u v) { return f32x4{asf(v.x & ~0xffu), asf(v.y & ~0xffu), asf(v.z & ~0xffu), asf(v.w & ~0xffu)}; }
template <int N> DI void poll16(rsrc_t rs, u32 o, u32 stride, v4u (&v)[N], u32 tA) {
  for (u32 spins = 0;; ++spins) {
    asm volatile("" ::: "memory");
    bool ok = true;
#pragma unroll
    for (int i = 0; i < N; ++i) v[i] = ld_sc1(rs, o + i * stride);
#pragma unroll
    for (int i = 0; i < N; ++i) ok = ok && (perm(v[i].y, v[i].x, 0x0c0c0400u) | perm(v[i].w, v[i].z, 0x04000c0cu)) == tA;
    if (__all(ok) || spins > SPIN_CAP) break;
    __builtin_amdgcn_s_sleep(1);
  }
}
DI void poll_flags(rsrc_t rs, u32 o, u32 tA, u32 tB) {
  for (u32 spins = 0;; ++spins) {
    asm volatile("" ::: "memory");
    const v2u v = ld_sc1_8(rs, o);
    if (__all(v.x == tA && v.y == tB) || spins > SPIN_CAP) break;
    __builtin_amdgcn_s_sleep(1);
  }
  asm volatile("" ::: "memory");
}
DI u32 lds_inc(LAS char* L, u32 o) {
  asm volatile("s_waitcnt lgkmcnt(0)" ::: "memory");
  u32 old = 0;
  if ((threadIdx.x & 63) == 0) old = __hip_atomic_fetch_add((LAS u32*)(L + o), 1u, __ATOMIC_RELAXED, __HIP_MEMORY_SCOPE_WORKGROUP);
  return __builtin_amdgcn_readfirstlane(old);
}
DI void lds_wait(LAS char* L, u32 o, u32 want) {
#pragma unroll 1
  for (u32 spins = 0; spins < (1u << 24); ++spins) {
    if (__builtin_amdgcn_readfirstlane(__hip_atomic_load((LAS u32*)(L + o), __ATOMIC_RELAXED, __HIP_MEMORY_SCOPE_WORKGROUP)) >= want) break;
    __builtin_amdgcn_s_sleep(1);
  }
  asm volatile("" ::: "memory");
}
DI u32 sw2(u32 row) { return (row & 2) | ((row >> 1) & 4); }
DI u32 fsw(u32 row) { return ((row & 3) << 2) | ((row >> 2) & 3); }

DI f32x4 gemv288(LAS char* L, u32 base, u32 xo) {
  f32x4 d = {0.f, 0.f, 0.f, 0.f};
#pragma unroll
  for (int ks = 0; ks < 9; ++ks) d = mfma16(ltr2(L, base + ks * 4096, base + ks * 4096 + 512), lld128(L, xo + ks * 64), d);
  return d;
}
DI v2u fetch_q(rsrc_t rs, u32 o, u32 stride, u32 tA) {
  v4u v[2];
  poll16<2>(rs, o, stride, v, tA);
  const f32x4 q = untag4(v[0]) + untag4(v[1]);
  return v2u{pk2(q[0], q[1]), pk2(q[2], q[3])};
}
template <int N> DI void reduce_oct(rsrc_t rs, u32 o, u32 tA, u16* out_, int lane) {
  GAS u16* out = (GAS u16*)out_ + ((lane >> 5) + ((lane >> 3) & 2)) * 1152 + (lane & 7);
  asm volatile("" : "+v"(out), "+v"(o));
  v4u v[N];
  poll16<N>(rs, o, 1024, v, tA);
  f32x4 a = untag4(v[0]);
#pragma unroll
  for (int i = 1; i < N; ++i) a += untag4(v[i]);
  const float c = sum_hi3(a);
  const u32 pk = pk2(c, dppf<XOR1>(c));
  if (!(lane & 9)) __hip_atomic_store((GAS u32*)out, pk, __ATOMIC_RELAXED, __HIP_MEMORY_SCOPE_AGENT);
}

struct Acc { float m, l; f32x4 o; };
DI float a_qk(const v4u (&K)[8], const v4u (&QF)[8], bool t1) {
  f32x4 d = {0.f, 0.f, 0.f, 0.f};
#pragma unroll
  for (int j = 0; j < 8; ++j) d = mfma16(K[j], QF[j], d);
  const float keep = t1 ? d[3] : d[0], send = t1 ? d[1] : d[2];
  return keep + dppf<XOR1>(send);
}
DI u32 a_soft(float S, Acc& a) {
  const float t = rbf(rbf(S) * SCALE) * LOG2E;
  float cm = fmaxf(t, dppf<XOR1>(t));
  cm = fmaxf(cm, dppf<ROR4>(cm));
  cm = fmaxf(cm, dppf<ROR8>(cm));
  cm = x32max(x16max(cm));
  const float mn = fmaxf(a.m, cm);
  const float al = ex2(a.m - mn), p = ex2(t - mn);
  a.l = a.l * al + p;
  a.m = mn;
  a.o *= al;
  return pk2(p, dppf<XOR2>(p));
}
template <int J> DI void a_pv1(u32 PP, v4u V, const u32 (&sel)[4], Acc& a) {
  const u32 X = dppu<BCAST + 4 * (J >> 1) + (J & 1)>(PP);
  a.o = mfma16(V, v4u{perm(X, X, sel[0]), perm(X, X, sel[1]), perm(X, X, sel[2]), perm(X, X, sel[3])}, a.o);
}
DI void a_pv(u32 PP, const v4u (&V)[8], const u32 (&sel)[4], Acc& a) {
  a_pv1<0>(PP, V[0], sel, a); a_pv1<1>(PP, V[1], sel, a); a_pv1<2>(PP, V[2], sel, a); a_pv1<3>(PP, V[3], sel, a);
  a_pv1<4>(PP, V[4], sel, a); a_pv1<5>(PP, V[5], sel, a); a_pv1<6>(PP, V[6], sel, a); a_pv1<7>(PP, V[7], sel, a);
}
DI void a_begin(LAS char* L, int lane, v4u (&QF)[8], Acc& a) {
  const int n = lane & 15, g = lane >> 4;
  const u32 o = L_QS + ((n >> 1) & 1) * 256 + (2 * g + (n & 1)) * 16;
  const v4u F0 = lld128(L, o), F1 = lld128(L, o + 128), Z = {0u, 0u, 0u, 0u};
#pragma unroll
  for (int u = 0; u < 4; ++u) { const bool on = (n >> 2) == u; QF[2 * u] = on ? F0 : Z; QF[2 * u + 1] = on ? F1 : Z; }
  a.m = -INFINITY; a.l = 0.f; a.o = f32x4{0.f, 0.f, 0.f, 0.f};
}

template <bool FIRST, class F>
DI void ba_pass(LAS char* L, int w, int lane, const v4u (&qf)[16], f32x16 (&acc)[2], float& m, float& l, F&& after_qk) {
  asm volatile("" : "+v"(lane));
  const int kb = w & 3, ch = w >> 2, hh = lane >> 5, r31 = lane & 31;
  f32x16 S;
#pragma unroll
  for (int i = 0; i < 16; ++i) S[i] = 0.f;
  const u32 rowb = (32 * kb + r31) * 1024 + ch * 512, f = fsw(r31);
#pragma unroll
  for (int ks = 0; ks < 16; ++ks) S = mfma32(lld128(L, rowb + (ks >> 3) * 256 + (((2 * (ks & 7) + hh) ^ f) * 16)), qf[ks], S);
  float s[8];
  {
    v4u a, b;
#pragma unroll
    for (int j = 0; j < 4; ++j) { a[j] = asu(ch ? S[j] : S[8 + j]); b[j] = asu(ch ? S[4 + j] : S[12 + j]); }
    if (FIRST) { asm volatile("" : "+v"(a), "+v"(b)); BAR(); }
    lst128(L, L_SX + w * 2048 + lane * 16, a);
    lst128(L, L_SX + w * 2048 + 1024 + lane * 16, b);
    asm volatile("" ::: "memory");
    after_qk();
    BAR();
    a = lld128(L, L_SX + (w ^ 4) * 2048 + lane * 16);
    b = lld128(L, L_SX + (w ^ 4) * 2048 + 1024 + lane * 16);
#pragma unroll
    for (int j = 0; j < 4; ++j) {
      s[j] = ((ch ? S[8 + j] : S[j]) + asf(a[j])) * (SCALE * LOG2E);
      s[4 + j] = ((ch ? S[12 + j] : S[4 + j]) + asf(b[j])) * (SCALE * LOG2E);
    }
  }
  float cm = fmaxf(fmaxf(fmaxf(s[0], s[1]), fmaxf(s[2], s[3])), fmaxf(fmaxf(s[4], s[5]), fmaxf(s[6], s[7])));
  cm = x32max(cm);
  if (lane < 32) *(LAS float*)(L + L_WM + (w * 32 + lane) * 4) = cm;
  BAR();
  float mn = FIRST ? -INFINITY : m;
#pragma unroll
  for (int i = 0; i < 8; ++i) mn = fmaxf(mn, *(LAS float*)(L + L_WM + (i * 32 + r31) * 4));
  float ps = 0.f;
#pragma unroll
  for (int j = 0; j < 8; ++j) { s[j] = ex2(s[j] - mn); ps += s[j]; }
  lst128(L, L_P + (2 * kb + ch) * 1024 + lane * 16, v4u{pk2(s[0], s[1]), pk2(s[2], s[3]), pk2(s[4], s[5]), pk2(s[6], s[7])});
  if (FIRST) {
    l = ps;
#pragma unroll
    for (int i = 0; i < 16; ++i) { acc[0][i] = 0.f; acc[1][i] = 0.f; }
  } else {
    const float al = ex2(m - mn);
    l = l * al + ps;
    acc[0] *= al;
    acc[1] *= al;
  }
  m = mn;
  BAR();
  const int q = (lane & 15) >> 2, pp = lane & 3, blk = (lane >> 4) & 1;
  u32 ta[2][2];
#pragma unroll
  for (int T = 0; T < 2; ++T)
#pragma unroll
    for (int t = 0; t < 2; ++t) {
      const u32 c = 8 * w + 4 * T + 2 * blk + (pp >> 1);
      ta[T][t] = (8 * t + 4 * hh + q) * 1024 + ((c ^ ((q << 2) | (2 * t + hh))) * 16) + 8 * (pp & 1);
    }
#pragma unroll
  for (int ks = 0; ks < 8; ++ks) {
    const v4u P = lld128(L, L_P + ks * 1024 + lane * 16);
#pragma unroll
    for (int T = 0; T < 2; ++T) acc[T] = mfma32(ltr2(L, ta[T][0] + ks * 16384, ta[T][1] + ks * 16384), P, acc[T]);
  }
  asm volatile("" : "+v"(acc[0]), "+v"(acc[1]));
}

__global__ void __launch_bounds__(512, 2) decode_mega(Params args) {
  extern __shared__ __attribute__((aligned(16))) char smem[];
  LAS char* L = (LAS char*)smem;
  v16u ka;
  v8u kb;
  v2u kc;
  asm volatile("s_load_dwordx16 %0, %3, 0x0\n\ts_load_dwordx8 %1, %3, 0x40\n\ts_load_dwordx2 %2, %3, 0x60"
               : "=&s"(ka), "=&s"(kb), "=&s"(kc) : "s"(__builtin_amdgcn_kernarg_segment_ptr()) : "memory");
  SB();
  const int tid = threadIdx.x, lane = tid & 63;
  int w = __builtin_amdgcn_readfirstlane(tid >> 6), wg = blockIdx.x;
  u32 vo, ld, step, lstep;
  const char* src0;
  {
#define PICK(m, a, b) ((b) ^ (((a) ^ (b)) & (m)))
    const u32 g = (u32)wg >> 3, r = (wg + g) & 7, hw = w & 3, r0 = r & 1;
    const u32 mH = -((u32)w >> 2), mA = (r >> 2) - 1, mW = -((r >> 2) & (r >> 1) & 1), mB = ~(mA | mW), mK = mW & mH, mX = mA & mH;
    const u32 koff = PICK(mA, PICK(mH, 40u & ((w & 1) - 1), 56u), PICK(mW, PICK(mH, 64u, 72u), 48u));
    asm volatile("s_load_dwordx2 %0, %1, %2" : "=s"(src0) : "s"(__builtin_amdgcn_kernarg_segment_ptr()), "s"(koff) : "memory");
    const u32 hoff = ((g & ~7u) | (wg & 7)) * 256;
    step = PICK(mB, 16384u, PICK(mK, 16u * 8192, 32u * 8192));
    lstep = PICK(mB, 16384u, 4096u);
    const u32 xo = (w & 2) * 448;
    ld = PICK(mX, L_XV + (w & 1) * 4608 + xo, PICK(mB, w, hw) * 1024 + (L_UV & mW & ~mH));
    const u32 sA = (r >> 1) * (288 * 8192) + g * 256, sB = (((wg & 7) * 8 + (g >> 3) * 2 + r0) * 256 + w) * 1024, sK = (r0 * 256 + 4 * hw) * 8192 + hoff;
    const u32 so = PICK(mX, xo, PICK(mB, sB, PICK(mK, sK, PICK(mA, sA, hoff) + 8 * 8192 * w + r0 * 128)));
    const u32 sh = PICK(mB | mX, 6u, PICK(mK, 4u, 3u)), row = (u32)lane >> sh;
    const u32 xs = PICK(mB, fsw(w), PICK(mK, 4 * hw, 4 * (w & 1))) & ~mX, xm = PICK(mK, 3u, 2u);
    vo = row * 8192 + ((((u32)lane - (row << sh)) ^ xs ^ (row & xm)) * 16) + so;
    u32 keep;
#define PIECE "global_load_lds_dwordx4 %1, %3 nt\n\ts_add_u32 m0, m0, %5\n\tv_add_u32 %1, %4, %1\n\t"
    asm volatile("s_mov_b32 %0, m0\n\ts_mov_b32 m0, %2\n\ts_cmp_lg_u32 %6, 0\n\ts_waitcnt lgkmcnt(0)\n\ts_cbranch_scc1 1f\n\t"
                 PIECE PIECE PIECE PIECE PIECE PIECE PIECE PIECE "s_branch 2f\n"
                 "1:\n\ts_setprio 3\n\tglobal_load_lds_dwordx4 %1, %3\n\tglobal_load_lds_dwordx4 %1, %3 offset:1792\n\tglobal_load_lds_dwordx4 %1, %3 offset:2688\n"
                 "2:\n\ts_mov_b32 m0, %0"
                 : "=&s"(keep), "+v"(vo) : "s"(ld), "s"(src0), "s"(step), "s"(lstep), "s"(mX) : "memory", "scc");
    ld += 8 * lstep;
  }
  asm volatile("s_waitcnt lgkmcnt(0)" : "+s"(ka), "+s"(kb), "+s"(kc), "+s"(src0), "+s"(w), "+s"(wg)::"memory");
  Params p;
#define PTR(v, i) ((char*)(((u64)v[(i) + 1] << 32) | v[(i)]))
  p.AK = PTR(ka, 2); p.AV = PTR(ka, 4); p.Awq = PTR(ka, 6); p.Awo = PTR(ka, 8); p.BC = PTR(ka, 12);
  p.Bwo = PTR(kb, 4); p.out = (u16*)PTR(kb, 6); p.ws = PTR(kc, 0);
  const int hw = w & 3, g = wg >> 3, s = wg & 7, r = (s + g) & 7, kvh = g >> 1;
  const bool isA = r < 4, isBA = r == 4 || r == 5, hlp = w >= 4;
  u64 epoch;
  asm volatile("s_load_dwordx2 %0, %1, 0x0" : "=s"(epoch) : "s"(p.ws) : "memory");
  if (hlp) __builtin_amdgcn_s_setprio(3);
  if (tid < 8 && !isBA) *(LAS u32*)(L + L_SYNC + tid * 4) = 0u;
  const int n16 = lane & 15, g4 = lane >> 4;
  const int bi = s * 8 + (g >> 3) * 2 + (r & 1);
  const int h = (g >> 3) * 8 + s, half = r - 6, bw = h * 2 + half;
  {
    int n = isA ? (hlp ? 0 : 1) : isBA ? 0 : 8;
#pragma unroll 1
    for (int bt = 0; bt < 2; ++bt) {
#pragma unroll 1
      for (; n > 0; --n) { VMWAIT(8); dma16(src0, vo, ld); vo += step; ld += lstep; }
      src0 = isA ? p.Awq : src0;
      ld = isA ? L_QAW + w * 1024 : (8 + w) * 1024;
      n = isA ? (hlp ? 0 : 9) : isBA ? 8 : 0;
      vo = (vo ^ (isA ? 0 : 32)) + (isA ? (u32)(((g & 1) - (r >> 1) - 1) * (288 * 8192) + kvh * 512 - g * 256 + ((r ^ 1) - (r & 1)) * 128) : (u32)(-120 * 1024));
    }
    asm volatile("s_waitcnt lgkmcnt(0)" : "+s"(epoch)::"memory");
  }
  const rsrc_t rs = __builtin_amdgcn_make_buffer_rsrc((void*)p.ws, 0, WS_BYTES, 0x00020000);
  const u32 trrow = 8 * g4 + (n16 >> 2);
  const u32 tr128 = trrow * 128 + ((2 * hw + ((lane & 3) >> 1)) ^ sw2(trrow)) * 16 + 8 * (lane & 1);
  const u32 tA = (((u32)epoch & 0x7f7f7f7fu) + 0x01010101u) ^ ((u32)epoch & 0x80808080u), tB = (u32)(epoch >> 32) + 1u;

  u32 voffK, voffV, sel[4];
#define STREAM_CONSTS() do { u32 l_; asm volatile("v_mbcnt_lo_u32_b32 %0, -1, 0\n\tv_mbcnt_hi_u32_b32 %0, -1, %0" : "=v"(l_)); \
    const u32 n_ = l_ & 15, g_ = l_ >> 4; \
    voffK = (n_ >> 1) * 4096 + (2 * g_ + (n_ & 1)) * 16; voffV = g_ * 8192 + n_ * 16; \
    const u32 h2 = (n_ >> 1) & 1, by = (2 * h2) | ((2 * h2 + 1) << 8); \
    const u32 on = (n_ & 1) ? ((by << 16) | 0x0c0cu) : (0x0c0c0000u | by); \
    _Pragma("unroll") for (int i = 0; i < 4; ++i) sel[i] = (n_ >> 2) == i ? on : 0x0c0c0c0cu; } while (0)
  const int pair = isA ? r >> 1 : isBA ? r & 1 : (r & 1) ^ 1, prod = isA ? r & 1 : isBA ? 2 : 3;
  const int nr = isA ? R_A : isBA ? R_BA : R_BW;
  const int p1 = isA ? 2 + prod : isBA ? 3 * R_BW + 7 : 7;
  auto roff = [&](int t) { return (u32)(t == 0 ? (isA ? prod : p1 - 3) : p1 + 3 * (t - 1)) * 32768u; };
  const size_t kv0 = ((size_t)((g & 1) * 2 + pair) * 16 + kvh) * (4096 * 256) + 4096 + w * 1024;
  const u32 q_off = WS_E2 + (g * 2 + pair) * 2048 + lane * 16;
#define LDK(o, j) ldnt(p.AK + kv0 + (o), voffK, ((j) >> 1) * 256 + ((j) & 1) * 128 - 4096)
#define LDV(o, j) ldnt(p.AV + kv0 + (o), voffV, ((j) >> 1) * 256 + ((j) & 1) * 4096 - 4096)
#define LOAD8(R, LD, id) do { _Pragma("unroll") for (int j = 0; j < 8; ++j) R[j] = LD(id, j); SB(); } while (0)
#define PACED8(R, LD, id) do { _Pragma("unroll") for (int j = 0; j < 8; ++j) { VMWAIT(8); R[j] = LD(id, j); SB(); } } while (0)
  const bool t1 = lane & 1;
  const int nu = r == 7 ? 3 : 2;
#define LOAD_WO(DST, SRC, NU, NI) do { \
    const u32 vx = (lane >> 3) * 1152 + (((lane & 7) ^ sw2((lane >> 3) | ((w & 1) << 3))) * 16); \
    u32 lx = (DST) + hw * 1024; \
    _Pragma("unroll 1") for (int u = 0; u < (NU); ++u) \
      _Pragma("unroll") for (int i = 0; i < (NI); ++i) { dma16((SRC) + (u32)(u * 128 + (32 * i + 8 * hw) * 1152), vx, lx); lx += 4096; } } while (0)
#define LOAD_WO_A() do { \
    LOAD_WO(L_UV, p.Awo + (u32)(h * 128 * 1152 + (r & 3) * 256), nu, 4); \
    VMWAIT(0); \
    lds_inc(L, SY_C1); } while (0)

  const u32 o0 = roff(0), o1 = roff(1);
  v4u K[8], V[8], QF[8];
  Acc acc;

  if (!isBA && !hlp) {
    STREAM_CONSTS();
    BAR();
    if (!isA) BAR();
    v4u K0[8], V0[8];
    PACED8(K0, LDK, o0);
    VMWAIT(8);
    if (isA) BAR();
    PACED8(V0, LDV, o0);
    PACED8(K, LDK, o1);
    PACED8(V, LDV, o1);
    BAR();
    a_begin(L, lane, QF, acc);
    const u32 PP = a_soft(a_qk(K0, QF, t1), acc);
    a_pv(PP, V0, sel, acc);
  } else if (isA) {
    const u32 xo = L_XV + (lane & 3) * 1152 + g4 * 16;
    VMWAIT(0);
    BAR();
    f32x4 d = gemv288(L, L_QBW + tr128, xo + (r >> 1) * 576);
    st_sc1(rs, n16 < 4 ? WS_E1 + ((g * 2 + (r >> 1)) * 4 + n16) * 512 + (r & 1) * 256 + hw * 64 + g4 * 16 : OOB, tag4(d, tA));
    BAR();
    d = gemv288(L, L_QAW + tr128, xo + 4608 + (g & 1) * 576);
    st_sc1(rs, n16 < 4 ? WS_E2 + ((kvh * 4 + n16) * 2 + (g & 1)) * 1024 + (r ^ 1) * 256 + hw * 64 + g4 * 16 : OOB, tag4(d, tA));
    __builtin_amdgcn_s_sleep(45);
    if (w == 4) *(LAS v2u*)(L + L_QS + lane * 8) = fetch_q(rs, q_off, 1024, tA);
    BAR();
  } else if (isBA) {
    const char* cb = p.BC + (u32)((bi * 256 + w + 128) * 1024);
    const u32 vc0 = (lane ^ fsw(w)) * 16;
    v4u qs[4], c2[16];
#pragma unroll
    for (int i = 0; i < 16; ++i) c2[i] = ldnt(cb + 8 * i * 1024, vc0 ^ ((i & 1) * 32), 0);
    SB();
    VMWAIT(0);
    poll_flags(rs, WS_FQ + (8 * w + (lane & 7)) * 128, tA, tB);
#pragma unroll
    for (int i = 0; i < 4; ++i) qs[i] = ld_sc1(rs, WS_QABS + ((bi >> 4) * 32 + 4 * w + i) * 1024 + (lane ^ fsw(4 * w + i)) * 16);
#pragma unroll
    for (int i = 0; i < 4; ++i) lst128(L, L_X + (4 * w + i) * 1024 + lane * 16, qs[i]);
    BAR();
    v4u qf[16];
    {
      const u32 rowb = L_X + (lane & 31) * 1024 + (w >> 2) * 512, f = fsw(lane & 31);
#pragma unroll
      for (int ks = 0; ks < 16; ++ks) qf[ks] = lld128(L, rowb + (ks >> 3) * 256 + (((2 * (ks & 7) + (lane >> 5)) ^ f) * 16));
    }
    f32x16 ac[2];
    float m, l;
    SB();
    ba_pass<true>(L, w, lane, qf, ac, m, l, [] {});
    if (tid < 8) *(LAS u32*)(L + L_SYNC + tid * 4) = 0u;
    BAR();
#pragma unroll
    for (int i = 0; i < 16; ++i) lst128(L, (8 * i + w) * 1024 + lane * 16, c2[i]);
    v2u qpk = {0u, 0u};
    if (w == 4) qpk = fetch_q(rs, q_off, 1024, tA);
    BAR();
    v4u K0[8], V0[8];
    if (!hlp) {
      STREAM_CONSTS();
      LOAD8(K0, LDK, o0);
    }
    ba_pass<false>(L, w, lane, qf, ac, m, l, [&] {
      if (!hlp) {
        STREAM_CONSTS();
        LOAD8(V0, LDV, o0);
      }
    });
    l = x32sum(l);
    if (lane < 32) *(LAS float*)(L + L_WL + (w * 32 + lane) * 4) = l;
    BAR();
    {
      const u32 hd = lane & 31, hh = lane >> 5;
#pragma unroll
      for (int T = 0; T < 2; ++T)
#pragma unroll
        for (int rq = 0; rq < 4; ++rq)
          *(LAS v2u*)(L + hd * 1024 + (((8 * w + 4 * T + rq) ^ (hd & 15)) * 16) + 8 * hh) =
              v2u{pk2(ac[T][4 * rq], ac[T][4 * rq + 1]), pk2(ac[T][4 * rq + 2], ac[T][4 * rq + 3])};
    }
    if (w == 4) *(LAS v2u*)(L + L_QS + lane * 8) = qpk;
    BAR();
    if (hlp) {
      __builtin_amdgcn_s_sleep(26);
#pragma unroll
      for (int i = 0; i < 8; ++i) {
        const u32 hd = 8 * hw + i;
        st_sc1(rs, WS_PART + (bi * 32 + hd) * 1024 + lane * 16, lld128(L, hd * 1024 + ((lane ^ (hd & 15)) * 16)));
      }
      float ls = 0.f;
#pragma unroll
      for (int i = 0; i < 8; ++i) ls += *(LAS float*)(L + L_WL + (i * 32 + (lane & 31)) * 4);
      VMWAIT(0);
      if (lds_inc(L, SY_A) == 3) st_sc1(rs, lane < 32 ? WS_MF + (bi * 8 + (lane & 7)) * 128 + (lane >> 3) * 16 : OOB, v4u{asu(m), tA, asu(ls), tB});
      lds_wait(L, SY_LAST, 4);
      LOAD_WO_A();
    } else {
      STREAM_CONSTS();
      a_begin(L, lane, QF, acc);
      const u32 PP = a_soft(a_qk(K0, QF, t1), acc);
      a_pv(PP, V0, sel, acc);
      asm volatile("" : "+v"(acc.o)::"memory");
      lds_wait(L, SY_A, 4);
      LOAD8(K, LDK, o1);
      LOAD8(V, LDV, o1);
    }
  } else {
    VMWAIT(0);
    if (w < 6) *(LAS v2u*)(L + L_QB + (2 * hw + (lane >> 5)) * 320 + (lane & 31) * 8) = fetch_q(rs, WS_E1 + h * 4096 + hw * 1024 + lane * 16, 2048, tA);
    BAR();
    {
      f32x4 d[4];
#pragma unroll
      for (int T = 0; T < 4; ++T) d[T] = f32x4{0.f, 0.f, 0.f, 0.f};
#pragma unroll
      for (int ks = 0; ks < 4; ++ks) {
        const v4u qb = lld128(L, L_QB + (lane & 3) * 320 + ks * 64 + g4 * 16);
        const u32 a0 = (64 * hw + n16) * 256 + (((4 * ks + g4) ^ n16) * 16);
#pragma unroll
        for (int T = 0; T < 4; ++T) d[T] = mfma16(lld128(L, a0 + T * 4096), qb, d[T]);
      }
      const int T = n16 >> 2;
      const f32x4 e = sel4(T & 2, sel4(T & 1, d[3], d[2]), sel4(T & 1, d[1], d[0]));
      st_sc1_8(rs, WS_QABS + ((lane & 3) * 32 + h) * 1024 + (half * 256 + 64 * hw + 16 * T + 4 * g4) * 2, v2u{pk2(e[0], e[1]), pk2(e[2], e[3])});
    }
    VMWAIT(0);
    BAR();
    if (tid == 256) st_sc1_8(rs, WS_FQ + bw * 128, v2u{tA, tB});
    if (w == 4) *(LAS v2u*)(L + L_QS + lane * 8) = fetch_q(rs, q_off, 1024, tA);
    BAR();
    v4u uf[16];
#pragma unroll
    for (int ks = 0; ks < 16; ++ks) uf[ks] = ltr2(L, L_UV + tr128 + ks * 4096, L_UV + tr128 + ks * 4096 + 512);
    lds_wait(L, SY_LAST, 4);
    v2u ml;
    for (u32 spins = 0;; ++spins) {
      asm volatile("" ::: "memory");
      const v4u v = ld_sc1(rs, WS_MF + ((hw * 16 + n16) * 8 + s) * 128 + (g >> 3) * 16);
      ml = v2u{v.x, v.z};
      if (__all(v.y == tA && v.w == tB) || spins > SPIN_CAP) break;
      __builtin_amdgcn_s_sleep(1);
    }
    asm volatile("" ::: "memory");
    {
      v4u pt[16];
#pragma unroll
      for (int i = 0; i < 16; ++i) pt[i] = ld_sc1(rs, WS_PART + ((hw * 16 + i) * 32 + h) * 1024 + lane * 16);
      float M = fmaxf(asf(ml.x), dppf<XOR1>(asf(ml.x)));
      M = fmaxf(M, dppf<XOR2>(M)); M = fmaxf(M, dppf<ROR4>(M)); M = fmaxf(M, dppf<ROR8>(M));
      const float wt = ex2(asf(ml.x) - M);
      float Ls = wt * asf(ml.y);
      Ls += dppf<XOR1>(Ls); Ls += dppf<XOR2>(Ls); Ls += dppf<XOR7>(Ls); Ls += dppf<XOR15>(Ls);
      const float wn = wt / Ls;
      float c[8];
#pragma unroll
      for (int k = 0; k < 8; ++k) c[k] = 0.f;
#pragma unroll
      for (int i = 0; i < 16; ++i) {
        const float wi = asf(__builtin_amdgcn_readlane(asu(wn), i));
#pragma unroll
        for (int k = 0; k < 4; ++k) { c[2 * k] += wi * asf(pt[i][k] << 16); c[2 * k + 1] += wi * asf(pt[i][k] & 0xffff0000u); }
      }
      lst128(L, L_CL + hw * 1088 + lane * 16, v4u{pk2(c[0], c[1]), pk2(c[2], c[3]), pk2(c[4], c[5]), pk2(c[6], c[7])});
    }
    LOAD_WO(L_WO, p.Bwo + (u32)(bw * 64 * 1152), 9, 2);
    lds_inc(L, SY_A);
    lds_wait(L, SY_A, 4);
    if (bw == 0 && tid == 256) __hip_atomic_store((GAS u64*)(p.ws + WS_EPOCH), ((u64)tB << 32) | tA, __ATOMIC_RELAXED, __HIP_MEMORY_SCOPE_AGENT);
    {
      f32x4 d = {0.f, 0.f, 0.f, 0.f};
#pragma unroll
      for (int ks = 0; ks < 16; ++ks) d = mfma16(lld128(L, L_CL + (lane & 3) * 1088 + ks * 64 + g4 * 16), uf[ks], d);
      const float dv = (g4 & 2) ? ((g4 & 1) ? d[3] : d[2]) : ((g4 & 1) ? d[1] : d[0]);
      *(LAS u16*)(L + L_OA + g4 * 320 + (16 * hw + n16) * 2) = (u16)pk2(dv, dv);
    }
  }

  if (!hlp) {
    float S = a_qk(K, QF, t1);
    for (int t = 2; t < nr; ++t) {
      const u32 id = roff(t);
      asm volatile("" : "+v"(S)::"memory");
      LOAD8(K, LDK, id);
      const u32 PP = a_soft(S, acc);
      a_pv(PP, V, sel, acc);
      asm volatile("" : "+v"(acc.o)::"memory");
      LOAD8(V, LDV, id);
      if (t + 1 == nr && lane == 0) __hip_atomic_fetch_add((LAS u32*)(L + SY_LAST), 1u, __ATOMIC_RELAXED, __HIP_MEMORY_SCOPE_WORKGROUP);
      S = a_qk(K, QF, t1);
    }
    const u32 PP = a_soft(S, acc);
    float ls = acc.l + dppf<XOR1>(acc.l);
    ls += dppf<ROR4>(ls);
    ls += dppf<ROR8>(ls);
    ls = x32sum(x16sum(ls));
    u32 fa = L_FL + lane * 16, ma = L_FM + (lane & 2) * 4, sa = SY_B, one = 1;
    u32 eo = WS_E3 + (((g * 2 + pair) * 4 + prod) * 2 + ((lane >> 1) & 1)) * 512 + g4 * 128 + (((n16 >> 2) << 1) | (n16 & 1)) * 16;
    if ((lane & 61) == 0) *(LAS f32x2*)(L + ma + w * 16) = f32x2{acc.m, ls};
    asm volatile("" : "+v"(fa), "+v"(ma), "+v"(sa), "+v"(one), "+v"(eo)::"memory");
    a_pv(PP, V, sel, acc);
    *(LAS f32x4*)(L + fa + w * 1024) = acc.o;
    u32 old;
    u64 ex;
    asm volatile("s_mov_b64 %1, exec\n\ts_mov_b64 exec, 1\n\tds_add_rtn_u32 %0, %2, %3\n\ts_mov_b64 exec, %1" : "=&v"(old), "=&s"(ex) : "v"(sa), "v"(one) : "memory");
    f32x2 ml[4];
    f32x4 aw[4];
#pragma unroll
    for (int i = 0; i < 4; ++i) ml[i] = *(LAS f32x2*)(L + ma + i * 16);
#pragma unroll
    for (int i = 0; i < 4; ++i) aw[i] = *(LAS f32x4*)(L + fa + i * 1024);
    const float M = fmaxf(fmaxf(ml[0].x, ml[1].x), fmaxf(ml[2].x, ml[3].x));
    float wt[4], Ls = 0.f;
#pragma unroll
    for (int i = 0; i < 4; ++i) { wt[i] = ex2(ml[i].x - M); Ls += wt[i] * ml[i].y; }
    asm volatile("" : "+v"(old) : "v"(Ls));
    const bool last = __builtin_amdgcn_readfirstlane(old) == 3;
    const f32x4 o = (wt[0] * aw[0] + wt[1] * aw[1] + wt[2] * aw[2] + wt[3] * aw[3]) * __builtin_amdgcn_rcpf(Ls);
    const u32 lse = asu(M + __builtin_amdgcn_logf(Ls));
    const u32 p01 = pk2(o[0], o[1]), p23 = pk2(o[2], o[3]);
    st_sc1(rs, last ? eo : OOB, v4u{perm(p01, tA, 0x07060500u), perm(p23, tA, 0x07060501u), perm(lse, tA, 0x07060502u),
                                    perm(p01, tA, 0x0c0c0403u) | perm(lse, p23, 0x04000c0cu)});
    if (!isA) {
      if (!isBA) {
        lds_wait(L, SY_A, 4);
        LOAD_WO_A();
      }
      const int i16 = lane >> 2;
      u32 go = WS_E3 + ((((h >> 1) * 4 + hw) * 4 + (lane & 3)) * 2 + (h & 1)) * 512 + i16 * 16;
      u32 oa = L_OB + hw * 320 + (4 * (i16 & 8) + (i16 & 7) + 8 * (lane & 3)) * 2, ra = L_OB + (lane & 3) * 320 + g4 * 16;
      u32 e4 = g4 < nu ? WS_E4 + ((16 * (r & 3) + 8 * g4 + 2 * hw + (n16 >> 3)) * 32 + h) * 128 + (n16 & 7) * 16 : OOB;
      u32 s2 = SY_C2;
      asm volatile("" : "+v"(go), "+v"(oa), "+v"(ra), "+v"(e4), "+v"(s2));
      lds_wait(L, SY_C1, 4);
      v4u wf[12];
#pragma unroll
      for (int i = 0; i < 12; ++i) wf[i] = ltr2(L, L_UV + tr128 + i * 4096, L_UV + tr128 + i * 4096 + 512);
      v4u v[2];
      poll16<2>(rs, go, 256, v, tA);
      const float m = asf(perm(v[0].z, v[0].w, 0x07060503u));
      float Mx = fmaxf(m, dppf<XOR1>(m));
      Mx = fmaxf(Mx, dppf<XOR2>(Mx));
      float wq = ex2(m - Mx), Lq = wq + dppf<XOR1>(wq);
      Lq += dppf<XOR2>(Lq);
      wq *= __builtin_amdgcn_rcpf(Lq);
      float y[4];
#pragma unroll
      for (int a = 0; a < 4; ++a) {
        const u32 x = a < 2 ? v[a].x : v[a - 2].y;
        const float lo = wq * asf(perm(x, v[a & 1].w, a < 2 ? 0x05010c0cu : 0x05020c0cu)), hi = wq * asf(x & 0xffff0000u);
        y[a] = (t1 ? hi : lo) + dppf<XOR1>(t1 ? lo : hi);
      }
      const bool t2 = lane & 2;
      const float z0 = (t2 ? y[2] : y[0]) + dppf<XOR2>(t2 ? y[0] : y[2]), z1 = (t2 ? y[3] : y[1]) + dppf<XOR2>(t2 ? y[1] : y[3]);
      const u32 zz = pk2(z0, z1);
      *(LAS u16*)(L + oa) = (u16)zz;
      *(LAS u16*)(L + oa + 128) = (u16)(zz >> 16);
      asm volatile("s_mov_b64 %0, exec\n\ts_mov_b64 exec, 1\n\tds_add_u32 %1, %2\n\ts_mov_b64 exec, %0" : "=&s"(ex) : "v"(s2), "v"(one) : "memory");
      v4u af[4];
      for (u32 spins = 0;; ++spins) {
        const u32 cnt = *(volatile LAS u32*)(L + s2);
        asm volatile("" ::: "memory");
#pragma unroll
        for (int ks = 0; ks < 4; ++ks) af[ks] = lld128(L, ra + ks * 64);
        asm volatile("" : "+v"(af[0]), "+v"(af[1]), "+v"(af[2]), "+v"(af[3])::"memory");
        if (__builtin_amdgcn_readfirstlane(cnt) >= 4 || spins > (1u << 24)) break;
      }
      f32x4 e[3];
#pragma unroll
      for (int u = 0; u < 3; ++u) e[u] = f32x4{0.f, 0.f, 0.f, 0.f};
#pragma unroll
      for (int ks = 0; ks < 4; ++ks)
#pragma unroll
        for (int u = 0; u < 3; ++u) e[u] = mfma16(af[ks], wf[4 * u + ks], e[u]);
      st_sc1(rs, e4, tag4(sel4(g4 & 2, e[2], sel4(g4 & 1, e[1], e[0])), tA));
    }
  } else if (isBA) {
    const int oct = hw * 64 + bi;
    if (oct < 72) {
      lds_wait(L, SY_C2, 4);
      __builtin_amdgcn_s_sleep(10);
      reduce_oct<4>(rs, WS_E4 + oct * 4096 + lane * 16, tA, p.out + oct * 8, lane);
    }
  } else if (!isA) {
    VMWAIT(0);
    lds_inc(L, SY_WO);
    lds_wait(L, SY_WO, 4);
    const v4u af0 = lld128(L, L_OA + (lane & 3) * 320 + g4 * 16), af1 = lld128(L, L_OA + (lane & 3) * 320 + 64 + g4 * 16);
    f32x4 e[9];
#pragma unroll
    for (int u = 0; u < 9; ++u) {
      const u32 t0 = L_WO + tr128 + u * 8192;
      e[u] = mfma16(af1, ltr2(L, t0 + 4096, t0 + 4608), mfma16(af0, ltr2(L, t0, t0 + 512), f32x4{0.f, 0.f, 0.f, 0.f}));
    }
    const u32 eo = WS_E6 + ((8 * g4 + 2 * hw + (n16 >> 3)) * 64 + bw) * 128 + (n16 & 7) * 16;
#pragma unroll
    for (int k = 0; k < 2; ++k)
      st_sc1(rs, eo + k * 32 * 8192, tag4(sel4(g4 & 2, sel4(g4 & 1, e[4 * k + 3], e[4 * k + 2]), sel4(g4 & 1, e[4 * k + 1], e[4 * k])), tA));
    st_sc1(rs, g4 == 0 ? eo + 64 * 8192 : OOB, tag4(e[8], tA));
    const int oct = hw * 64 + bw;
    if (oct < 72) reduce_oct<8>(rs, WS_E6 + oct * 8192 + lane * 16, tA, p.out + 576 + oct * 8, lane);
  }
}

extern "C" void kernel_launch(void* const* d_in, const int* in_sizes, int n_in, void* d_out, int out_size, void* d_ws, size_t ws_size,
                              hipStream_t stream) {
  Params p;
  p.Ax = (const char*)d_in[0]; p.AK = (const char*)d_in[1]; p.AV = (const char*)d_in[2]; p.Awq = (const char*)d_in[3]; p.Awo = (const char*)d_in[4];
  p.Bx = (const char*)d_in[5]; p.BC = (const char*)d_in[6]; p.Bwq = (const char*)d_in[7]; p.Bwuk = (const char*)d_in[8]; p.Bwuv = (const char*)d_in[9];
  p.Bwo = (const char*)d_in[10];
  p.out = (u16*)d_out;
  p.ws = (char*)d_ws;
  (void)hipFuncSetAttribute((const void*)decode_mega, hipFuncAttributeMaxDynamicSharedMemorySize, LDS_BYTES);
  decode_mega<<<256, 512, LDS_BYTES, stream>>>(p);
}
```
